```python
import torch

def _softmax(x, axis=-1):
    return torch.softmax(x, dim=axis)
def _gelu(x):
    return torch.nn.functional.gelu(x, approximate="tanh")
def _silu(x):
    return torch.nn.functional.silu(x)
def _sigmoid(x):
    return torch.sigmoid(x)
def _layernorm(x, axis=-1, eps=1e-5):
    m = x.mean(dim=axis, keepdim=True)
    v = x.var(dim=axis, keepdim=True, unbiased=False)
    return (x - m) / torch.sqrt(v + eps)
def _scatter_add(tgt, idx, src, axis=0):
    return tgt.clone().index_add_(axis, idx, src)
def _segment_sum(src, seg, n):
    out = torch.zeros((n,) + src.shape[1:], dtype=src.dtype)
    return out.index_add_(0, seg, src)
def _pad(x, pw):
    flat = tuple(v for pair in reversed(pw) for v in pair)
    return torch.nn.functional.pad(x, flat)

kernel_name = 'concat_heads__gqa_block_cached__gqa_block_cached__near_ridge'

def setup_inputs():

    _w = (lambda a: (lambda t: t.to(torch.bfloat16) if t.is_floating_point() else t)(torch.as_tensor(a)))
    def _A_setup():
        B, Sq, D = 8, 144, 576
        Hq, Hkv, Sk, Dh = 32, 16, 1024, 128
        assert Hq % Hkv == 0, (Hq, Hkv)
        return {
            "x": _w((lambda *s: torch.randn(*s, dtype=torch.bfloat16) * 0.8)(B, Sq, D)),
            "k_cache": _w((lambda *s: torch.randn(*s, dtype=torch.bfloat16) * 0.8)(B, Hkv, Sk, Dh)),
            "v_cache": _w((lambda *s: torch.randn(*s, dtype=torch.bfloat16) * 0.05)(B, Hkv, Sk, Dh)),
            "wq": _w((lambda *s: torch.randn(*s, dtype=torch.bfloat16) * 0.8)(D, Hq * Dh) * (D ** -0.5)),
            "wo": _w((lambda *s: torch.randn(*s, dtype=torch.bfloat16) * 0.05)(Hq * Dh, D) * ((Hq * Dh) ** -0.5)),
        }
    _A_in = _A_setup()

    def _B_setup():
        B, Sq, D = 8, 144, 576
        Hq, Hkv, Sk, Dh = 32, 16, 1024, 128
        assert Hq % Hkv == 0, (Hq, Hkv)
        return {
            "x": _w((lambda *s: torch.randn(*s, dtype=torch.bfloat16) * 0.8)(B, Sq, D)),
            "k_cache": _w((lambda *s: torch.randn(*s, dtype=torch.bfloat16) * 0.8)(B, Hkv, Sk, Dh)),
            "v_cache": _w((lambda *s: torch.randn(*s, dtype=torch.bfloat16) * 0.05)(B, Hkv, Sk, Dh)),
            "wq": _w((lambda *s: torch.randn(*s, dtype=torch.bfloat16) * 0.8)(D, Hq * Dh) * (D ** -0.5)),
            "wo": _w((lambda *s: torch.randn(*s, dtype=torch.bfloat16) * 0.05)(Hq * Dh, D) * ((Hq * Dh) ** -0.5)),
        }
    _B_in = _B_setup()

    return {
        "A_x": _A_in["x"],
        "A_k_cache": _A_in["k_cache"],
        "A_v_cache": _A_in["v_cache"],
        "A_wq": _A_in["wq"],
        "A_wo": _A_in["wo"],
        "B_x": _B_in["x"],
        "B_k_cache": _B_in["k_cache"],
        "B_v_cache": _B_in["v_cache"],
        "B_wq": _B_in["wq"],
        "B_wo": _B_in["wo"],
    }

def reference(A_x, A_k_cache, A_v_cache, A_wq, A_wo, B_x, B_k_cache, B_v_cache, B_wq, B_wo):
    def _A_ref(x, k_cache, v_cache, wq, wo):
        B, Sq, D = x.shape
        Hq, Dh = 32, 128
        Hkv = k_cache.shape[1]
        rep = Hq // Hkv
        q = (lambda x, *d: x.permute(*d))((x @ wq).reshape(B, Sq, Hq, Dh), 0, 2, 1, 3)
        k = (lambda x, n, axis: torch.repeat_interleave(x, n, dim=axis))(k_cache, rep, axis=1)
        v = (lambda x, n, axis: torch.repeat_interleave(x, n, dim=axis))(v_cache, rep, axis=1)
        scores = torch.einsum("bhqd,bhkd->bhqk", q, k) * (Dh ** -0.5)
        p = _softmax(scores, axis=-1)
        o = torch.einsum("bhqk,bhkd->bhqd", p, v)
        return (lambda x, *d: x.permute(*d))(o, 0, 2, 1, 3).reshape(B, Sq, Hq * Dh) @ wo

    def _B_ref(x, k_cache, v_cache, wq, wo):
        B, Sq, D = x.shape
        Hq, Dh = 32, 128
        Hkv = k_cache.shape[1]
        rep = Hq // Hkv
        q = (lambda x, *d: x.permute(*d))((x @ wq).reshape(B, Sq, Hq, Dh), 0, 2, 1, 3)
        k = (lambda x, n, axis: torch.repeat_interleave(x, n, dim=axis))(k_cache, rep, axis=1)
        v = (lambda x, n, axis: torch.repeat_interleave(x, n, dim=axis))(v_cache, rep, axis=1)
        scores = torch.einsum("bhqd,bhkd->bhqk", q, k) * (Dh ** -0.5)
        p = _softmax(scores, axis=-1)
        o = torch.einsum("bhqk,bhkd->bhqd", p, v)
        return (lambda x, *d: x.permute(*d))(o, 0, 2, 1, 3).reshape(B, Sq, Hq * Dh) @ wo

    a = _A_ref(A_x, A_k_cache, A_v_cache, A_wq, A_wo)
    b = _B_ref(B_x, B_k_cache, B_v_cache, B_wq, B_wo)
    return torch.concatenate([a, b], axis=-1)
```

```cpp
#include <hip/hip_runtime.h>
#include <hip/hip_bf16.h>
typedef unsigned short u16;
__device__ __forceinline__ float bf2f(u16 x){ return __uint_as_float(((unsigned)x)<<16); }
__device__ __forceinline__ u16 f2bf(float f){ unsigned u=__float_as_uint(f); u += 0x7FFF + ((u>>16)&1); return (u16)(u>>16); }
constexpr int B=8,SQ=144,D=576,HQ=32,HKV=16,SK=1024,DH=128;
__global__ void ref_gemm(const u16* A,const u16* W,u16* C,int M,int N,int K,int ldc,int coff){
  int n=blockIdx.x*blockDim.x+threadIdx.x; int m=blockIdx.y; if(n>=N) return;
  float acc=0; for(int k=0;k<K;++k) acc += bf2f(A[(size_t)m*K+k])*bf2f(W[(size_t)k*N+n]);
  C[(size_t)m*ldc+coff+n]=f2bf(acc);
}
__global__ void ref_attn(const u16* Q,const u16* Kc,const u16* Vc,u16* O){
  int q=blockIdx.x,h=blockIdx.y,b=blockIdx.z; int kv=h/(HQ/HKV); int t=threadIdx.x;
  __shared__ float s[SK]; __shared__ float red[256]; __shared__ float qs[DH];
  if(t<DH) qs[t]=bf2f(Q[((size_t)(b*SQ+q))*(HQ*DH)+h*DH+t]);
  __syncthreads();
  const u16* Kp=Kc+((size_t)(b*HKV+kv))*SK*DH; const u16* Vp=Vc+((size_t)(b*HKV+kv))*SK*DH;
  float mx=-1e30f;
  for(int k=t;k<SK;k+=256){ float a=0; for(int d=0;d<DH;++d) a+=qs[d]*bf2f(Kp[(size_t)k*DH+d]);
    a=bf2f(f2bf(a)); a=bf2f(f2bf(a*0.08838834764831845f)); s[k]=a; mx=fmaxf(mx,a);}
  red[t]=mx; __syncthreads(); for(int o=128;o>0;o>>=1){ if(t<o) red[t]=fmaxf(red[t],red[t+o]); __syncthreads(); }
  mx=red[0]; __syncthreads(); float sum=0;
  for(int k=t;k<SK;k+=256){ float e=__expf(s[k]-mx); s[k]=e; sum+=e; }
  red[t]=sum; __syncthreads(); for(int o=128;o>0;o>>=1){ if(t<o) red[t]+=red[t+o]; __syncthreads(); }
  sum=red[0]; __syncthreads();
  for(int k=t;k<SK;k+=256) s[k]=bf2f(f2bf(s[k]/sum));
  __syncthreads();
  if(t<DH){ float a=0; for(int k=0;k<SK;++k) a+=s[k]*bf2f(Vp[(size_t)k*DH+t]); O[((size_t)(b*SQ+q))*(HQ*DH)+h*DH+t]=f2bf(a);}
}
extern "C" void kernel_launch(void* const* d_in,const int* in_sizes,int n_in,void* d_out,int out_size,void* d_ws,size_t ws_size,hipStream_t stream){
  u16* Qw=(u16*)d_ws; u16* Ow=Qw+(size_t)B*SQ*HQ*DH;
  for(int l=0;l<2;++l){
    const u16* x=(const u16*)d_in[5*l+0]; const u16* k=(const u16*)d_in[5*l+1]; const u16* v=(const u16*)d_in[5*l+2];
    const u16* wq=(const u16*)d_in[5*l+3]; const u16* wo=(const u16*)d_in[5*l+4];
    ref_gemm<<<dim3(HQ*DH/256,B*SQ),256,0,stream>>>(x,wq,Qw,B*SQ,HQ*DH,D,HQ*DH,0);
    ref_attn<<<dim3(SQ,HQ,B),256,0,stream>>>(Qw,k,v,Ow);
    ref_gemm<<<dim3((D+255)/256,B*SQ),256,0,stream>>>(Ow,wo,(u16*)d_out,B*SQ,D,HQ*DH,2*D,l*D);
  }
}
```

```cpp
#include <hip/hip_runtime.h>
#include <hip/hip_bf16.h>
#define KZ_ASM \
  "v_mov_b64 v[236:237], %1\n\t" \
  "v_mov_b64 v[238:239], %2\n\t" \
  "v_mov_b64 v[240:241], %3\n\t" \
  "v_mov_b64 v[242:243], %4\n\t" \
  "v_mov_b64 v[244:245], %5\n\t" \
  "v_mov_b64 v[246:247], %6\n\t" \
  "v_mov_b64 v[216:217], %7\n\t" \
  "v_mov_b64 v[226:227], %8\n\t" \
  "v_mov_b64 v[220:221], %9\n\t" \
  "v_mov_b64 v[228:229], %10\n\t" \
  "s_mov_b64 s[40:41], %11\n\t" \
  "s_mov_b64 s[44:45], %12\n\t" \
  "s_mov_b64 s[48:49], %13\n\t" \
  "s_mov_b64 s[52:53], %14\n\t" \
  "s_mov_b64 s[56:57], %15\n\t" \
  "s_mov_b64 s[64:65], %16\n\t" \
  "s_mov_b64 s[70:71], %17\n\t" \
  "s_mov_b64 s[82:83], %18\n\t" \
  "s_mov_b64 s[84:85], %19\n\t" \
  "s_mov_b32 s42, 165888\n\t" \
  "s_mov_b32 s43, 0x20000\n\t" \
  "s_mov_b32 s46, %20\n\t" \
  "s_mov_b32 s47, 0x20000\n\t" \
  "s_mov_b32 s50, 262144\n\t" \
  "s_mov_b32 s51, 0x20000\n\t" \
  "s_mov_b32 s54, 262144\n\t" \
  "s_mov_b32 s55, 0x20000\n\t" \
  "s_mov_b32 s58, 9437184\n\t" \
  "s_mov_b32 s59, 0x20000\n\t" \
  "s_mov_b64 s[96:97], %24\n\t" \
  "s_mov_b32 s98, 16384\n\t" \
  "s_mov_b32 s99, 0x20000\n\t" \
  "s_mov_b32 s90, s70\n\t" \
  "s_add_u32 s91, s70, 4096\n\t" \
  "s_add_u32 s92, s70, 8192\n\t" \
  "s_add_u32 s93, s70, 12288\n\t" \
  "s_lshl_b32 s94, s85, 10\n\t" \
  "s_add_u32 s94, s94, 16384\n\t" \
  "s_mov_b32 s88, 0x3e0293ee\n\t" \
  "s_mov_b32 s89, 0x49800000\n\t" \
  "v_mov_b32 v247, 0\n\t" \
  "s_nop 4\n\t" \
  "s_getpc_b64 s[60:61]\n\t" \
  "PFL_%=:\n\t" \
  "v_lshlrev_b32 v248, 3, v244\n\t" \
  "s_lshl_b32 s62, s70, 3\n\t" \
  "v_add_u32 v248, s62, v248\n\t" \
  "s_mov_b32 s63, END_%=-PFL_%=-4\n\t" \
  "s_nop 4\n\t" \
  "v_min_u32 v249, s63, v248\n\t" \
  "v_and_b32 v249, -4, v249\n\t" \
  "global_load_dword v218, v249, s[60:61]\n\t" \
  "v_add_u32 v248, 0x8000, v248\n\t" \
  "v_min_u32 v250, s63, v248\n\t" \
  "v_and_b32 v250, -4, v250\n\t" \
  "global_load_dword v219, v250, s[60:61]\n\t" \
  "buffer_load_dwordx2 v[214:215], v247, s[96:99], 0 offen sc1\n\t" \
  "s_mov_b32 s69, 65536\n\t" \
  "s_mul_i32 s63, s90, 9\n\t" \
  "s_add_u32 m0, s69, s90\n\t" \
  "s_add_u32 s63, s63, s65\n\t" \
  "buffer_load_dwordx4 v241, s[40:43], s63 offen lds\n\t" \
  "s_mul_i32 s63, s91, 9\n\t" \
  "s_add_u32 m0, s69, s91\n\t" \
  "s_add_u32 s63, s63, s65\n\t" \
  "buffer_load_dwordx4 v241, s[40:43], s63 offen lds\n\t" \
  "s_mul_i32 s63, s92, 9\n\t" \
  "s_add_u32 m0, s69, s92\n\t" \
  "s_add_u32 s63, s63, s65\n\t" \
  "buffer_load_dwordx4 v241, s[40:43], s63 offen lds\n\t" \
  "s_add_u32 m0, 0, s70\n\t" \
  "s_add_u32 s61, s64, 65536\n\t" \
  "buffer_load_dwordx4 v240, s[44:47], s64 offen lds\n\t" \
  "s_add_u32 m0, m0, 4096\n\t" \
  "s_add_u32 s62, s64, 131072\n\t" \
  "buffer_load_dwordx4 v240, s[44:47], s61 offen lds\n\t" \
  "s_add_u32 m0, m0, 4096\n\t" \
  "s_add_u32 s61, s64, 196608\n\t" \
  "buffer_load_dwordx4 v240, s[44:47], s62 offen lds\n\t" \
  "s_add_u32 m0, m0, 4096\n\t" \
  "s_add_u32 s64, s64, 262144\n\t" \
  "buffer_load_dwordx4 v240, s[44:47], s61 offen lds\n\t" \
  "s_cmp_ge_u32 s64, 4718592\n\t" \
  "s_cselect_b32 s62, 4718592, 0\n\t" \
  "s_sub_u32 s64, s64, s62\n\t" \
  "s_mul_i32 s63, s93, 9\n\t" \
  "s_add_u32 m0, s69, s93\n\t" \
  "s_add_u32 s63, s63, s65\n\t" \
  "buffer_load_dwordx4 v241, s[40:43], s63 offen lds\n\t" \
  "s_mul_i32 s63, s94, 9\n\t" \
  "s_add_u32 m0, s69, s94\n\t" \
  "s_add_u32 s63, s63, s65\n\t" \
  "buffer_load_dwordx4 v241, s[40:43], s63 offen lds\n\t" \
  "s_add_u32 s65, s65, 128\n\t" \
  "s_cmp_ge_u32 s65, 1152\n\t" \
  "s_cselect_b32 s63, 1152, 0\n\t" \
  "s_sub_u32 s65, s65, s63\n\t" \
  "s_add_u32 m0, 16384, s70\n\t" \
  "s_add_u32 s61, s64, 65536\n\t" \
  "buffer_load_dwordx4 v240, s[44:47], s64 offen lds\n\t" \
  "s_add_u32 m0, m0, 4096\n\t" \
  "s_add_u32 s62, s64, 131072\n\t" \
  "buffer_load_dwordx4 v240, s[44:47], s61 offen lds\n\t" \
  "s_add_u32 m0, m0, 4096\n\t" \
  "s_add_u32 s61, s64, 196608\n\t" \
  "buffer_load_dwordx4 v240, s[44:47], s62 offen lds\n\t" \
  "s_add_u32 m0, m0, 4096\n\t" \
  "s_add_u32 s64, s64, 262144\n\t" \
  "buffer_load_dwordx4 v240, s[44:47], s61 offen lds\n\t" \
  "s_cmp_ge_u32 s64, 4718592\n\t" \
  "s_cselect_b32 s62, 4718592, 0\n\t" \
  "s_sub_u32 s64, s64, s62\n\t" \
  "v_mov_b32 v32, 0\n\t" \
  "v_mov_b32 v33, 0\n\t" \
  "v_mov_b32 v34, 0\n\t" \
  "v_mov_b32 v35, 0\n\t" \
  "v_mov_b32 v36, 0\n\t" \
  "v_mov_b32 v37, 0\n\t" \
  "v_mov_b32 v38, 0\n\t" \
  "v_mov_b32 v39, 0\n\t" \
  "v_mov_b32 v40, 0\n\t" \
  "v_mov_b32 v41, 0\n\t" \
  "v_mov_b32 v42, 0\n\t" \
  "v_mov_b32 v43, 0\n\t" \
  "v_mov_b32 v44, 0\n\t" \
  "v_mov_b32 v45, 0\n\t" \
  "v_mov_b32 v46, 0\n\t" \
  "v_mov_b32 v47, 0\n\t" \
  "v_mov_b32 v48, 0\n\t" \
  "v_mov_b32 v49, 0\n\t" \
  "v_mov_b32 v50, 0\n\t" \
  "v_mov_b32 v51, 0\n\t" \
  "v_mov_b32 v52, 0\n\t" \
  "v_mov_b32 v53, 0\n\t" \
  "v_mov_b32 v54, 0\n\t" \
  "v_mov_b32 v55, 0\n\t" \
  "v_mov_b32 v56, 0\n\t" \
  "v_mov_b32 v57, 0\n\t" \
  "v_mov_b32 v58, 0\n\t" \
  "v_mov_b32 v59, 0\n\t" \
  "v_mov_b32 v60, 0\n\t" \
  "v_mov_b32 v61, 0\n\t" \
  "v_mov_b32 v62, 0\n\t" \
  "v_mov_b32 v63, 0\n\t" \
  "v_mov_b32 v64, 0\n\t" \
  "v_mov_b32 v65, 0\n\t" \
  "v_mov_b32 v66, 0\n\t" \
  "v_mov_b32 v67, 0\n\t" \
  "v_mov_b32 v68, 0\n\t" \
  "v_mov_b32 v69, 0\n\t" \
  "v_mov_b32 v70, 0\n\t" \
  "v_mov_b32 v71, 0\n\t" \
  "v_mov_b32 v72, 0\n\t" \
  "v_mov_b32 v73, 0\n\t" \
  "v_mov_b32 v74, 0\n\t" \
  "v_mov_b32 v75, 0\n\t" \
  "v_mov_b32 v76, 0\n\t" \
  "v_mov_b32 v77, 0\n\t" \
  "v_mov_b32 v78, 0\n\t" \
  "v_mov_b32 v79, 0\n\t" \
  "v_mov_b32 v80, 0\n\t" \
  "v_mov_b32 v81, 0\n\t" \
  "v_mov_b32 v82, 0\n\t" \
  "v_mov_b32 v83, 0\n\t" \
  "v_mov_b32 v84, 0\n\t" \
  "v_mov_b32 v85, 0\n\t" \
  "v_mov_b32 v86, 0\n\t" \
  "v_mov_b32 v87, 0\n\t" \
  "v_mov_b32 v88, 0\n\t" \
  "v_mov_b32 v89, 0\n\t" \
  "v_mov_b32 v90, 0\n\t" \
  "v_mov_b32 v91, 0\n\t" \
  "v_mov_b32 v92, 0\n\t" \
  "v_mov_b32 v93, 0\n\t" \
  "v_mov_b32 v94, 0\n\t" \
  "v_mov_b32 v95, 0\n\t" \
  "v_mov_b32 v96, 0\n\t" \
  "v_mov_b32 v97, 0\n\t" \
  "v_mov_b32 v98, 0\n\t" \
  "v_mov_b32 v99, 0\n\t" \
  "v_mov_b32 v100, 0\n\t" \
  "v_mov_b32 v101, 0\n\t" \
  "v_mov_b32 v102, 0\n\t" \
  "v_mov_b32 v103, 0\n\t" \
  "v_mov_b32 v104, 0\n\t" \
  "v_mov_b32 v105, 0\n\t" \
  "v_mov_b32 v106, 0\n\t" \
  "v_mov_b32 v107, 0\n\t" \
  "v_mov_b32 v108, 0\n\t" \
  "v_mov_b32 v109, 0\n\t" \
  "v_mov_b32 v110, 0\n\t" \
  "v_mov_b32 v111, 0\n\t" \
  "v_mov_b32 v112, 0\n\t" \
  "v_mov_b32 v113, 0\n\t" \
  "v_mov_b32 v114, 0\n\t" \
  "v_mov_b32 v115, 0\n\t" \
  "v_mov_b32 v116, 0\n\t" \
  "v_mov_b32 v117, 0\n\t" \
  "v_mov_b32 v118, 0\n\t" \
  "v_mov_b32 v119, 0\n\t" \
  "v_mov_b32 v120, 0\n\t" \
  "v_mov_b32 v121, 0\n\t" \
  "v_mov_b32 v122, 0\n\t" \
  "v_mov_b32 v123, 0\n\t" \
  "v_mov_b32 v124, 0\n\t" \
  "v_mov_b32 v125, 0\n\t" \
  "v_mov_b32 v126, 0\n\t" \
  "v_mov_b32 v127, 0\n\t" \
  "v_mov_b32 v128, 0\n\t" \
  "v_mov_b32 v129, 0\n\t" \
  "v_mov_b32 v130, 0\n\t" \
  "v_mov_b32 v131, 0\n\t" \
  "v_mov_b32 v132, 0\n\t" \
  "v_mov_b32 v133, 0\n\t" \
  "v_mov_b32 v134, 0\n\t" \
  "v_mov_b32 v135, 0\n\t" \
  "v_mov_b32 v136, 0\n\t" \
  "v_mov_b32 v137, 0\n\t" \
  "v_mov_b32 v138, 0\n\t" \
  "v_mov_b32 v139, 0\n\t" \
  "v_mov_b32 v140, 0\n\t" \
  "v_mov_b32 v141, 0\n\t" \
  "v_mov_b32 v142, 0\n\t" \
  "v_mov_b32 v143, 0\n\t" \
  "v_mov_b32 v144, 0\n\t" \
  "v_mov_b32 v145, 0\n\t" \
  "v_mov_b32 v146, 0\n\t" \
  "v_mov_b32 v147, 0\n\t" \
  "v_mov_b32 v148, 0\n\t" \
  "v_mov_b32 v149, 0\n\t" \
  "v_mov_b32 v150, 0\n\t" \
  "v_mov_b32 v151, 0\n\t" \
  "v_mov_b32 v152, 0\n\t" \
  "v_mov_b32 v153, 0\n\t" \
  "v_mov_b32 v154, 0\n\t" \
  "v_mov_b32 v155, 0\n\t" \
  "v_mov_b32 v156, 0\n\t" \
  "v_mov_b32 v157, 0\n\t" \
  "v_mov_b32 v158, 0\n\t" \
  "v_mov_b32 v159, 0\n\t" \
  "v_mov_b32 v160, 0\n\t" \
  "v_mov_b32 v161, 0\n\t" \
  "v_mov_b32 v162, 0\n\t" \
  "v_mov_b32 v163, 0\n\t" \
  "v_mov_b32 v164, 0\n\t" \
  "v_mov_b32 v165, 0\n\t" \
  "v_mov_b32 v166, 0\n\t" \
  "v_mov_b32 v167, 0\n\t" \
  "v_mov_b32 v168, 0\n\t" \
  "v_mov_b32 v169, 0\n\t" \
  "v_mov_b32 v170, 0\n\t" \
  "v_mov_b32 v171, 0\n\t" \
  "v_mov_b32 v172, 0\n\t" \
  "v_mov_b32 v173, 0\n\t" \
  "v_mov_b32 v174, 0\n\t" \
  "v_mov_b32 v175, 0\n\t" \
  "s_mov_b32 s67, 65536\n\t" \
  "s_mov_b32 s68, 83968\n\t" \
  "s_mov_b32 s66, 0\n\t" \
  "s_mov_b32 s95, 0\n\t" \
  "s_mov_b32 s72, 120832\n\t" \
  "s_mov_b32 s73, 65536\n\t" \
  "s_mov_b32 s74, 49152\n\t" \
  "s_mov_b32 s75, 0\n\t" \
  "s_mov_b32 s76, 81920\n\t" \
  "s_mov_b32 s77, 137216\n\t" \
  "s_mov_b32 s78, 32768\n\t" \
  "s_mov_b32 s79, 16384\n\t" \
  "s_mov_b32 s80, 49152\n\t" \
  "s_mov_b32 s81, 32768\n\t" \
  "v_add_u32 v232, s66, v236\n\t" \
  "v_add_u32 v233, s66, v237\n\t" \
  "v_add_u32 v234, s67, v238\n\t" \
  "v_add_u32 v235, s67, v239\n\t" \
  "s_waitcnt vmcnt(4)\n\t" \
  "s_barrier\n\t" \
  "ds_read_b64_tr_b16 v[176:177], v232 offset:0\n\t" \
  "ds_read_b64_tr_b16 v[178:179], v232 offset:256\n\t" \
  "ds_read_b64_tr_b16 v[180:181], v232 offset:8\n\t" \
  "ds_read_b64_tr_b16 v[182:183], v232 offset:264\n\t" \
  "ds_read_b64_tr_b16 v[184:185], v232 offset:512\n\t" \
  "ds_read_b64_tr_b16 v[186:187], v232 offset:768\n\t" \
  "ds_read_b64_tr_b16 v[188:189], v232 offset:520\n\t" \
  "ds_read_b64_tr_b16 v[190:191], v232 offset:776\n\t" \
  "ds_read_b128 a[180:183], v234 offset:0\n\t" \
  "ds_read_b128 a[184:187], v234 offset:2048\n\t" \
  "ds_read_b128 a[188:191], v234 offset:4096\n\t" \
  "ds_read_b128 a[192:195], v234 offset:6144\n\t" \
  "ds_read_b128 a[196:199], v235 offset:0\n\t" \
  "s_mov_b32 s69, 83968\n\t" \
  "s_mul_i32 s63, s90, 9\n\t" \
  "s_add_u32 m0, s69, s90\n\t" \
  "s_add_u32 s63, s63, s65\n\t" \
  "buffer_load_dwordx4 v241, s[40:43], s63 offen lds\n\t" \
  "s_mul_i32 s63, s91, 9\n\t" \
  "s_add_u32 m0, s69, s91\n\t" \
  "s_add_u32 s63, s63, s65\n\t" \
  "buffer_load_dwordx4 v241, s[40:43], s63 offen lds\n\t" \
  "s_mul_i32 s63, s92, 9\n\t" \
  "s_add_u32 m0, s69, s92\n\t" \
  "s_add_u32 s63, s63, s65\n\t" \
  "buffer_load_dwordx4 v241, s[40:43], s63 offen lds\n\t" \
  "s_add_u32 m0, 32768, s70\n\t" \
  "s_add_u32 s61, s64, 65536\n\t" \
  "buffer_load_dwordx4 v240, s[44:47], s64 offen lds\n\t" \
  "s_add_u32 m0, m0, 4096\n\t" \
  "s_add_u32 s62, s64, 131072\n\t" \
  "buffer_load_dwordx4 v240, s[44:47], s61 offen lds\n\t" \
  "s_add_u32 m0, m0, 4096\n\t" \
  "s_add_u32 s61, s64, 196608\n\t" \
  "buffer_load_dwordx4 v240, s[44:47], s62 offen lds\n\t" \
  "s_add_u32 m0, m0, 4096\n\t" \
  "s_add_u32 s64, s64, 262144\n\t" \
  "buffer_load_dwordx4 v240, s[44:47], s61 offen lds\n\t" \
  "s_cmp_ge_u32 s64, 4718592\n\t" \
  "s_cselect_b32 s62, 4718592, 0\n\t" \
  "s_sub_u32 s64, s64, s62\n\t" \
  "s_mul_i32 s63, s93, 9\n\t" \
  "s_add_u32 m0, s69, s93\n\t" \
  "s_add_u32 s63, s63, s65\n\t" \
  "buffer_load_dwordx4 v241, s[40:43], s63 offen lds\n\t" \
  "s_mul_i32 s63, s94, 9\n\t" \
  "s_add_u32 m0, s69, s94\n\t" \
  "s_add_u32 s63, s63, s65\n\t" \
  "buffer_load_dwordx4 v241, s[40:43], s63 offen lds\n\t" \
  "s_add_u32 s65, s65, 128\n\t" \
  "s_cmp_ge_u32 s65, 1152\n\t" \
  "s_cselect_b32 s63, 1152, 0\n\t" \
  "s_sub_u32 s65, s65, s63\n\t" \
  "s_mov_b32 s69, 102400\n\t" \
  "s_waitcnt lgkmcnt(0)\n\t" \
  "QLOOP_%=:\n\t" \
  "s_waitcnt vmcnt(6)\n\t" \
  "s_barrier\n\t" \
  "s_waitcnt lgkmcnt(8)\n\t" \
  "v_mfma_f32_16x16x32_bf16 v[32:35], v[176:179], a[180:183], v[32:35]\n\t" \
  "s_add_u32 s71, s66, 49152\n\t" \
  "s_and_b32 s71, s71, 0xffff\n\t" \
  "v_mfma_f32_16x16x32_bf16 v[64:67], v[176:179], a[184:187], v[64:67]\n\t" \
  "s_add_u32 m0, s71, s70\n\t" \
  "s_add_u32 s61, s64, 65536\n\t" \
  "v_mfma_f32_16x16x32_bf16 v[96:99], v[176:179], a[188:191], v[96:99]\n\t" \
  "buffer_load_dwordx4 v240, s[44:47], s64 offen lds\n\t" \
  "s_add_u32 m0, m0, 4096\n\t" \
  "s_waitcnt lgkmcnt(4)\n\t" \
  "v_mfma_f32_16x16x32_bf16 v[128:131], v[176:179], a[192:195], v[128:131]\n\t" \
  "s_add_u32 s62, s64, 131072\n\t" \
  "buffer_load_dwordx4 v240, s[44:47], s61 offen lds\n\t" \
  "v_mfma_f32_16x16x32_bf16 v[160:163], v[176:179], a[196:199], v[160:163]\n\t" \
  "ds_read_b64_tr_b16 v[176:177], v233 offset:1024\n\t" \
  "ds_read_b64_tr_b16 v[178:179], v233 offset:1280\n\t" \
  "v_mfma_f32_16x16x32_bf16 v[36:39], v[180:183], a[180:183], v[36:39]\n\t" \
  "s_add_u32 m0, m0, 4096\n\t" \
  "s_add_u32 s61, s64, 196608\n\t" \
  "v_mfma_f32_16x16x32_bf16 v[68:71], v[180:183], a[184:187], v[68:71]\n\t" \
  "buffer_load_dwordx4 v240, s[44:47], s62 offen lds\n\t" \
  "s_add_u32 m0, m0, 4096\n\t" \
  "v_mfma_f32_16x16x32_bf16 v[100:103], v[180:183], a[188:191], v[100:103]\n\t" \
  "s_add_u32 s64, s64, 262144\n\t" \
  "buffer_load_dwordx4 v240, s[44:47], s61 offen lds\n\t" \
  "v_mfma_f32_16x16x32_bf16 v[132:135], v[180:183], a[192:195], v[132:135]\n\t" \
  "s_cmp_ge_u32 s64, 4718592\n\t" \
  "s_cselect_b32 s62, 4718592, 0\n\t" \
  "v_mfma_f32_16x16x32_bf16 v[164:167], v[180:183], a[196:199], v[164:167]\n\t" \
  "ds_read_b64_tr_b16 v[180:181], v233 offset:1032\n\t" \
  "ds_read_b64_tr_b16 v[182:183], v233 offset:1288\n\t" \
  "s_waitcnt lgkmcnt(6)\n\t" \
  "v_mfma_f32_16x16x32_bf16 v[40:43], v[184:187], a[180:183], v[40:43]\n\t" \
  "s_sub_u32 s64, s64, s62\n\t" \
  "s_mul_i32 s63, s90, 9\n\t" \
  "v_mfma_f32_16x16x32_bf16 v[72:75], v[184:187], a[184:187], v[72:75]\n\t" \
  "s_add_u32 m0, s69, s90\n\t" \
  "s_add_u32 s63, s63, s65\n\t" \
  "v_mfma_f32_16x16x32_bf16 v[104:107], v[184:187], a[188:191], v[104:107]\n\t" \
  "buffer_load_dwordx4 v241, s[40:43], s63 offen lds\n\t" \
  "s_mul_i32 s63, s91, 9\n\t" \
  "v_mfma_f32_16x16x32_bf16 v[136:139], v[184:187], a[192:195], v[136:139]\n\t" \
  "s_add_u32 m0, s69, s91\n\t" \
  "s_add_u32 s63, s63, s65\n\t" \
  "v_mfma_f32_16x16x32_bf16 v[168:171], v[184:187], a[196:199], v[168:171]\n\t" \
  "ds_read_b64_tr_b16 v[184:185], v233 offset:1536\n\t" \
  "ds_read_b64_tr_b16 v[186:187], v233 offset:1792\n\t" \
  "s_waitcnt lgkmcnt(6)\n\t" \
  "v_mfma_f32_16x16x32_bf16 v[44:47], v[188:191], a[180:183], v[44:47]\n\t" \
  "buffer_load_dwordx4 v241, s[40:43], s63 offen lds\n\t" \
  "s_mul_i32 s63, s92, 9\n\t" \
  "v_mfma_f32_16x16x32_bf16 v[76:79], v[188:191], a[184:187], v[76:79]\n\t" \
  "s_add_u32 m0, s69, s92\n\t" \
  "s_add_u32 s63, s63, s65\n\t" \
  "v_mfma_f32_16x16x32_bf16 v[108:111], v[188:191], a[188:191], v[108:111]\n\t" \
  "buffer_load_dwordx4 v241, s[40:43], s63 offen lds\n\t" \
  "v_mfma_f32_16x16x32_bf16 v[140:143], v[188:191], a[192:195], v[140:143]\n\t" \
  "v_mfma_f32_16x16x32_bf16 v[172:175], v[188:191], a[196:199], v[172:175]\n\t" \
  "ds_read_b64_tr_b16 v[188:189], v233 offset:1544\n\t" \
  "ds_read_b64_tr_b16 v[190:191], v233 offset:1800\n\t" \
  "s_waitcnt lgkmcnt(4)\n\t" \
  "v_mfma_f32_16x16x32_bf16 v[48:51], v[176:179], a[180:183], v[48:51]\n\t" \
  "v_mfma_f32_16x16x32_bf16 v[80:83], v[176:179], a[184:187], v[80:83]\n\t" \
  "ds_read_b128 a[200:203], v234 offset:512\n\t" \
  "v_mfma_f32_16x16x32_bf16 v[112:115], v[176:179], a[188:191], v[112:115]\n\t" \
  "ds_read_b128 a[204:207], v234 offset:2560\n\t" \
  "v_mfma_f32_16x16x32_bf16 v[144:147], v[176:179], a[192:195], v[144:147]\n\t" \
  "ds_read_b128 a[208:211], v234 offset:4608\n\t" \
  "ds_read_b64_tr_b16 v[176:177], v232 offset:16384\n\t" \
  "v_mfma_f32_16x16x32_bf16 v[52:55], v[180:183], a[180:183], v[52:55]\n\t" \
  "ds_read_b64_tr_b16 v[178:179], v232 offset:16640\n\t" \
  "ds_read_b128 a[212:215], v234 offset:6656\n\t" \
  "v_mfma_f32_16x16x32_bf16 v[84:87], v[180:183], a[184:187], v[84:87]\n\t" \
  "ds_read_b128 a[216:219], v235 offset:512\n\t" \
  "v_mfma_f32_16x16x32_bf16 v[116:119], v[180:183], a[188:191], v[116:119]\n\t" \
  "v_mfma_f32_16x16x32_bf16 v[148:151], v[180:183], a[192:195], v[148:151]\n\t" \
  "ds_read_b64_tr_b16 v[180:181], v232 offset:16392\n\t" \
  "ds_read_b64_tr_b16 v[182:183], v232 offset:16648\n\t" \
  "s_waitcnt lgkmcnt(10)\n\t" \
  "v_mfma_f32_16x16x32_bf16 v[56:59], v[184:187], a[180:183], v[56:59]\n\t" \
  "v_mfma_f32_16x16x32_bf16 v[88:91], v[184:187], a[184:187], v[88:91]\n\t" \
  "v_mfma_f32_16x16x32_bf16 v[120:123], v[184:187], a[188:191], v[120:123]\n\t" \
  "v_mfma_f32_16x16x32_bf16 v[152:155], v[184:187], a[192:195], v[152:155]\n\t" \
  "ds_read_b64_tr_b16 v[184:185], v232 offset:16896\n\t" \
  "ds_read_b64_tr_b16 v[186:187], v232 offset:17152\n\t" \
  "s_waitcnt lgkmcnt(11)\n\t" \
  "v_mfma_f32_16x16x32_bf16 v[60:63], v[188:191], a[180:183], v[60:63]\n\t" \
  "v_mfma_f32_16x16x32_bf16 v[92:95], v[188:191], a[184:187], v[92:95]\n\t" \
  "v_mfma_f32_16x16x32_bf16 v[124:127], v[188:191], a[188:191], v[124:127]\n\t" \
  "v_mfma_f32_16x16x32_bf16 v[156:159], v[188:191], a[192:195], v[156:159]\n\t" \
  "ds_read_b64_tr_b16 v[188:189], v232 offset:16904\n\t" \
  "ds_read_b64_tr_b16 v[190:191], v232 offset:17160\n\t" \
  "s_mov_b32 s71, s66\n\t" \
  "s_xor_b32 s86, s66, 32768\n\t" \
  "s_waitcnt vmcnt(7)\n\t" \
  "s_barrier\n\t" \
  "s_waitcnt lgkmcnt(8)\n\t" \
  "v_mfma_f32_16x16x32_bf16 v[32:35], v[176:179], a[200:203], v[32:35]\n\t" \
  "s_add_u32 m0, s71, s70\n\t" \
  "s_add_u32 s61, s64, 65536\n\t" \
  "v_mfma_f32_16x16x32_bf16 v[64:67], v[176:179], a[204:207], v[64:67]\n\t" \
  "buffer_load_dwordx4 v240, s[44:47], s64 offen lds\n\t" \
  "s_add_u32 m0, m0, 4096\n\t" \
  "v_mfma_f32_16x16x32_bf16 v[96:99], v[176:179], a[208:211], v[96:99]\n\t" \
  "s_add_u32 s62, s64, 131072\n\t" \
  "buffer_load_dwordx4 v240, s[44:47], s61 offen lds\n\t" \
  "s_waitcnt lgkmcnt(4)\n\t" \
  "v_mfma_f32_16x16x32_bf16 v[128:131], v[176:179], a[212:215], v[128:131]\n\t" \
  "s_add_u32 m0, m0, 4096\n\t" \
  "s_add_u32 s61, s64, 196608\n\t" \
  "v_mfma_f32_16x16x32_bf16 v[160:163], v[176:179], a[216:219], v[160:163]\n\t" \
  "ds_read_b64_tr_b16 v[176:177], v233 offset:17408\n\t" \
  "ds_read_b64_tr_b16 v[178:179], v233 offset:17664\n\t" \
  "v_mfma_f32_16x16x32_bf16 v[36:39], v[180:183], a[200:203], v[36:39]\n\t" \
  "buffer_load_dwordx4 v240, s[44:47], s62 offen lds\n\t" \
  "s_add_u32 m0, m0, 4096\n\t" \
  "v_mfma_f32_16x16x32_bf16 v[68:71], v[180:183], a[204:207], v[68:71]\n\t" \
  "s_add_u32 s64, s64, 262144\n\t" \
  "buffer_load_dwordx4 v240, s[44:47], s61 offen lds\n\t" \
  "v_mfma_f32_16x16x32_bf16 v[100:103], v[180:183], a[208:211], v[100:103]\n\t" \
  "s_cmp_ge_u32 s64, 4718592\n\t" \
  "s_cselect_b32 s62, 4718592, 0\n\t" \
  "v_mfma_f32_16x16x32_bf16 v[132:135], v[180:183], a[212:215], v[132:135]\n\t" \
  "s_sub_u32 s64, s64, s62\n\t" \
  "s_mul_i32 s63, s93, 9\n\t" \
  "v_mfma_f32_16x16x32_bf16 v[164:167], v[180:183], a[216:219], v[164:167]\n\t" \
  "ds_read_b64_tr_b16 v[180:181], v233 offset:17416\n\t" \
  "ds_read_b64_tr_b16 v[182:183], v233 offset:17672\n\t" \
  "s_waitcnt lgkmcnt(6)\n\t" \
  "v_mfma_f32_16x16x32_bf16 v[40:43], v[184:187], a[200:203], v[40:43]\n\t" \
  "s_add_u32 m0, s69, s93\n\t" \
  "s_add_u32 s63, s63, s65\n\t" \
  "v_mfma_f32_16x16x32_bf16 v[72:75], v[184:187], a[204:207], v[72:75]\n\t" \
  "buffer_load_dwordx4 v241, s[40:43], s63 offen lds\n\t" \
  "s_mul_i32 s63, s94, 9\n\t" \
  "v_mfma_f32_16x16x32_bf16 v[104:107], v[184:187], a[208:211], v[104:107]\n\t" \
  "s_add_u32 m0, s69, s94\n\t" \
  "s_add_u32 s63, s63, s65\n\t" \
  "v_mfma_f32_16x16x32_bf16 v[136:139], v[184:187], a[212:215], v[136:139]\n\t" \
  "buffer_load_dwordx4 v241, s[40:43], s63 offen lds\n\t" \
  "s_add_u32 s65, s65, 128\n\t" \
  "v_mfma_f32_16x16x32_bf16 v[168:171], v[184:187], a[216:219], v[168:171]\n\t" \
  "ds_read_b64_tr_b16 v[184:185], v233 offset:17920\n\t" \
  "ds_read_b64_tr_b16 v[186:187], v233 offset:18176\n\t" \
  "s_waitcnt lgkmcnt(6)\n\t" \
  "v_mfma_f32_16x16x32_bf16 v[44:47], v[188:191], a[200:203], v[44:47]\n\t" \
  "s_cmp_ge_u32 s65, 1152\n\t" \
  "s_cselect_b32 s63, 1152, 0\n\t" \
  "v_mfma_f32_16x16x32_bf16 v[76:79], v[188:191], a[204:207], v[76:79]\n\t" \
  "s_sub_u32 s65, s65, s63\n\t" \
  "v_mfma_f32_16x16x32_bf16 v[108:111], v[188:191], a[208:211], v[108:111]\n\t" \
  "v_mfma_f32_16x16x32_bf16 v[140:143], v[188:191], a[212:215], v[140:143]\n\t" \
  "v_mfma_f32_16x16x32_bf16 v[172:175], v[188:191], a[216:219], v[172:175]\n\t" \
  "ds_read_b64_tr_b16 v[188:189], v233 offset:17928\n\t" \
  "ds_read_b64_tr_b16 v[190:191], v233 offset:18184\n\t" \
  "s_waitcnt lgkmcnt(6)\n\t" \
  "v_mfma_f32_16x16x32_bf16 v[48:51], v[176:179], a[200:203], v[48:51]\n\t" \
  "v_add_u32 v232, s86, v236\n\t" \
  "v_add_u32 v233, s86, v237\n\t" \
  "v_mfma_f32_16x16x32_bf16 v[80:83], v[176:179], a[204:207], v[80:83]\n\t" \
  "v_add_u32 v234, s68, v238\n\t" \
  "v_add_u32 v235, s68, v239\n\t" \
  "v_mfma_f32_16x16x32_bf16 v[112:115], v[176:179], a[208:211], v[112:115]\n\t" \
  "ds_read_b128 a[180:183], v234 offset:0\n\t" \
  "ds_read_b128 a[184:187], v234 offset:2048\n\t" \
  "v_mfma_f32_16x16x32_bf16 v[144:147], v[176:179], a[212:215], v[144:147]\n\t" \
  "ds_read_b128 a[188:191], v234 offset:4096\n\t" \
  "ds_read_b64_tr_b16 v[176:177], v232 offset:0\n\t" \
  "s_waitcnt lgkmcnt(8)\n\t" \
  "v_mfma_f32_16x16x32_bf16 v[52:55], v[180:183], a[200:203], v[52:55]\n\t" \
  "ds_read_b64_tr_b16 v[178:179], v232 offset:256\n\t" \
  "ds_read_b128 a[192:195], v234 offset:6144\n\t" \
  "v_mfma_f32_16x16x32_bf16 v[84:87], v[180:183], a[204:207], v[84:87]\n\t" \
  "ds_read_b128 a[196:199], v235 offset:0\n\t" \
  "v_mfma_f32_16x16x32_bf16 v[116:119], v[180:183], a[208:211], v[116:119]\n\t" \
  "v_mfma_f32_16x16x32_bf16 v[148:151], v[180:183], a[212:215], v[148:151]\n\t" \
  "ds_read_b64_tr_b16 v[180:181], v232 offset:8\n\t" \
  "ds_read_b64_tr_b16 v[182:183], v232 offset:264\n\t" \
  "s_waitcnt lgkmcnt(9)\n\t" \
  "v_mfma_f32_16x16x32_bf16 v[56:59], v[184:187], a[200:203], v[56:59]\n\t" \
  "v_mfma_f32_16x16x32_bf16 v[88:91], v[184:187], a[204:207], v[88:91]\n\t" \
  "v_mfma_f32_16x16x32_bf16 v[120:123], v[184:187], a[208:211], v[120:123]\n\t" \
  "v_mfma_f32_16x16x32_bf16 v[152:155], v[184:187], a[212:215], v[152:155]\n\t" \
  "ds_read_b64_tr_b16 v[184:185], v232 offset:512\n\t" \
  "ds_read_b64_tr_b16 v[186:187], v232 offset:768\n\t" \
  "v_mfma_f32_16x16x32_bf16 v[60:63], v[188:191], a[200:203], v[60:63]\n\t" \
  "v_mfma_f32_16x16x32_bf16 v[92:95], v[188:191], a[204:207], v[92:95]\n\t" \
  "v_mfma_f32_16x16x32_bf16 v[124:127], v[188:191], a[208:211], v[124:127]\n\t" \
  "v_mfma_f32_16x16x32_bf16 v[156:159], v[188:191], a[212:215], v[156:159]\n\t" \
  "ds_read_b64_tr_b16 v[188:189], v232 offset:520\n\t" \
  "ds_read_b64_tr_b16 v[190:191], v232 offset:776\n\t" \
  "s_mov_b32 s66, s86\n\t" \
  "s_mov_b32 s60, s67\n\t" \
  "s_mov_b32 s67, s68\n\t" \
  "s_mov_b32 s68, s69\n\t" \
  "s_mov_b32 s69, s60\n\t" \
  "s_add_u32 s95, s95, 1\n\t" \
  "s_cmp_lt_u32 s95, 7\n\t" \
  "s_cbranch_scc1 QLOOP_%=\n\t" \
  "s_waitcnt vmcnt(6)\n\t" \
  "s_barrier\n\t" \
  "s_waitcnt lgkmcnt(8)\n\t" \
  "v_mfma_f32_16x16x32_bf16 v[32:35], v[176:179], a[180:183], v[32:35]\n\t" \
  "s_add_u32 s71, s66, 49152\n\t" \
  "s_and_b32 s71, s71, 0xffff\n\t" \
  "v_mfma_f32_16x16x32_bf16 v[64:67], v[176:179], a[184:187], v[64:67]\n\t" \
  "s_add_u32 m0, s71, s70\n\t" \
  "s_add_u32 s61, s64, 65536\n\t" \
  "v_mfma_f32_16x16x32_bf16 v[96:99], v[176:179], a[188:191], v[96:99]\n\t" \
  "buffer_load_dwordx4 v240, s[44:47], s64 offen lds\n\t" \
  "s_add_u32 m0, m0, 4096\n\t" \
  "s_waitcnt lgkmcnt(4)\n\t" \
  "v_mfma_f32_16x16x32_bf16 v[128:131], v[176:179], a[192:195], v[128:131]\n\t" \
  "s_add_u32 s62, s64, 131072\n\t" \
  "buffer_load_dwordx4 v240, s[44:47], s61 offen lds\n\t" \
  "v_mfma_f32_16x16x32_bf16 v[160:163], v[176:179], a[196:199], v[160:163]\n\t" \
  "ds_read_b64_tr_b16 v[176:177], v233 offset:1024\n\t" \
  "ds_read_b64_tr_b16 v[178:179], v233 offset:1280\n\t" \
  "v_mfma_f32_16x16x32_bf16 v[36:39], v[180:183], a[180:183], v[36:39]\n\t" \
  "s_add_u32 m0, m0, 4096\n\t" \
  "s_add_u32 s61, s64, 196608\n\t" \
  "v_mfma_f32_16x16x32_bf16 v[68:71], v[180:183], a[184:187], v[68:71]\n\t" \
  "buffer_load_dwordx4 v240, s[44:47], s62 offen lds\n\t" \
  "s_add_u32 m0, m0, 4096\n\t" \
  "v_mfma_f32_16x16x32_bf16 v[100:103], v[180:183], a[188:191], v[100:103]\n\t" \
  "s_add_u32 s64, s64, 262144\n\t" \
  "buffer_load_dwordx4 v240, s[44:47], s61 offen lds\n\t" \
  "v_mfma_f32_16x16x32_bf16 v[132:135], v[180:183], a[192:195], v[132:135]\n\t" \
  "s_cmp_ge_u32 s64, 4718592\n\t" \
  "s_cselect_b32 s62, 4718592, 0\n\t" \
  "v_mfma_f32_16x16x32_bf16 v[164:167], v[180:183], a[196:199], v[164:167]\n\t" \
  "ds_read_b64_tr_b16 v[180:181], v233 offset:1032\n\t" \
  "ds_read_b64_tr_b16 v[182:183], v233 offset:1288\n\t" \
  "s_waitcnt lgkmcnt(6)\n\t" \
  "v_mfma_f32_16x16x32_bf16 v[40:43], v[184:187], a[180:183], v[40:43]\n\t" \
  "s_sub_u32 s64, s64, s62\n\t" \
  "s_add_u32 m0, s82, 120832\n\t" \
  "v_mfma_f32_16x16x32_bf16 v[72:75], v[184:187], a[184:187], v[72:75]\n\t" \
  "s_mov_b32 s61, 0\n\t" \
  "buffer_load_dwordx4 v220, s[48:51], s61 offen nt lds\n\t" \
  "v_mfma_f32_16x16x32_bf16 v[104:107], v[184:187], a[188:191], v[104:107]\n\t" \
  "s_add_u32 s63, s61, 4096\n\t" \
  "buffer_load_dwordx4 v220, s[48:51], s61 offen offset:2048 nt lds\n\t" \
  "v_mfma_f32_16x16x32_bf16 v[136:139], v[184:187], a[192:195], v[136:139]\n\t" \
  "s_add_u32 m0, m0, 4096\n\t" \
  "s_nop 0\n\t" \
  "buffer_load_dwordx4 v220, s[48:51], s63 offen nt lds\n\t" \
  "v_mfma_f32_16x16x32_bf16 v[168:171], v[184:187], a[196:199], v[168:171]\n\t" \
  "ds_read_b64_tr_b16 v[184:185], v233 offset:1536\n\t" \
  "ds_read_b64_tr_b16 v[186:187], v233 offset:1792\n\t" \
  "s_waitcnt lgkmcnt(6)\n\t" \
  "v_mfma_f32_16x16x32_bf16 v[44:47], v[188:191], a[180:183], v[44:47]\n\t" \
  "buffer_load_dwordx4 v220, s[48:51], s63 offen offset:2048 nt lds\n\t" \
  "v_accvgpr_write_b32 a0, 0\n\t" \
  "v_mfma_f32_16x16x32_bf16 v[76:79], v[188:191], a[184:187], v[76:79]\n\t" \
  "v_accvgpr_write_b32 a1, 0\n\t" \
  "v_accvgpr_write_b32 a2, 0\n\t" \
  "v_mfma_f32_16x16x32_bf16 v[108:111], v[188:191], a[188:191], v[108:111]\n\t" \
  "v_accvgpr_write_b32 a3, 0\n\t" \
  "v_accvgpr_write_b32 a4, 0\n\t" \
  "v_mfma_f32_16x16x32_bf16 v[140:143], v[188:191], a[192:195], v[140:143]\n\t" \
  "v_accvgpr_write_b32 a5, 0\n\t" \
  "v_accvgpr_write_b32 a6, 0\n\t" \
  "v_mfma_f32_16x16x32_bf16 v[172:175], v[188:191], a[196:199], v[172:175]\n\t" \
  "ds_read_b64_tr_b16 v[188:189], v233 offset:1544\n\t" \
  "ds_read_b64_tr_b16 v[190:191], v233 offset:1800\n\t" \
  "s_waitcnt lgkmcnt(6)\n\t" \
  "v_mfma_f32_16x16x32_bf16 v[48:51], v[176:179], a[180:183], v[48:51]\n\t" \
  "v_accvgpr_write_b32 a7, 0\n\t" \
  "v_accvgpr_write_b32 a8, 0\n\t" \
  "v_mfma_f32_16x16x32_bf16 v[80:83], v[176:179], a[184:187], v[80:83]\n\t" \
  "ds_read_b128 a[200:203], v234 offset:512\n\t" \
  "v_accvgpr_write_b32 a9, 0\n\t" \
  "v_mfma_f32_16x16x32_bf16 v[112:115], v[176:179], a[188:191], v[112:115]\n\t" \
  "ds_read_b128 a[204:207], v234 offset:2560\n\t" \
  "v_accvgpr_write_b32 a10, 0\n\t" \
  "v_mfma_f32_16x16x32_bf16 v[144:147], v[176:179], a[192:195], v[144:147]\n\t" \
  "ds_read_b128 a[208:211], v234 offset:4608\n\t" \
  "ds_read_b64_tr_b16 v[176:177], v232 offset:16384\n\t" \
  "s_waitcnt lgkmcnt(8)\n\t" \
  "v_mfma_f32_16x16x32_bf16 v[52:55], v[180:183], a[180:183], v[52:55]\n\t" \
  "ds_read_b64_tr_b16 v[178:179], v232 offset:16640\n\t" \
  "ds_read_b128 a[212:215], v234 offset:6656\n\t" \
  "v_mfma_f32_16x16x32_bf16 v[84:87], v[180:183], a[184:187], v[84:87]\n\t" \
  "ds_read_b128 a[216:219], v235 offset:512\n\t" \
  "v_accvgpr_write_b32 a11, 0\n\t" \
  "v_mfma_f32_16x16x32_bf16 v[116:119], v[180:183], a[188:191], v[116:119]\n\t" \
  "v_accvgpr_write_b32 a12, 0\n\t" \
  "v_accvgpr_write_b32 a13, 0\n\t" \
  "v_mfma_f32_16x16x32_bf16 v[148:151], v[180:183], a[192:195], v[148:151]\n\t" \
  "ds_read_b64_tr_b16 v[180:181], v232 offset:16392\n\t" \
  "ds_read_b64_tr_b16 v[182:183], v232 offset:16648\n\t" \
  "s_waitcnt lgkmcnt(11)\n\t" \
  "v_mfma_f32_16x16x32_bf16 v[56:59], v[184:187], a[180:183], v[56:59]\n\t" \
  "v_accvgpr_write_b32 a14, 0\n\t" \
  "v_accvgpr_write_b32 a15, 0\n\t" \
  "v_mfma_f32_16x16x32_bf16 v[88:91], v[184:187], a[184:187], v[88:91]\n\t" \
  "v_accvgpr_write_b32 a16, 0\n\t" \
  "v_accvgpr_write_b32 a17, 0\n\t" \
  "v_mfma_f32_16x16x32_bf16 v[120:123], v[184:187], a[188:191], v[120:123]\n\t" \
  "v_accvgpr_write_b32 a18, 0\n\t" \
  "v_accvgpr_write_b32 a19, 0\n\t" \
  "v_mfma_f32_16x16x32_bf16 v[152:155], v[184:187], a[192:195], v[152:155]\n\t" \
  "ds_read_b64_tr_b16 v[184:185], v232 offset:16896\n\t" \
  "ds_read_b64_tr_b16 v[186:187], v232 offset:17152\n\t" \
  "s_waitcnt lgkmcnt(11)\n\t" \
  "v_mfma_f32_16x16x32_bf16 v[60:63], v[188:191], a[180:183], v[60:63]\n\t" \
  "v_mfma_f32_16x16x32_bf16 v[92:95], v[188:191], a[184:187], v[92:95]\n\t" \
  "v_mfma_f32_16x16x32_bf16 v[124:127], v[188:191], a[188:191], v[124:127]\n\t" \
  "v_mfma_f32_16x16x32_bf16 v[156:159], v[188:191], a[192:195], v[156:159]\n\t" \
  "ds_read_b64_tr_b16 v[188:189], v232 offset:16904\n\t" \
  "ds_read_b64_tr_b16 v[190:191], v232 offset:17160\n\t" \
  "s_xor_b32 s86, s66, 32768\n\t" \
  "s_waitcnt vmcnt(8)\n\t" \
  "s_barrier\n\t" \
  "s_waitcnt lgkmcnt(8)\n\t" \
  "v_mfma_f32_16x16x32_bf16 v[32:35], v[176:179], a[200:203], v[32:35]\n\t" \
  "s_add_u32 m0, s82, 137216\n\t" \
  "s_mov_b32 s61, 0\n\t" \
  "v_mfma_f32_16x16x32_bf16 v[64:67], v[176:179], a[204:207], v[64:67]\n\t" \
  "buffer_load_dwordx4 v221, s[52:55], s61 offen nt lds\n\t" \
  "s_add_u32 s63, s61, 4096\n\t" \
  "v_mfma_f32_16x16x32_bf16 v[96:99], v[176:179], a[208:211], v[96:99]\n\t" \
  "buffer_load_dwordx4 v221, s[52:55], s61 offen offset:2048 nt lds\n\t" \
  "s_add_u32 m0, m0, 4096\n\t" \
  "s_waitcnt lgkmcnt(4)\n\t" \
  "v_mfma_f32_16x16x32_bf16 v[128:131], v[176:179], a[212:215], v[128:131]\n\t" \
  "buffer_load_dwordx4 v221, s[52:55], s63 offen nt lds\n\t" \
  "buffer_load_dwordx4 v221, s[52:55], s63 offen offset:2048 nt lds\n\t" \
  "v_mfma_f32_16x16x32_bf16 v[160:163], v[176:179], a[216:219], v[160:163]\n\t" \
  "ds_read_b64_tr_b16 v[176:177], v233 offset:17408\n\t" \
  "ds_read_b64_tr_b16 v[178:179], v233 offset:17664\n\t" \
  "v_mfma_f32_16x16x32_bf16 v[36:39], v[180:183], a[200:203], v[36:39]\n\t" \
  "v_accvgpr_write_b32 a20, 0\n\t" \
  "v_accvgpr_write_b32 a21, 0\n\t" \
  "v_mfma_f32_16x16x32_bf16 v[68:71], v[180:183], a[204:207], v[68:71]\n\t" \
  "v_accvgpr_write_b32 a22, 0\n\t" \
  "v_accvgpr_write_b32 a23, 0\n\t" \
  "v_mfma_f32_16x16x32_bf16 v[100:103], v[180:183], a[208:211], v[100:103]\n\t" \
  "v_accvgpr_write_b32 a24, 0\n\t" \
  "v_accvgpr_write_b32 a25, 0\n\t" \
  "v_mfma_f32_16x16x32_bf16 v[132:135], v[180:183], a[212:215], v[132:135]\n\t" \
  "v_accvgpr_write_b32 a26, 0\n\t" \
  "v_accvgpr_write_b32 a27, 0\n\t" \
  "v_mfma_f32_16x16x32_bf16 v[164:167], v[180:183], a[216:219], v[164:167]\n\t" \
  "ds_read_b64_tr_b16 v[180:181], v233 offset:17416\n\t" \
  "ds_read_b64_tr_b16 v[182:183], v233 offset:17672\n\t" \
  "s_waitcnt lgkmcnt(6)\n\t" \
  "v_mfma_f32_16x16x32_bf16 v[40:43], v[184:187], a[200:203], v[40:43]\n\t" \
  "v_accvgpr_write_b32 a28, 0\n\t" \
  "v_accvgpr_write_b32 a29, 0\n\t" \
  "v_mfma_f32_16x16x32_bf16 v[72:75], v[184:187], a[204:207], v[72:75]\n\t" \
  "v_accvgpr_write_b32 a30, 0\n\t" \
  "v_accvgpr_write_b32 a31, 0\n\t" \
  "v_mfma_f32_16x16x32_bf16 v[104:107], v[184:187], a[208:211], v[104:107]\n\t" \
  "v_accvgpr_write_b32 a32, 0\n\t" \
  "v_accvgpr_write_b32 a33, 0\n\t" \
  "v_mfma_f32_16x16x32_bf16 v[136:139], v[184:187], a[212:215], v[136:139]\n\t" \
  "v_accvgpr_write_b32 a34, 0\n\t" \
  "v_accvgpr_write_b32 a35, 0\n\t" \
  "v_mfma_f32_16x16x32_bf16 v[168:171], v[184:187], a[216:219], v[168:171]\n\t" \
  "ds_read_b64_tr_b16 v[184:185], v233 offset:17920\n\t" \
  "ds_read_b64_tr_b16 v[186:187], v233 offset:18176\n\t" \
  "s_waitcnt lgkmcnt(6)\n\t" \
  "v_mfma_f32_16x16x32_bf16 v[44:47], v[188:191], a[200:203], v[44:47]\n\t" \
  "v_accvgpr_write_b32 a36, 0\n\t" \
  "v_accvgpr_write_b32 a37, 0\n\t" \
  "v_mfma_f32_16x16x32_bf16 v[76:79], v[188:191], a[204:207], v[76:79]\n\t" \
  "v_accvgpr_write_b32 a38, 0\n\t" \
  "v_accvgpr_write_b32 a39, 0\n\t" \
  "v_mfma_f32_16x16x32_bf16 v[108:111], v[188:191], a[208:211], v[108:111]\n\t" \
  "v_accvgpr_write_b32 a40, 0\n\t" \
  "v_accvgpr_write_b32 a41, 0\n\t" \
  "v_mfma_f32_16x16x32_bf16 v[140:143], v[188:191], a[212:215], v[140:143]\n\t" \
  "v_accvgpr_write_b32 a42, 0\n\t" \
  "v_accvgpr_write_b32 a43, 0\n\t" \
  "v_mfma_f32_16x16x32_bf16 v[172:175], v[188:191], a[216:219], v[172:175]\n\t" \
  "ds_read_b64_tr_b16 v[188:189], v233 offset:17928\n\t" \
  "ds_read_b64_tr_b16 v[190:191], v233 offset:18184\n\t" \
  "s_waitcnt lgkmcnt(6)\n\t" \
  "v_mfma_f32_16x16x32_bf16 v[48:51], v[176:179], a[200:203], v[48:51]\n\t" \
  "v_add_u32 v232, s86, v236\n\t" \
  "v_add_u32 v233, s86, v237\n\t" \
  "v_mfma_f32_16x16x32_bf16 v[80:83], v[176:179], a[204:207], v[80:83]\n\t" \
  "v_add_u32 v234, s68, v238\n\t" \
  "v_add_u32 v235, s68, v239\n\t" \
  "v_mfma_f32_16x16x32_bf16 v[112:115], v[176:179], a[208:211], v[112:115]\n\t" \
  "ds_read_b128 a[180:183], v234 offset:0\n\t" \
  "ds_read_b128 a[184:187], v234 offset:2048\n\t" \
  "v_mfma_f32_16x16x32_bf16 v[144:147], v[176:179], a[212:215], v[144:147]\n\t" \
  "ds_read_b128 a[188:191], v234 offset:4096\n\t" \
  "ds_read_b64_tr_b16 v[176:177], v232 offset:0\n\t" \
  "s_waitcnt lgkmcnt(8)\n\t" \
  "v_mfma_f32_16x16x32_bf16 v[52:55], v[180:183], a[200:203], v[52:55]\n\t" \
  "ds_read_b64_tr_b16 v[178:179], v232 offset:256\n\t" \
  "ds_read_b128 a[192:195], v234 offset:6144\n\t" \
  "v_mfma_f32_16x16x32_bf16 v[84:87], v[180:183], a[204:207], v[84:87]\n\t" \
  "ds_read_b128 a[196:199], v235 offset:0\n\t" \
  "v_accvgpr_write_b32 a44, 0\n\t" \
  "v_mfma_f32_16x16x32_bf16 v[116:119], v[180:183], a[208:211], v[116:119]\n\t" \
  "v_accvgpr_write_b32 a45, 0\n\t" \
  "v_accvgpr_write_b32 a46, 0\n\t" \
  "v_mfma_f32_16x16x32_bf16 v[148:151], v[180:183], a[212:215], v[148:151]\n\t" \
  "ds_read_b64_tr_b16 v[180:181], v232 offset:8\n\t" \
  "ds_read_b64_tr_b16 v[182:183], v232 offset:264\n\t" \
  "s_waitcnt lgkmcnt(11)\n\t" \
  "v_mfma_f32_16x16x32_bf16 v[56:59], v[184:187], a[200:203], v[56:59]\n\t" \
  "v_accvgpr_write_b32 a47, 0\n\t" \
  "v_accvgpr_write_b32 a48, 0\n\t" \
  "v_mfma_f32_16x16x32_bf16 v[88:91], v[184:187], a[204:207], v[88:91]\n\t" \
  "v_accvgpr_write_b32 a49, 0\n\t" \
  "v_accvgpr_write_b32 a50, 0\n\t" \
  "v_mfma_f32_16x16x32_bf16 v[120:123], v[184:187], a[208:211], v[120:123]\n\t" \
  "v_accvgpr_write_b32 a51, 0\n\t" \
  "v_accvgpr_write_b32 a52, 0\n\t" \
  "v_mfma_f32_16x16x32_bf16 v[152:155], v[184:187], a[212:215], v[152:155]\n\t" \
  "ds_read_b64_tr_b16 v[184:185], v232 offset:512\n\t" \
  "ds_read_b64_tr_b16 v[186:187], v232 offset:768\n\t" \
  "s_waitcnt lgkmcnt(11)\n\t" \
  "v_mfma_f32_16x16x32_bf16 v[60:63], v[188:191], a[200:203], v[60:63]\n\t" \
  "v_accvgpr_write_b32 a53, 0\n\t" \
  "v_accvgpr_write_b32 a54, 0\n\t" \
  "v_mfma_f32_16x16x32_bf16 v[92:95], v[188:191], a[204:207], v[92:95]\n\t" \
  "v_accvgpr_write_b32 a55, 0\n\t" \
  "v_mfma_f32_16x16x32_bf16 v[124:127], v[188:191], a[208:211], v[124:127]\n\t" \
  "v_mfma_f32_16x16x32_bf16 v[156:159], v[188:191], a[212:215], v[156:159]\n\t" \
  "ds_read_b64_tr_b16 v[188:189], v232 offset:520\n\t" \
  "ds_read_b64_tr_b16 v[190:191], v232 offset:776\n\t" \
  "s_mov_b32 s66, s86\n\t" \
  "s_mov_b32 s60, s67\n\t" \
  "s_mov_b32 s67, s68\n\t" \
  "s_mov_b32 s68, s69\n\t" \
  "s_mov_b32 s69, s60\n\t" \
  "s_waitcnt vmcnt(8)\n\t" \
  "s_barrier\n\t" \
  "s_waitcnt lgkmcnt(8)\n\t" \
  "v_mfma_f32_16x16x32_bf16 v[32:35], v[176:179], a[180:183], v[32:35]\n\t" \
  "s_add_u32 m0, s82, 65536\n\t" \
  "s_mov_b32 s61, 16384\n\t" \
  "v_mfma_f32_16x16x32_bf16 v[64:67], v[176:179], a[184:187], v[64:67]\n\t" \
  "buffer_load_dwordx4 v220, s[48:51], s61 offen nt lds\n\t" \
  "s_add_u32 s63, s61, 4096\n\t" \
  "v_mfma_f32_16x16x32_bf16 v[96:99], v[176:179], a[188:191], v[96:99]\n\t" \
  "buffer_load_dwordx4 v220, s[48:51], s61 offen offset:2048 nt lds\n\t" \
  "s_add_u32 m0, m0, 4096\n\t" \
  "s_waitcnt lgkmcnt(4)\n\t" \
  "v_mfma_f32_16x16x32_bf16 v[128:131], v[176:179], a[192:195], v[128:131]\n\t" \
  "buffer_load_dwordx4 v220, s[48:51], s63 offen nt lds\n\t" \
  "buffer_load_dwordx4 v220, s[48:51], s63 offen offset:2048 nt lds\n\t" \
  "v_mfma_f32_16x16x32_bf16 v[160:163], v[176:179], a[196:199], v[160:163]\n\t" \
  "ds_read_b64_tr_b16 v[176:177], v233 offset:1024\n\t" \
  "ds_read_b64_tr_b16 v[178:179], v233 offset:1280\n\t" \
  "v_mfma_f32_16x16x32_bf16 v[36:39], v[180:183], a[180:183], v[36:39]\n\t" \
  "v_accvgpr_write_b32 a56, 0\n\t" \
  "v_accvgpr_write_b32 a57, 0\n\t" \
  "v_mfma_f32_16x16x32_bf16 v[68:71], v[180:183], a[184:187], v[68:71]\n\t" \
  "v_accvgpr_write_b32 a58, 0\n\t" \
  "v_accvgpr_write_b32 a59, 0\n\t" \
  "v_mfma_f32_16x16x32_bf16 v[100:103], v[180:183], a[188:191], v[100:103]\n\t" \
  "v_accvgpr_write_b32 a60, 0\n\t" \
  "v_accvgpr_write_b32 a61, 0\n\t" \
  "v_mfma_f32_16x16x32_bf16 v[132:135], v[180:183], a[192:195], v[132:135]\n\t" \
  "v_accvgpr_write_b32 a62, 0\n\t" \
  "v_accvgpr_write_b32 a63, 0\n\t" \
  "v_mfma_f32_16x16x32_bf16 v[164:167], v[180:183], a[196:199], v[164:167]\n\t" \
  "ds_read_b64_tr_b16 v[180:181], v233 offset:1032\n\t" \
  "ds_read_b64_tr_b16 v[182:183], v233 offset:1288\n\t" \
  "s_waitcnt lgkmcnt(6)\n\t" \
  "v_mfma_f32_16x16x32_bf16 v[40:43], v[184:187], a[180:183], v[40:43]\n\t" \
  "v_accvgpr_write_b32 a64, 0\n\t" \
  "v_accvgpr_write_b32 a65, 0\n\t" \
  "v_mfma_f32_16x16x32_bf16 v[72:75], v[184:187], a[184:187], v[72:75]\n\t" \
  "v_accvgpr_write_b32 a66, 0\n\t" \
  "v_accvgpr_write_b32 a67, 0\n\t" \
  "v_mfma_f32_16x16x32_bf16 v[104:107], v[184:187], a[188:191], v[104:107]\n\t" \
  "v_accvgpr_write_b32 a68, 0\n\t" \
  "v_accvgpr_write_b32 a69, 0\n\t" \
  "v_mfma_f32_16x16x32_bf16 v[136:139], v[184:187], a[192:195], v[136:139]\n\t" \
  "v_accvgpr_write_b32 a70, 0\n\t" \
  "v_accvgpr_write_b32 a71, 0\n\t" \
  "v_mfma_f32_16x16x32_bf16 v[168:171], v[184:187], a[196:199], v[168:171]\n\t" \
  "ds_read_b64_tr_b16 v[184:185], v233 offset:1536\n\t" \
  "ds_read_b64_tr_b16 v[186:187], v233 offset:1792\n\t" \
  "s_waitcnt lgkmcnt(6)\n\t" \
  "v_mfma_f32_16x16x32_bf16 v[44:47], v[188:191], a[180:183], v[44:47]\n\t" \
  "v_accvgpr_write_b32 a72, 0\n\t" \
  "v_accvgpr_write_b32 a73, 0\n\t" \
  "v_mfma_f32_16x16x32_bf16 v[76:79], v[188:191], a[184:187], v[76:79]\n\t" \
  "v_accvgpr_write_b32 a74, 0\n\t" \
  "v_accvgpr_write_b32 a75, 0\n\t" \
  "v_mfma_f32_16x16x32_bf16 v[108:111], v[188:191], a[188:191], v[108:111]\n\t" \
  "v_accvgpr_write_b32 a76, 0\n\t" \
  "v_accvgpr_write_b32 a77, 0\n\t" \
  "v_mfma_f32_16x16x32_bf16 v[140:143], v[188:191], a[192:195], v[140:143]\n\t" \
  "v_accvgpr_write_b32 a78, 0\n\t" \
  "v_accvgpr_write_b32 a79, 0\n\t" \
  "v_mfma_f32_16x16x32_bf16 v[172:175], v[188:191], a[196:199], v[172:175]\n\t" \
  "ds_read_b64_tr_b16 v[188:189], v233 offset:1544\n\t" \
  "ds_read_b64_tr_b16 v[190:191], v233 offset:1800\n\t" \
  "s_waitcnt lgkmcnt(6)\n\t" \
  "v_mfma_f32_16x16x32_bf16 v[48:51], v[176:179], a[180:183], v[48:51]\n\t" \
  "v_accvgpr_write_b32 a80, 0\n\t" \
  "v_accvgpr_write_b32 a81, 0\n\t" \
  "v_mfma_f32_16x16x32_bf16 v[80:83], v[176:179], a[184:187], v[80:83]\n\t" \
  "ds_read_b128 a[200:203], v234 offset:512\n\t" \
  "v_accvgpr_write_b32 a82, 0\n\t" \
  "v_mfma_f32_16x16x32_bf16 v[112:115], v[176:179], a[188:191], v[112:115]\n\t" \
  "ds_read_b128 a[204:207], v234 offset:2560\n\t" \
  "v_accvgpr_write_b32 a83, 0\n\t" \
  "v_mfma_f32_16x16x32_bf16 v[144:147], v[176:179], a[192:195], v[144:147]\n\t" \
  "ds_read_b128 a[208:211], v234 offset:4608\n\t" \
  "ds_read_b64_tr_b16 v[176:177], v232 offset:16384\n\t" \
  "s_waitcnt lgkmcnt(8)\n\t" \
  "v_mfma_f32_16x16x32_bf16 v[52:55], v[180:183], a[180:183], v[52:55]\n\t" \
  "ds_read_b64_tr_b16 v[178:179], v232 offset:16640\n\t" \
  "ds_read_b128 a[212:215], v234 offset:6656\n\t" \
  "v_mfma_f32_16x16x32_bf16 v[84:87], v[180:183], a[184:187], v[84:87]\n\t" \
  "ds_read_b128 a[216:219], v235 offset:512\n\t" \
  "v_accvgpr_write_b32 a84, 0\n\t" \
  "v_mfma_f32_16x16x32_bf16 v[116:119], v[180:183], a[188:191], v[116:119]\n\t" \
  "v_accvgpr_write_b32 a85, 0\n\t" \
  "v_accvgpr_write_b32 a86, 0\n\t" \
  "v_mfma_f32_16x16x32_bf16 v[148:151], v[180:183], a[192:195], v[148:151]\n\t" \
  "ds_read_b64_tr_b16 v[180:181], v232 offset:16392\n\t" \
  "ds_read_b64_tr_b16 v[182:183], v232 offset:16648\n\t" \
  "s_waitcnt lgkmcnt(11)\n\t" \
  "v_mfma_f32_16x16x32_bf16 v[56:59], v[184:187], a[180:183], v[56:59]\n\t" \
  "v_accvgpr_write_b32 a87, 0\n\t" \
  "v_accvgpr_write_b32 a88, 0\n\t" \
  "v_mfma_f32_16x16x32_bf16 v[88:91], v[184:187], a[184:187], v[88:91]\n\t" \
  "v_accvgpr_write_b32 a89, 0\n\t" \
  "v_accvgpr_write_b32 a90, 0\n\t" \
  "v_mfma_f32_16x16x32_bf16 v[120:123], v[184:187], a[188:191], v[120:123]\n\t" \
  "v_accvgpr_write_b32 a91, 0\n\t" \
  "v_accvgpr_write_b32 a92, 0\n\t" \
  "v_mfma_f32_16x16x32_bf16 v[152:155], v[184:187], a[192:195], v[152:155]\n\t" \
  "ds_read_b64_tr_b16 v[184:185], v232 offset:16896\n\t" \
  "ds_read_b64_tr_b16 v[186:187], v232 offset:17152\n\t" \
  "s_waitcnt lgkmcnt(11)\n\t" \
  "v_mfma_f32_16x16x32_bf16 v[60:63], v[188:191], a[180:183], v[60:63]\n\t" \
  "v_accvgpr_write_b32 a93, 0\n\t" \
  "v_accvgpr_write_b32 a94, 0\n\t" \
  "v_mfma_f32_16x16x32_bf16 v[92:95], v[188:191], a[184:187], v[92:95]\n\t" \
  "v_accvgpr_write_b32 a95, 0\n\t" \
  "v_mfma_f32_16x16x32_bf16 v[124:127], v[188:191], a[188:191], v[124:127]\n\t" \
  "v_mfma_f32_16x16x32_bf16 v[156:159], v[188:191], a[192:195], v[156:159]\n\t" \
  "ds_read_b64_tr_b16 v[188:189], v232 offset:16904\n\t" \
  "ds_read_b64_tr_b16 v[190:191], v232 offset:17160\n\t" \
  "s_waitcnt vmcnt(12)\n\t" \
  "s_barrier\n\t" \
  "s_waitcnt lgkmcnt(8)\n\t" \
  "v_mfma_f32_16x16x32_bf16 v[32:35], v[176:179], a[200:203], v[32:35]\n\t" \
  "s_add_u32 m0, s82, 32768\n\t" \
  "s_mov_b32 s61, 16384\n\t" \
  "v_mfma_f32_16x16x32_bf16 v[64:67], v[176:179], a[204:207], v[64:67]\n\t" \
  "buffer_load_dwordx4 v221, s[52:55], s61 offen nt lds\n\t" \
  "s_add_u32 s63, s61, 4096\n\t" \
  "v_mfma_f32_16x16x32_bf16 v[96:99], v[176:179], a[208:211], v[96:99]\n\t" \
  "buffer_load_dwordx4 v221, s[52:55], s61 offen offset:2048 nt lds\n\t" \
  "s_add_u32 m0, m0, 4096\n\t" \
  "s_waitcnt lgkmcnt(4)\n\t" \
  "v_mfma_f32_16x16x32_bf16 v[128:131], v[176:179], a[212:215], v[128:131]\n\t" \
  "buffer_load_dwordx4 v221, s[52:55], s63 offen nt lds\n\t" \
  "buffer_load_dwordx4 v221, s[52:55], s63 offen offset:2048 nt lds\n\t" \
  "v_mfma_f32_16x16x32_bf16 v[160:163], v[176:179], a[216:219], v[160:163]\n\t" \
  "ds_read_b64_tr_b16 v[176:177], v233 offset:17408\n\t" \
  "ds_read_b64_tr_b16 v[178:179], v233 offset:17664\n\t" \
  "v_mfma_f32_16x16x32_bf16 v[36:39], v[180:183], a[200:203], v[36:39]\n\t" \
  "s_add_u32 m0, s82, 49152\n\t" \
  "s_mov_b32 s61, 32768\n\t" \
  "v_mfma_f32_16x16x32_bf16 v[68:71], v[180:183], a[204:207], v[68:71]\n\t" \
  "buffer_load_dwordx4 v220, s[48:51], s61 offen nt lds\n\t" \
  "s_add_u32 s63, s61, 4096\n\t" \
  "v_mfma_f32_16x16x32_bf16 v[100:103], v[180:183], a[208:211], v[100:103]\n\t" \
  "buffer_load_dwordx4 v220, s[48:51], s61 offen offset:2048 nt lds\n\t" \
  "s_add_u32 m0, m0, 4096\n\t" \
  "v_mfma_f32_16x16x32_bf16 v[132:135], v[180:183], a[212:215], v[132:135]\n\t" \
  "buffer_load_dwordx4 v220, s[48:51], s63 offen nt lds\n\t" \
  "buffer_load_dwordx4 v220, s[48:51], s63 offen offset:2048 nt lds\n\t" \
  "v_mfma_f32_16x16x32_bf16 v[164:167], v[180:183], a[216:219], v[164:167]\n\t" \
  "ds_read_b64_tr_b16 v[180:181], v233 offset:17416\n\t" \
  "ds_read_b64_tr_b16 v[182:183], v233 offset:17672\n\t" \
  "s_waitcnt lgkmcnt(6)\n\t" \
  "v_mfma_f32_16x16x32_bf16 v[40:43], v[184:187], a[200:203], v[40:43]\n\t" \
  "v_accvgpr_write_b32 a96, 0\n\t" \
  "v_accvgpr_write_b32 a97, 0\n\t" \
  "v_mfma_f32_16x16x32_bf16 v[72:75], v[184:187], a[204:207], v[72:75]\n\t" \
  "v_accvgpr_write_b32 a98, 0\n\t" \
  "v_accvgpr_write_b32 a99, 0\n\t" \
  "v_mfma_f32_16x16x32_bf16 v[104:107], v[184:187], a[208:211], v[104:107]\n\t" \
  "v_accvgpr_write_b32 a100, 0\n\t" \
  "v_accvgpr_write_b32 a101, 0\n\t" \
  "v_mfma_f32_16x16x32_bf16 v[136:139], v[184:187], a[212:215], v[136:139]\n\t" \
  "v_accvgpr_write_b32 a102, 0\n\t" \
  "v_accvgpr_write_b32 a103, 0\n\t" \
  "v_mfma_f32_16x16x32_bf16 v[168:171], v[184:187], a[216:219], v[168:171]\n\t" \
  "ds_read_b64_tr_b16 v[184:185], v233 offset:17920\n\t" \
  "ds_read_b64_tr_b16 v[186:187], v233 offset:18176\n\t" \
  "s_waitcnt lgkmcnt(6)\n\t" \
  "v_mfma_f32_16x16x32_bf16 v[44:47], v[188:191], a[200:203], v[44:47]\n\t" \
  "v_accvgpr_write_b32 a104, 0\n\t" \
  "v_accvgpr_write_b32 a105, 0\n\t" \
  "v_mfma_f32_16x16x32_bf16 v[76:79], v[188:191], a[204:207], v[76:79]\n\t" \
  "v_accvgpr_write_b32 a106, 0\n\t" \
  "v_accvgpr_write_b32 a107, 0\n\t" \
  "v_mfma_f32_16x16x32_bf16 v[108:111], v[188:191], a[208:211], v[108:111]\n\t" \
  "v_accvgpr_write_b32 a108, 0\n\t" \
  "v_accvgpr_write_b32 a109, 0\n\t" \
  "v_mfma_f32_16x16x32_bf16 v[140:143], v[188:191], a[212:215], v[140:143]\n\t" \
  "v_accvgpr_write_b32 a110, 0\n\t" \
  "v_accvgpr_write_b32 a111, 0\n\t" \
  "v_mfma_f32_16x16x32_bf16 v[172:175], v[188:191], a[216:219], v[172:175]\n\t" \
  "ds_read_b64_tr_b16 v[188:189], v233 offset:17928\n\t" \
  "ds_read_b64_tr_b16 v[190:191], v233 offset:18184\n\t" \
  "s_waitcnt lgkmcnt(6)\n\t" \
  "v_mfma_f32_16x16x32_bf16 v[48:51], v[176:179], a[200:203], v[48:51]\n\t" \
  "v_accvgpr_write_b32 a112, 0\n\t" \
  "v_accvgpr_write_b32 a113, 0\n\t" \
  "v_mfma_f32_16x16x32_bf16 v[80:83], v[176:179], a[204:207], v[80:83]\n\t" \
  "v_accvgpr_write_b32 a114, 0\n\t" \
  "v_accvgpr_write_b32 a115, 0\n\t" \
  "v_mfma_f32_16x16x32_bf16 v[112:115], v[176:179], a[208:211], v[112:115]\n\t" \
  "v_accvgpr_write_b32 a116, 0\n\t" \
  "v_accvgpr_write_b32 a117, 0\n\t" \
  "v_mfma_f32_16x16x32_bf16 v[144:147], v[176:179], a[212:215], v[144:147]\n\t" \
  "v_accvgpr_write_b32 a118, 0\n\t" \
  "v_accvgpr_write_b32 a119, 0\n\t" \
  "s_waitcnt lgkmcnt(4)\n\t" \
  "v_mfma_f32_16x16x32_bf16 v[52:55], v[180:183], a[200:203], v[52:55]\n\t" \
  "v_accvgpr_write_b32 a120, 0\n\t" \
  "v_accvgpr_write_b32 a121, 0\n\t" \
  "v_mfma_f32_16x16x32_bf16 v[84:87], v[180:183], a[204:207], v[84:87]\n\t" \
  "v_accvgpr_write_b32 a122, 0\n\t" \
  "v_accvgpr_write_b32 a123, 0\n\t" \
  "v_mfma_f32_16x16x32_bf16 v[116:119], v[180:183], a[208:211], v[116:119]\n\t" \
  "v_accvgpr_write_b32 a124, 0\n\t" \
  "v_accvgpr_write_b32 a125, 0\n\t" \
  "v_mfma_f32_16x16x32_bf16 v[148:151], v[180:183], a[212:215], v[148:151]\n\t" \
  "v_accvgpr_write_b32 a126, 0\n\t" \
  "v_accvgpr_write_b32 a127, 0\n\t" \
  "s_waitcnt lgkmcnt(2)\n\t" \
  "v_mfma_f32_16x16x32_bf16 v[56:59], v[184:187], a[200:203], v[56:59]\n\t" \
  "v_accvgpr_write_b32 a128, 0\n\t" \
  "v_accvgpr_write_b32 a129, 0\n\t" \
  "v_mfma_f32_16x16x32_bf16 v[88:91], v[184:187], a[204:207], v[88:91]\n\t" \
  "v_accvgpr_write_b32 a130, 0\n\t" \
  "v_accvgpr_write_b32 a131, 0\n\t" \
  "v_mfma_f32_16x16x32_bf16 v[120:123], v[184:187], a[208:211], v[120:123]\n\t" \
  "v_accvgpr_write_b32 a132, 0\n\t" \
  "v_accvgpr_write_b32 a133, 0\n\t" \
  "v_mfma_f32_16x16x32_bf16 v[152:155], v[184:187], a[212:215], v[152:155]\n\t" \
  "v_accvgpr_write_b32 a134, 0\n\t" \
  "v_accvgpr_write_b32 a135, 0\n\t" \
  "s_waitcnt lgkmcnt(0)\n\t" \
  "v_mfma_f32_16x16x32_bf16 v[60:63], v[188:191], a[200:203], v[60:63]\n\t" \
  "v_accvgpr_write_b32 a136, 0\n\t" \
  "v_accvgpr_write_b32 a137, 0\n\t" \
  "v_mfma_f32_16x16x32_bf16 v[92:95], v[188:191], a[204:207], v[92:95]\n\t" \
  "v_accvgpr_write_b32 a138, 0\n\t" \
  "v_accvgpr_write_b32 a139, 0\n\t" \
  "v_mfma_f32_16x16x32_bf16 v[124:127], v[188:191], a[208:211], v[124:127]\n\t" \
  "v_mfma_f32_16x16x32_bf16 v[156:159], v[188:191], a[212:215], v[156:159]\n\t" \
  "v_accvgpr_write_b32 a140, 0\n\t" \
  "v_accvgpr_write_b32 a141, 0\n\t" \
  "v_accvgpr_write_b32 a142, 0\n\t" \
  "v_accvgpr_write_b32 a143, 0\n\t" \
  "v_accvgpr_write_b32 a144, 0\n\t" \
  "v_accvgpr_write_b32 a145, 0\n\t" \
  "v_accvgpr_write_b32 a146, 0\n\t" \
  "v_accvgpr_write_b32 a147, 0\n\t" \
  "v_accvgpr_write_b32 a148, 0\n\t" \
  "v_accvgpr_write_b32 a149, 0\n\t" \
  "v_accvgpr_write_b32 a150, 0\n\t" \
  "v_accvgpr_write_b32 a151, 0\n\t" \
  "v_accvgpr_write_b32 a152, 0\n\t" \
  "v_accvgpr_write_b32 a153, 0\n\t" \
  "v_accvgpr_write_b32 a154, 0\n\t" \
  "v_accvgpr_write_b32 a155, 0\n\t" \
  "v_accvgpr_write_b32 a156, 0\n\t" \
  "v_accvgpr_write_b32 a157, 0\n\t" \
  "v_accvgpr_write_b32 a158, 0\n\t" \
  "v_accvgpr_write_b32 a159, 0\n\t" \
  "v_accvgpr_write_b32 a160, 0\n\t" \
  "v_accvgpr_write_b32 a161, 0\n\t" \
  "v_accvgpr_write_b32 a162, 0\n\t" \
  "v_accvgpr_write_b32 a163, 0\n\t" \
  "v_accvgpr_write_b32 a164, 0\n\t" \
  "v_accvgpr_write_b32 a165, 0\n\t" \
  "v_accvgpr_write_b32 a166, 0\n\t" \
  "v_accvgpr_write_b32 a167, 0\n\t" \
  "v_accvgpr_write_b32 a168, 0\n\t" \
  "v_accvgpr_write_b32 a169, 0\n\t" \
  "v_accvgpr_write_b32 a170, 0\n\t" \
  "v_accvgpr_write_b32 a171, 0\n\t" \
  "v_accvgpr_write_b32 a172, 0\n\t" \
  "v_accvgpr_write_b32 a173, 0\n\t" \
  "v_accvgpr_write_b32 a174, 0\n\t" \
  "v_accvgpr_write_b32 a175, 0\n\t" \
  "v_accvgpr_write_b32 a176, 0\n\t" \
  "v_accvgpr_write_b32 a177, 0\n\t" \
  "v_accvgpr_write_b32 a178, 0\n\t" \
  "v_accvgpr_write_b32 a179, 0\n\t" \
  "v_pk_mul_f32 v[160:161], v[160:161], s[88:89] op_sel_hi:[1,0]\n\t" \
  "v_pk_mul_f32 v[162:163], v[162:163], s[88:89] op_sel_hi:[1,0]\n\t" \
  "v_pk_mul_f32 v[164:165], v[164:165], s[88:89] op_sel_hi:[1,0]\n\t" \
  "v_pk_mul_f32 v[166:167], v[166:167], s[88:89] op_sel_hi:[1,0]\n\t" \
  "v_cvt_pk_bf16_f32 v160, v160, v161\n\t" \
  "v_cvt_pk_bf16_f32 v161, v162, v163\n\t" \
  "v_cvt_pk_bf16_f32 v162, v164, v165\n\t" \
  "v_cvt_pk_bf16_f32 v163, v166, v167\n\t" \
  "v_pk_mul_f32 v[168:169], v[168:169], s[88:89] op_sel_hi:[1,0]\n\t" \
  "v_pk_mul_f32 v[170:171], v[170:171], s[88:89] op_sel_hi:[1,0]\n\t" \
  "v_pk_mul_f32 v[172:173], v[172:173], s[88:89] op_sel_hi:[1,0]\n\t" \
  "v_pk_mul_f32 v[174:175], v[174:175], s[88:89] op_sel_hi:[1,0]\n\t" \
  "v_cvt_pk_bf16_f32 v168, v168, v169\n\t" \
  "v_cvt_pk_bf16_f32 v169, v170, v171\n\t" \
  "v_cvt_pk_bf16_f32 v170, v172, v173\n\t" \
  "v_cvt_pk_bf16_f32 v171, v174, v175\n\t" \
  "ds_write_b128 v242, v[160:163] offset:0\n\t" \
  "ds_write_b128 v242, v[168:171] offset:1024\n\t" \
  "v_pk_mul_f32 v[32:33], v[32:33], s[88:89] op_sel_hi:[1,0]\n\t" \
  "v_pk_mul_f32 v[34:35], v[34:35], s[88:89] op_sel_hi:[1,0]\n\t" \
  "v_pk_mul_f32 v[36:37], v[36:37], s[88:89] op_sel_hi:[1,0]\n\t" \
  "v_pk_mul_f32 v[38:39], v[38:39], s[88:89] op_sel_hi:[1,0]\n\t" \
  "v_cvt_pk_bf16_f32 v32, v32, v33\n\t" \
  "v_cvt_pk_bf16_f32 v33, v34, v35\n\t" \
  "v_cvt_pk_bf16_f32 v34, v36, v37\n\t" \
  "v_cvt_pk_bf16_f32 v35, v38, v39\n\t" \
  "v_pk_mul_f32 v[40:41], v[40:41], s[88:89] op_sel_hi:[1,0]\n\t" \
  "v_pk_mul_f32 v[42:43], v[42:43], s[88:89] op_sel_hi:[1,0]\n\t" \
  "v_pk_mul_f32 v[44:45], v[44:45], s[88:89] op_sel_hi:[1,0]\n\t" \
  "v_pk_mul_f32 v[46:47], v[46:47], s[88:89] op_sel_hi:[1,0]\n\t" \
  "v_cvt_pk_bf16_f32 v40, v40, v41\n\t" \
  "v_cvt_pk_bf16_f32 v41, v42, v43\n\t" \
  "v_cvt_pk_bf16_f32 v42, v44, v45\n\t" \
  "v_cvt_pk_bf16_f32 v43, v46, v47\n\t" \
  "v_pk_mul_f32 v[48:49], v[48:49], s[88:89] op_sel_hi:[1,0]\n\t" \
  "v_pk_mul_f32 v[50:51], v[50:51], s[88:89] op_sel_hi:[1,0]\n\t" \
  "v_pk_mul_f32 v[52:53], v[52:53], s[88:89] op_sel_hi:[1,0]\n\t" \
  "v_pk_mul_f32 v[54:55], v[54:55], s[88:89] op_sel_hi:[1,0]\n\t" \
  "v_cvt_pk_bf16_f32 v48, v48, v49\n\t" \
  "v_cvt_pk_bf16_f32 v49, v50, v51\n\t" \
  "v_cvt_pk_bf16_f32 v50, v52, v53\n\t" \
  "v_cvt_pk_bf16_f32 v51, v54, v55\n\t" \
  "v_pk_mul_f32 v[56:57], v[56:57], s[88:89] op_sel_hi:[1,0]\n\t" \
  "v_pk_mul_f32 v[58:59], v[58:59], s[88:89] op_sel_hi:[1,0]\n\t" \
  "v_pk_mul_f32 v[60:61], v[60:61], s[88:89] op_sel_hi:[1,0]\n\t" \
  "v_pk_mul_f32 v[62:63], v[62:63], s[88:89] op_sel_hi:[1,0]\n\t" \
  "v_cvt_pk_bf16_f32 v56, v56, v57\n\t" \
  "v_cvt_pk_bf16_f32 v57, v58, v59\n\t" \
  "v_cvt_pk_bf16_f32 v58, v60, v61\n\t" \
  "v_cvt_pk_bf16_f32 v59, v62, v63\n\t" \
  "v_pk_mul_f32 v[64:65], v[64:65], s[88:89] op_sel_hi:[1,0]\n\t" \
  "v_pk_mul_f32 v[66:67], v[66:67], s[88:89] op_sel_hi:[1,0]\n\t" \
  "v_pk_mul_f32 v[68:69], v[68:69], s[88:89] op_sel_hi:[1,0]\n\t" \
  "v_pk_mul_f32 v[70:71], v[70:71], s[88:89] op_sel_hi:[1,0]\n\t" \
  "v_cvt_pk_bf16_f32 v64, v64, v65\n\t" \
  "v_cvt_pk_bf16_f32 v65, v66, v67\n\t" \
  "v_cvt_pk_bf16_f32 v66, v68, v69\n\t" \
  "v_cvt_pk_bf16_f32 v67, v70, v71\n\t" \
  "v_pk_mul_f32 v[72:73], v[72:73], s[88:89] op_sel_hi:[1,0]\n\t" \
  "v_pk_mul_f32 v[74:75], v[74:75], s[88:89] op_sel_hi:[1,0]\n\t" \
  "v_pk_mul_f32 v[76:77], v[76:77], s[88:89] op_sel_hi:[1,0]\n\t" \
  "v_pk_mul_f32 v[78:79], v[78:79], s[88:89] op_sel_hi:[1,0]\n\t" \
  "v_cvt_pk_bf16_f32 v72, v72, v73\n\t" \
  "v_cvt_pk_bf16_f32 v73, v74, v75\n\t" \
  "v_cvt_pk_bf16_f32 v74, v76, v77\n\t" \
  "v_cvt_pk_bf16_f32 v75, v78, v79\n\t" \
  "v_pk_mul_f32 v[80:81], v[80:81], s[88:89] op_sel_hi:[1,0]\n\t" \
  "v_pk_mul_f32 v[82:83], v[82:83], s[88:89] op_sel_hi:[1,0]\n\t" \
  "v_pk_mul_f32 v[84:85], v[84:85], s[88:89] op_sel_hi:[1,0]\n\t" \
  "v_pk_mul_f32 v[86:87], v[86:87], s[88:89] op_sel_hi:[1,0]\n\t" \
  "v_cvt_pk_bf16_f32 v80, v80, v81\n\t" \
  "v_cvt_pk_bf16_f32 v81, v82, v83\n\t" \
  "v_cvt_pk_bf16_f32 v82, v84, v85\n\t" \
  "v_cvt_pk_bf16_f32 v83, v86, v87\n\t" \
  "v_pk_mul_f32 v[88:89], v[88:89], s[88:89] op_sel_hi:[1,0]\n\t" \
  "v_pk_mul_f32 v[90:91], v[90:91], s[88:89] op_sel_hi:[1,0]\n\t" \
  "v_pk_mul_f32 v[92:93], v[92:93], s[88:89] op_sel_hi:[1,0]\n\t" \
  "v_pk_mul_f32 v[94:95], v[94:95], s[88:89] op_sel_hi:[1,0]\n\t" \
  "v_cvt_pk_bf16_f32 v88, v88, v89\n\t" \
  "v_cvt_pk_bf16_f32 v89, v90, v91\n\t" \
  "v_cvt_pk_bf16_f32 v90, v92, v93\n\t" \
  "v_cvt_pk_bf16_f32 v91, v94, v95\n\t" \
  "v_pk_mul_f32 v[96:97], v[96:97], s[88:89] op_sel_hi:[1,0]\n\t" \
  "v_pk_mul_f32 v[98:99], v[98:99], s[88:89] op_sel_hi:[1,0]\n\t" \
  "v_pk_mul_f32 v[100:101], v[100:101], s[88:89] op_sel_hi:[1,0]\n\t" \
  "v_pk_mul_f32 v[102:103], v[102:103], s[88:89] op_sel_hi:[1,0]\n\t" \
  "v_cvt_pk_bf16_f32 v96, v96, v97\n\t" \
  "v_cvt_pk_bf16_f32 v97, v98, v99\n\t" \
  "v_cvt_pk_bf16_f32 v98, v100, v101\n\t" \
  "v_cvt_pk_bf16_f32 v99, v102, v103\n\t" \
  "v_pk_mul_f32 v[104:105], v[104:105], s[88:89] op_sel_hi:[1,0]\n\t" \
  "v_pk_mul_f32 v[106:107], v[106:107], s[88:89] op_sel_hi:[1,0]\n\t" \
  "v_pk_mul_f32 v[108:109], v[108:109], s[88:89] op_sel_hi:[1,0]\n\t" \
  "v_pk_mul_f32 v[110:111], v[110:111], s[88:89] op_sel_hi:[1,0]\n\t" \
  "v_cvt_pk_bf16_f32 v104, v104, v105\n\t" \
  "v_cvt_pk_bf16_f32 v105, v106, v107\n\t" \
  "v_cvt_pk_bf16_f32 v106, v108, v109\n\t" \
  "v_cvt_pk_bf16_f32 v107, v110, v111\n\t" \
  "v_pk_mul_f32 v[112:113], v[112:113], s[88:89] op_sel_hi:[1,0]\n\t" \
  "v_pk_mul_f32 v[114:115], v[114:115], s[88:89] op_sel_hi:[1,0]\n\t" \
  "v_pk_mul_f32 v[116:117], v[116:117], s[88:89] op_sel_hi:[1,0]\n\t" \
  "v_pk_mul_f32 v[118:119], v[118:119], s[88:89] op_sel_hi:[1,0]\n\t" \
  "v_cvt_pk_bf16_f32 v112, v112, v113\n\t" \
  "v_cvt_pk_bf16_f32 v113, v114, v115\n\t" \
  "v_cvt_pk_bf16_f32 v114, v116, v117\n\t" \
  "v_cvt_pk_bf16_f32 v115, v118, v119\n\t" \
  "v_pk_mul_f32 v[120:121], v[120:121], s[88:89] op_sel_hi:[1,0]\n\t" \
  "v_pk_mul_f32 v[122:123], v[122:123], s[88:89] op_sel_hi:[1,0]\n\t" \
  "v_pk_mul_f32 v[124:125], v[124:125], s[88:89] op_sel_hi:[1,0]\n\t" \
  "v_pk_mul_f32 v[126:127], v[126:127], s[88:89] op_sel_hi:[1,0]\n\t" \
  "v_cvt_pk_bf16_f32 v120, v120, v121\n\t" \
  "v_cvt_pk_bf16_f32 v121, v122, v123\n\t" \
  "v_cvt_pk_bf16_f32 v122, v124, v125\n\t" \
  "v_cvt_pk_bf16_f32 v123, v126, v127\n\t" \
  "v_pk_mul_f32 v[128:129], v[128:129], s[88:89] op_sel_hi:[1,0]\n\t" \
  "v_pk_mul_f32 v[130:131], v[130:131], s[88:89] op_sel_hi:[1,0]\n\t" \
  "v_pk_mul_f32 v[132:133], v[132:133], s[88:89] op_sel_hi:[1,0]\n\t" \
  "v_pk_mul_f32 v[134:135], v[134:135], s[88:89] op_sel_hi:[1,0]\n\t" \
  "v_cvt_pk_bf16_f32 v128, v128, v129\n\t" \
  "v_cvt_pk_bf16_f32 v129, v130, v131\n\t" \
  "v_cvt_pk_bf16_f32 v130, v132, v133\n\t" \
  "v_cvt_pk_bf16_f32 v131, v134, v135\n\t" \
  "v_pk_mul_f32 v[136:137], v[136:137], s[88:89] op_sel_hi:[1,0]\n\t" \
  "v_pk_mul_f32 v[138:139], v[138:139], s[88:89] op_sel_hi:[1,0]\n\t" \
  "v_pk_mul_f32 v[140:141], v[140:141], s[88:89] op_sel_hi:[1,0]\n\t" \
  "v_pk_mul_f32 v[142:143], v[142:143], s[88:89] op_sel_hi:[1,0]\n\t" \
  "v_cvt_pk_bf16_f32 v136, v136, v137\n\t" \
  "v_cvt_pk_bf16_f32 v137, v138, v139\n\t" \
  "v_cvt_pk_bf16_f32 v138, v140, v141\n\t" \
  "v_cvt_pk_bf16_f32 v139, v142, v143\n\t" \
  "v_pk_mul_f32 v[144:145], v[144:145], s[88:89] op_sel_hi:[1,0]\n\t" \
  "v_pk_mul_f32 v[146:147], v[146:147], s[88:89] op_sel_hi:[1,0]\n\t" \
  "v_pk_mul_f32 v[148:149], v[148:149], s[88:89] op_sel_hi:[1,0]\n\t" \
  "v_pk_mul_f32 v[150:151], v[150:151], s[88:89] op_sel_hi:[1,0]\n\t" \
  "v_cvt_pk_bf16_f32 v144, v144, v145\n\t" \
  "v_cvt_pk_bf16_f32 v145, v146, v147\n\t" \
  "v_cvt_pk_bf16_f32 v146, v148, v149\n\t" \
  "v_cvt_pk_bf16_f32 v147, v150, v151\n\t" \
  "v_pk_mul_f32 v[152:153], v[152:153], s[88:89] op_sel_hi:[1,0]\n\t" \
  "v_pk_mul_f32 v[154:155], v[154:155], s[88:89] op_sel_hi:[1,0]\n\t" \
  "v_pk_mul_f32 v[156:157], v[156:157], s[88:89] op_sel_hi:[1,0]\n\t" \
  "v_pk_mul_f32 v[158:159], v[158:159], s[88:89] op_sel_hi:[1,0]\n\t" \
  "v_cvt_pk_bf16_f32 v152, v152, v153\n\t" \
  "v_cvt_pk_bf16_f32 v153, v154, v155\n\t" \
  "v_cvt_pk_bf16_f32 v154, v156, v157\n\t" \
  "v_cvt_pk_bf16_f32 v155, v158, v159\n\t" \
  "s_cmp_eq_u32 s85, 0\n\t" \
  "s_cbranch_scc1 QE_%=\n\t" \
  "s_nop 1\n\t" \
  "v_accvgpr_write_b32 a188, v32\n\t" \
  "v_accvgpr_write_b32 a189, v33\n\t" \
  "v_accvgpr_write_b32 a190, v34\n\t" \
  "v_accvgpr_write_b32 a191, v35\n\t" \
  "v_accvgpr_write_b32 a192, v40\n\t" \
  "v_accvgpr_write_b32 a193, v41\n\t" \
  "v_accvgpr_write_b32 a194, v42\n\t" \
  "v_accvgpr_write_b32 a195, v43\n\t" \
  "v_accvgpr_write_b32 a180, v48\n\t" \
  "v_accvgpr_write_b32 a181, v49\n\t" \
  "v_accvgpr_write_b32 a182, v50\n\t" \
  "v_accvgpr_write_b32 a183, v51\n\t" \
  "v_accvgpr_write_b32 a184, v56\n\t" \
  "v_accvgpr_write_b32 a185, v57\n\t" \
  "v_accvgpr_write_b32 a186, v58\n\t" \
  "v_accvgpr_write_b32 a187, v59\n\t" \
  "v_accvgpr_write_b32 a204, v64\n\t" \
  "v_accvgpr_write_b32 a205, v65\n\t" \
  "v_accvgpr_write_b32 a206, v66\n\t" \
  "v_accvgpr_write_b32 a207, v67\n\t" \
  "v_accvgpr_write_b32 a208, v72\n\t" \
  "v_accvgpr_write_b32 a209, v73\n\t" \
  "v_accvgpr_write_b32 a210, v74\n\t" \
  "v_accvgpr_write_b32 a211, v75\n\t" \
  "v_accvgpr_write_b32 a196, v80\n\t" \
  "v_accvgpr_write_b32 a197, v81\n\t" \
  "v_accvgpr_write_b32 a198, v82\n\t" \
  "v_accvgpr_write_b32 a199, v83\n\t" \
  "v_accvgpr_write_b32 a200, v88\n\t" \
  "v_accvgpr_write_b32 a201, v89\n\t" \
  "v_accvgpr_write_b32 a202, v90\n\t" \
  "v_accvgpr_write_b32 a203, v91\n\t" \
  "v_accvgpr_write_b32 a220, v96\n\t" \
  "v_accvgpr_write_b32 a221, v97\n\t" \
  "v_accvgpr_write_b32 a222, v98\n\t" \
  "v_accvgpr_write_b32 a223, v99\n\t" \
  "v_accvgpr_write_b32 a224, v104\n\t" \
  "v_accvgpr_write_b32 a225, v105\n\t" \
  "v_accvgpr_write_b32 a226, v106\n\t" \
  "v_accvgpr_write_b32 a227, v107\n\t" \
  "v_accvgpr_write_b32 a212, v112\n\t" \
  "v_accvgpr_write_b32 a213, v113\n\t" \
  "v_accvgpr_write_b32 a214, v114\n\t" \
  "v_accvgpr_write_b32 a215, v115\n\t" \
  "v_accvgpr_write_b32 a216, v120\n\t" \
  "v_accvgpr_write_b32 a217, v121\n\t" \
  "v_accvgpr_write_b32 a218, v122\n\t" \
  "v_accvgpr_write_b32 a219, v123\n\t" \
  "v_accvgpr_write_b32 a236, v128\n\t" \
  "v_accvgpr_write_b32 a237, v129\n\t" \
  "v_accvgpr_write_b32 a238, v130\n\t" \
  "v_accvgpr_write_b32 a239, v131\n\t" \
  "v_accvgpr_write_b32 a240, v136\n\t" \
  "v_accvgpr_write_b32 a241, v137\n\t" \
  "v_accvgpr_write_b32 a242, v138\n\t" \
  "v_accvgpr_write_b32 a243, v139\n\t" \
  "v_accvgpr_write_b32 a228, v144\n\t" \
  "v_accvgpr_write_b32 a229, v145\n\t" \
  "v_accvgpr_write_b32 a230, v146\n\t" \
  "v_accvgpr_write_b32 a231, v147\n\t" \
  "v_accvgpr_write_b32 a232, v152\n\t" \
  "v_accvgpr_write_b32 a233, v153\n\t" \
  "v_accvgpr_write_b32 a234, v154\n\t" \
  "v_accvgpr_write_b32 a235, v155\n\t" \
  "s_branch QD_%=\n\t" \
  "QE_%=:\n\t" \
  "s_nop 1\n\t" \
  "v_accvgpr_write_b32 a180, v32\n\t" \
  "v_accvgpr_write_b32 a181, v33\n\t" \
  "v_accvgpr_write_b32 a182, v34\n\t" \
  "v_accvgpr_write_b32 a183, v35\n\t" \
  "v_accvgpr_write_b32 a184, v40\n\t" \
  "v_accvgpr_write_b32 a185, v41\n\t" \
  "v_accvgpr_write_b32 a186, v42\n\t" \
  "v_accvgpr_write_b32 a187, v43\n\t" \
  "v_accvgpr_write_b32 a188, v48\n\t" \
  "v_accvgpr_write_b32 a189, v49\n\t" \
  "v_accvgpr_write_b32 a190, v50\n\t" \
  "v_accvgpr_write_b32 a191, v51\n\t" \
  "v_accvgpr_write_b32 a192, v56\n\t" \
  "v_accvgpr_write_b32 a193, v57\n\t" \
  "v_accvgpr_write_b32 a194, v58\n\t" \
  "v_accvgpr_write_b32 a195, v59\n\t" \
  "v_accvgpr_write_b32 a196, v64\n\t" \
  "v_accvgpr_write_b32 a197, v65\n\t" \
  "v_accvgpr_write_b32 a198, v66\n\t" \
  "v_accvgpr_write_b32 a199, v67\n\t" \
  "v_accvgpr_write_b32 a200, v72\n\t" \
  "v_accvgpr_write_b32 a201, v73\n\t" \
  "v_accvgpr_write_b32 a202, v74\n\t" \
  "v_accvgpr_write_b32 a203, v75\n\t" \
  "v_accvgpr_write_b32 a204, v80\n\t" \
  "v_accvgpr_write_b32 a205, v81\n\t" \
  "v_accvgpr_write_b32 a206, v82\n\t" \
  "v_accvgpr_write_b32 a207, v83\n\t" \
  "v_accvgpr_write_b32 a208, v88\n\t" \
  "v_accvgpr_write_b32 a209, v89\n\t" \
  "v_accvgpr_write_b32 a210, v90\n\t" \
  "v_accvgpr_write_b32 a211, v91\n\t" \
  "v_accvgpr_write_b32 a212, v96\n\t" \
  "v_accvgpr_write_b32 a213, v97\n\t" \
  "v_accvgpr_write_b32 a214, v98\n\t" \
  "v_accvgpr_write_b32 a215, v99\n\t" \
  "v_accvgpr_write_b32 a216, v104\n\t" \
  "v_accvgpr_write_b32 a217, v105\n\t" \
  "v_accvgpr_write_b32 a218, v106\n\t" \
  "v_accvgpr_write_b32 a219, v107\n\t" \
  "v_accvgpr_write_b32 a220, v112\n\t" \
  "v_accvgpr_write_b32 a221, v113\n\t" \
  "v_accvgpr_write_b32 a222, v114\n\t" \
  "v_accvgpr_write_b32 a223, v115\n\t" \
  "v_accvgpr_write_b32 a224, v120\n\t" \
  "v_accvgpr_write_b32 a225, v121\n\t" \
  "v_accvgpr_write_b32 a226, v122\n\t" \
  "v_accvgpr_write_b32 a227, v123\n\t" \
  "v_accvgpr_write_b32 a228, v128\n\t" \
  "v_accvgpr_write_b32 a229, v129\n\t" \
  "v_accvgpr_write_b32 a230, v130\n\t" \
  "v_accvgpr_write_b32 a231, v131\n\t" \
  "v_accvgpr_write_b32 a232, v136\n\t" \
  "v_accvgpr_write_b32 a233, v137\n\t" \
  "v_accvgpr_write_b32 a234, v138\n\t" \
  "v_accvgpr_write_b32 a235, v139\n\t" \
  "v_accvgpr_write_b32 a236, v144\n\t" \
  "v_accvgpr_write_b32 a237, v145\n\t" \
  "v_accvgpr_write_b32 a238, v146\n\t" \
  "v_accvgpr_write_b32 a239, v147\n\t" \
  "v_accvgpr_write_b32 a240, v152\n\t" \
  "v_accvgpr_write_b32 a241, v153\n\t" \
  "v_accvgpr_write_b32 a242, v154\n\t" \
  "v_accvgpr_write_b32 a243, v155\n\t" \
  "QD_%=:\n\t" \
  "s_waitcnt lgkmcnt(0)\n\t" \
  "s_waitcnt vmcnt(8)\n\t" \
  "s_barrier\n\t" \
  "ds_read_b128 v[160:163], v243 offset:0\n\t" \
  "ds_read_b128 v[164:167], v243 offset:1024\n\t" \
  "ds_read_b128 v[168:171], v243 offset:2048\n\t" \
  "ds_read_b128 v[172:175], v243 offset:3072\n\t" \
  "v_add_u32 v212, s72, v216\n\t" \
  "v_add_u32 v213, s72, v217\n\t" \
  "ds_read_b128 v[104:107], v212 offset:0\n\t" \
  "ds_read_b128 v[108:111], v212 offset:512\n\t" \
  "ds_read_b128 v[112:115], v212 offset:1024\n\t" \
  "ds_read_b128 v[116:119], v212 offset:1536\n\t" \
  "ds_read_b128 v[120:123], v212 offset:4096\n\t" \
  "ds_read_b128 v[124:127], v212 offset:4608\n\t" \
  "ds_read_b128 v[128:131], v212 offset:5120\n\t" \
  "ds_read_b128 v[132:135], v212 offset:5632\n\t" \
  "v_mov_b32 v208, 0x3f803f80\n\t" \
  "v_mov_b32 v209, 0x3f803f80\n\t" \
  "v_mov_b32 v210, 0x3f803f80\n\t" \
  "v_mov_b32 v211, 0x3f803f80\n\t" \
  "s_add_u32 m0, s82, s75\n\t" \
  "s_mov_b32 s61, 49152\n\t" \
  "buffer_load_dwordx4 v220, s[48:51], s61 offen nt lds\n\t" \
  "s_add_u32 s63, s61, 4096\n\t" \
  "buffer_load_dwordx4 v220, s[48:51], s61 offen offset:2048 nt lds\n\t" \
  "s_add_u32 m0, m0, 4096\n\t" \
  "s_nop 0\n\t" \
  "buffer_load_dwordx4 v220, s[48:51], s63 offen nt lds\n\t" \
  "buffer_load_dwordx4 v220, s[48:51], s63 offen offset:2048 nt lds\n\t" \
  "s_add_u32 m0, s82, s79\n\t" \
  "s_mov_b32 s61, 32768\n\t" \
  "buffer_load_dwordx4 v221, s[52:55], s61 offen nt lds\n\t" \
  "s_add_u32 s63, s61, 4096\n\t" \
  "buffer_load_dwordx4 v221, s[52:55], s61 offen offset:2048 nt lds\n\t" \
  "s_add_u32 m0, m0, 4096\n\t" \
  "s_nop 0\n\t" \
  "buffer_load_dwordx4 v221, s[52:55], s63 offen nt lds\n\t" \
  "buffer_load_dwordx4 v221, s[52:55], s63 offen offset:2048 nt lds\n\t" \
  "s_waitcnt lgkmcnt(5)\n\t" \
  "v_mfma_f32_16x16x32_bf16 v[96:99], v[104:107], v[160:163], 0\n\t" \
  "s_mov_b64 s[60:61], %21\n\t" \
  "s_mov_b64 s[62:63], %25\n\t" \
  "s_mul_i32 s63, s63, 288\n\t" \
  "v_mfma_f32_16x16x32_bf16 v[96:99], v[108:111], v[164:167], v[96:99]\n\t" \
  "s_sub_u32 s60, s60, s63\n\t" \
  "s_subb_u32 s61, s61, 0\n\t" \
  "s_lshl_b32 s62, s62, 10\n\t" \
  "v_mfma_f32_16x16x32_bf16 v[96:99], v[112:115], v[168:171], v[96:99]\n\t" \
  "s_mov_b64 s[64:65], %17\n\t" \
  "s_lshl_b32 s65, s65, 5\n\t" \
  "s_add_u32 s62, s62, s65\n\t" \
  "s_waitcnt lgkmcnt(2)\n\t" \
  "v_mfma_f32_16x16x32_bf16 v[96:99], v[116:119], v[172:175], v[96:99]\n\t" \
  "s_mul_i32 s62, s62, 1152\n\t" \
  "s_lshr_b32 s64, s64, 10\n\t" \
  "s_mul_i32 s64, s64, 9216\n\t" \
  "v_mfma_f32_16x16x32_bf16 v[100:103], v[120:123], v[160:163], 0\n\t" \
  "s_add_u32 s62, s62, s64\n\t" \
  "v_lshlrev_b32 v249, 3, v244\n\t" \
  "v_and_b32 v250, 0x380, v249\n\t" \
  "v_mfma_f32_16x16x32_bf16 v[100:103], v[124:127], v[164:167], v[100:103]\n\t" \
  "v_add_u32 v249, s62, v249\n\t" \
  "s_add_u32 s62, s62, 8192\n\t" \
  "v_add_u32 v250, s62, v250\n\t" \
  "s_waitcnt lgkmcnt(0)\n\t" \
  "v_mfma_f32_16x16x32_bf16 v[100:103], v[128:131], v[168:171], v[100:103]\n\t" \
  "global_load_dword v218, v249, s[60:61]\n\t" \
  "global_load_dword v219, v250, s[60:61]\n\t" \
  "v_lshrrev_b32 v254, 4, v244\n\t" \
  "v_mfma_f32_16x16x32_bf16 v[100:103], v[132:135], v[172:175], v[100:103]\n\t" \
  "v_and_b32 v255, 15, v254\n\t" \
  "v_min_u32 v255, 8, v255\n\t" \
  "s_lshl_b32 s62, s85, 11\n\t" \
  "v_mfma_f32_16x16x32_bf16 v[32:35], v[104:107], a[180:183], 0\n\t" \
  "s_sub_u32 s62, s84, s62\n\t" \
  "s_sub_u32 s62, s62, 1048576\n\t" \
  "v_lshlrev_b32 v252, 17, v255\n\t" \
  "v_mfma_f32_16x16x32_bf16 v[32:35], v[108:111], a[184:187], v[32:35]\n\t" \
  "v_add_u32 v252, s62, v252\n\t" \
  "v_mov_b32 v253, 0\n\t" \
  "v_add_co_u32 v252, vcc, s56, v252\n\t" \
  "v_mfma_f32_16x16x32_bf16 v[32:35], v[112:115], a[188:191], v[32:35]\n\t" \
  "v_mov_b32 v251, s57\n\t" \
  "s_nop 1\n\t" \
  "v_mfma_f32_16x16x32_bf16 v[32:35], v[116:119], a[192:195], v[32:35]\n\t" \
  "v_addc_co_u32 v253, vcc, v251, v253, vcc\n\t" \
  "s_mov_b64 s[62:63], %23\n\t" \
  "v_mul_u32_u24 v250, 0x9000, v255\n\t" \
  "v_mfma_f32_16x16x32_bf16 v[36:39], v[120:123], a[180:183], 0\n\t" \
  "v_max3_f32 v156, v96, v97, v98\n\t" \
  "v_max3_f32 v247, v99, v100, v101\n\t" \
  "v_max3_f32 v156, v156, v102, v103\n\t" \
  "v_mfma_f32_16x16x32_bf16 v[36:39], v[124:127], a[184:187], v[36:39]\n\t" \
  "v_max_f32 v156, v156, v247\n\t" \
  "v_mov_b32 v247, v156\n\t" \
  "s_nop 1\n\t" \
  "v_permlane16_swap_b32 v156, v247\n\t" \
  "v_mfma_f32_16x16x32_bf16 v[36:39], v[128:131], a[188:191], v[36:39]\n\t" \
  "v_max_f32 v156, v156, v247\n\t" \
  "v_mov_b32 v247, v156\n\t" \
  "s_nop 1\n\t" \
  "v_permlane32_swap_b32 v156, v247\n\t" \
  "v_mfma_f32_16x16x32_bf16 v[36:39], v[132:135], a[192:195], v[36:39]\n\t" \
  "v_max_f32 v156, v156, v247\n\t" \
  "v_sub_f32 v96, v96, v156\n\t" \
  "v_sub_f32 v97, v97, v156\n\t" \
  "v_mfma_f32_16x16x32_bf16 v[40:43], v[104:107], a[196:199], 0\n\t" \
  "v_sub_f32 v98, v98, v156\n\t" \
  "v_sub_f32 v99, v99, v156\n\t" \
  "v_sub_f32 v100, v100, v156\n\t" \
  "v_mfma_f32_16x16x32_bf16 v[40:43], v[108:111], a[200:203], v[40:43]\n\t" \
  "v_sub_f32 v101, v101, v156\n\t" \
  "v_sub_f32 v102, v102, v156\n\t" \
  "v_sub_f32 v103, v103, v156\n\t" \
  "v_mfma_f32_16x16x32_bf16 v[40:43], v[112:115], a[204:207], v[40:43]\n\t" \
  "v_sub_f32 v156, 0, v156\n\t" \
  "v_mov_b32 v157, v156\n\t" \
  "v_mov_b32 v158, v156\n\t" \
  "v_mfma_f32_16x16x32_bf16 v[40:43], v[116:119], a[208:211], v[40:43]\n\t" \
  "v_mov_b32 v159, v156\n\t" \
  "v_exp_f32 v96, v96\n\t" \
  "v_mfma_f32_16x16x32_bf16 v[44:47], v[120:123], a[196:199], 0\n\t" \
  "v_exp_f32 v97, v97\n\t" \
  "v_mov_b32 v251, 0\n\t" \
  "v_mfma_f32_16x16x32_bf16 v[44:47], v[124:127], a[200:203], v[44:47]\n\t" \
  "v_exp_f32 v98, v98\n\t" \
  "v_add_co_u32 v250, vcc, s62, v250\n\t" \
  "v_mfma_f32_16x16x32_bf16 v[44:47], v[128:131], a[204:207], v[44:47]\n\t" \
  "v_exp_f32 v99, v99\n\t" \
  "v_mov_b32 v249, s63\n\t" \
  "v_mfma_f32_16x16x32_bf16 v[44:47], v[132:135], a[208:211], v[44:47]\n\t" \
  "v_exp_f32 v100, v100\n\t" \
  "v_mfma_f32_16x16x32_bf16 v[48:51], v[104:107], a[212:215], 0\n\t" \
  "v_exp_f32 v101, v101\n\t" \
  "v_mfma_f32_16x16x32_bf16 v[48:51], v[108:111], a[216:219], v[48:51]\n\t" \
  "v_exp_f32 v102, v102\n\t" \
  "v_mfma_f32_16x16x32_bf16 v[48:51], v[112:115], a[220:223], v[48:51]\n\t" \
  "v_exp_f32 v103, v103\n\t" \
  "v_max3_f32 v140, v32, v33, v34\n\t" \
  "v_mfma_f32_16x16x32_bf16 v[48:51], v[116:119], a[224:227], v[48:51]\n\t" \
  "v_max3_f32 v247, v35, v36, v37\n\t" \
  "v_max3_f32 v140, v140, v38, v39\n\t" \
  "v_max_f32 v140, v140, v247\n\t" \
  "v_mfma_f32_16x16x32_bf16 v[52:55], v[120:123], a[212:215], 0\n\t" \
  "v_mov_b32 v247, v140\n\t" \
  "s_nop 1\n\t" \
  "v_permlane16_swap_b32 v140, v247\n\t" \
  "v_max_f32 v140, v140, v247\n\t" \
  "v_mfma_f32_16x16x32_bf16 v[52:55], v[124:127], a[216:219], v[52:55]\n\t" \
  "v_mov_b32 v247, v140\n\t" \
  "s_nop 1\n\t" \
  "v_permlane32_swap_b32 v140, v247\n\t" \
  "v_max_f32 v140, v140, v247\n\t" \
  "v_mfma_f32_16x16x32_bf16 v[52:55], v[128:131], a[220:223], v[52:55]\n\t" \
  "v_sub_f32 v32, v32, v140\n\t" \
  "v_sub_f32 v33, v33, v140\n\t" \
  "v_sub_f32 v34, v34, v140\n\t" \
  "v_mfma_f32_16x16x32_bf16 v[52:55], v[132:135], a[224:227], v[52:55]\n\t" \
  "v_sub_f32 v35, v35, v140\n\t" \
  "v_sub_f32 v36, v36, v140\n\t" \
  "v_sub_f32 v37, v37, v140\n\t" \
  "v_mfma_f32_16x16x32_bf16 v[56:59], v[104:107], a[228:231], 0\n\t" \
  "v_sub_f32 v38, v38, v140\n\t" \
  "v_sub_f32 v39, v39, v140\n\t" \
  "v_sub_f32 v140, 0, v140\n\t" \
  "v_mfma_f32_16x16x32_bf16 v[56:59], v[108:111], a[232:235], v[56:59]\n\t" \
  "v_mov_b32 v141, v140\n\t" \
  "v_mov_b32 v142, v140\n\t" \
  "v_mov_b32 v143, v140\n\t" \
  "v_mfma_f32_16x16x32_bf16 v[56:59], v[112:115], a[236:239], v[56:59]\n\t" \
  "v_max3_f32 v144, v40, v41, v42\n\t" \
  "v_max3_f32 v247, v43, v44, v45\n\t" \
  "v_max3_f32 v144, v144, v46, v47\n\t" \
  "v_mfma_f32_16x16x32_bf16 v[56:59], v[116:119], a[240:243], v[56:59]\n\t" \
  "v_max_f32 v144, v144, v247\n\t" \
  "v_mov_b32 v247, v144\n\t" \
  "s_nop 1\n\t" \
  "v_permlane16_swap_b32 v144, v247\n\t" \
  "v_mfma_f32_16x16x32_bf16 v[60:63], v[120:123], a[228:231], 0\n\t" \
  "v_max_f32 v144, v144, v247\n\t" \
  "v_mov_b32 v247, v144\n\t" \
  "s_nop 1\n\t" \
  "v_permlane32_swap_b32 v144, v247\n\t" \
  "v_mfma_f32_16x16x32_bf16 v[60:63], v[124:127], a[232:235], v[60:63]\n\t" \
  "v_max_f32 v144, v144, v247\n\t" \
  "v_sub_f32 v40, v40, v144\n\t" \
  "v_sub_f32 v41, v41, v144\n\t" \
  "v_mfma_f32_16x16x32_bf16 v[60:63], v[128:131], a[236:239], v[60:63]\n\t" \
  "v_sub_f32 v42, v42, v144\n\t" \
  "v_sub_f32 v43, v43, v144\n\t" \
  "v_sub_f32 v44, v44, v144\n\t" \
  "v_mfma_f32_16x16x32_bf16 v[60:63], v[132:135], a[240:243], v[60:63]\n\t" \
  "v_sub_f32 v45, v45, v144\n\t" \
  "v_sub_f32 v46, v46, v144\n\t" \
  "v_sub_f32 v47, v47, v144\n\t" \
  "v_sub_f32 v144, 0, v144\n\t" \
  "v_mov_b32 v145, v144\n\t" \
  "v_mov_b32 v146, v144\n\t" \
  "v_mov_b32 v147, v144\n\t" \
  "v_max3_f32 v148, v48, v49, v50\n\t" \
  "v_max3_f32 v247, v51, v52, v53\n\t" \
  "v_max3_f32 v148, v148, v54, v55\n\t" \
  "v_max_f32 v148, v148, v247\n\t" \
  "v_mov_b32 v247, v148\n\t" \
  "s_nop 1\n\t" \
  "v_permlane16_swap_b32 v148, v247\n\t" \
  "v_max_f32 v148, v148, v247\n\t" \
  "v_mov_b32 v247, v148\n\t" \
  "s_nop 1\n\t" \
  "v_permlane32_swap_b32 v148, v247\n\t" \
  "v_max_f32 v148, v148, v247\n\t" \
  "v_sub_f32 v48, v48, v148\n\t" \
  "v_sub_f32 v49, v49, v148\n\t" \
  "v_sub_f32 v50, v50, v148\n\t" \
  "v_sub_f32 v51, v51, v148\n\t" \
  "v_sub_f32 v52, v52, v148\n\t" \
  "v_sub_f32 v53, v53, v148\n\t" \
  "v_sub_f32 v54, v54, v148\n\t" \
  "v_sub_f32 v55, v55, v148\n\t" \
  "v_sub_f32 v148, 0, v148\n\t" \
  "v_mov_b32 v149, v148\n\t" \
  "v_mov_b32 v150, v148\n\t" \
  "v_mov_b32 v151, v148\n\t" \
  "v_max3_f32 v152, v56, v57, v58\n\t" \
  "v_max3_f32 v247, v59, v60, v61\n\t" \
  "v_max3_f32 v152, v152, v62, v63\n\t" \
  "v_max_f32 v152, v152, v247\n\t" \
  "v_mov_b32 v247, v152\n\t" \
  "s_nop 1\n\t" \
  "v_permlane16_swap_b32 v152, v247\n\t" \
  "v_max_f32 v152, v152, v247\n\t" \
  "v_mov_b32 v247, v152\n\t" \
  "s_nop 1\n\t" \
  "v_permlane32_swap_b32 v152, v247\n\t" \
  "v_max_f32 v152, v152, v247\n\t" \
  "v_sub_f32 v56, v56, v152\n\t" \
  "v_sub_f32 v57, v57, v152\n\t" \
  "v_sub_f32 v58, v58, v152\n\t" \
  "v_sub_f32 v59, v59, v152\n\t" \
  "v_sub_f32 v60, v60, v152\n\t" \
  "v_sub_f32 v61, v61, v152\n\t" \
  "v_sub_f32 v62, v62, v152\n\t" \
  "v_sub_f32 v63, v63, v152\n\t" \
  "v_sub_f32 v152, 0, v152\n\t" \
  "v_mov_b32 v153, v152\n\t" \
  "v_mov_b32 v154, v152\n\t" \
  "v_mov_b32 v155, v152\n\t" \
  "s_nop 1\n\t" \
  "v_addc_co_u32 v251, vcc, v249, v251, vcc\n\t" \
  "v_cmp_lt_u32 vcc, 15, v254\n\t" \
  "s_nop 1\n\t" \
  "v_cndmask_b32 v252, v252, v250, vcc\n\t" \
  "v_cndmask_b32 v253, v253, v251, vcc\n\t" \
  "s_mov_b64 s[62:63], %22\n\t" \
  "v_and_b32 v250, 3, v255\n\t" \
  "v_mul_u32_u24 v250, 0xc000, v250\n\t" \
  "v_add_u32 v250, 0x2c00, v250\n\t" \
  "v_mov_b32 v251, 0\n\t" \
  "v_add_co_u32 v250, vcc, s62, v250\n\t" \
  "v_mov_b32 v249, s63\n\t" \
  "s_nop 1\n\t" \
  "v_addc_co_u32 v251, vcc, v249, v251, vcc\n\t" \
  "v_cmp_lt_u32 vcc, 31, v254\n\t" \
  "s_nop 1\n\t" \
  "v_cndmask_b32 v252, v252, v250, vcc\n\t" \
  "v_cndmask_b32 v253, v253, v251, vcc\n\t" \
  "s_nop 1\n\t" \
  "global_load_dword v230, v[252:253], off\n\t" \
  "v_lshrrev_b32 v232, 4, v244\n\t" \
  "v_add_u32 v232, 0, v232\n\t" \
  "v_mul_u32_u24 v248, 0xe39, v232\n\t" \
  "v_lshrrev_b32 v248, 16, v248\n\t" \
  "v_mul_u32_u24 v248, 0x360, v248\n\t" \
  "v_lshl_add_u32 v232, v232, 4, v248\n\t" \
  "v_lshrrev_b32 v233, 4, v244\n\t" \
  "v_add_u32 v233, 64, v233\n\t" \
  "v_mul_u32_u24 v248, 0xe39, v233\n\t" \
  "v_lshrrev_b32 v248, 16, v248\n\t" \
  "v_mul_u32_u24 v248, 0x360, v248\n\t" \
  "v_lshl_add_u32 v233, v233, 4, v248\n\t" \
  "v_lshrrev_b32 v234, 4, v244\n\t" \
  "v_add_u32 v234, 128, v234\n\t" \
  "v_mul_u32_u24 v248, 0xe39, v234\n\t" \
  "v_lshrrev_b32 v248, 16, v248\n\t" \
  "v_mul_u32_u24 v248, 0x360, v248\n\t" \
  "v_lshl_add_u32 v234, v234, 4, v248\n\t" \
  "v_lshrrev_b32 v235, 4, v244\n\t" \
  "v_add_u32 v235, 192, v235\n\t" \
  "v_mul_u32_u24 v248, 0xe39, v235\n\t" \
  "v_lshrrev_b32 v248, 16, v248\n\t" \
  "v_mul_u32_u24 v248, 0x360, v248\n\t" \
  "v_lshl_add_u32 v235, v235, 4, v248\n\t" \
  "v_lshrrev_b32 v236, 4, v244\n\t" \
  "v_add_u32 v236, 256, v236\n\t" \
  "v_mul_u32_u24 v248, 0xe39, v236\n\t" \
  "v_lshrrev_b32 v248, 16, v248\n\t" \
  "v_mul_u32_u24 v248, 0x360, v248\n\t" \
  "v_lshl_add_u32 v236, v236, 4, v248\n\t" \
  "v_lshrrev_b32 v237, 4, v244\n\t" \
  "v_add_u32 v237, 320, v237\n\t" \
  "v_mul_u32_u24 v248, 0xe39, v237\n\t" \
  "v_lshrrev_b32 v248, 16, v248\n\t" \
  "v_mul_u32_u24 v248, 0x360, v248\n\t" \
  "v_lshl_add_u32 v237, v237, 4, v248\n\t" \
  "v_lshrrev_b32 v238, 4, v244\n\t" \
  "v_add_u32 v238, 384, v238\n\t" \
  "v_mul_u32_u24 v248, 0xe39, v238\n\t" \
  "v_lshrrev_b32 v248, 16, v248\n\t" \
  "v_mul_u32_u24 v248, 0x360, v248\n\t" \
  "v_lshl_add_u32 v238, v238, 4, v248\n\t" \
  "v_lshrrev_b32 v239, 4, v244\n\t" \
  "v_add_u32 v239, 448, v239\n\t" \
  "v_mul_u32_u24 v248, 0xe39, v239\n\t" \
  "v_lshrrev_b32 v248, 16, v248\n\t" \
  "v_mul_u32_u24 v248, 0x360, v248\n\t" \
  "v_lshl_add_u32 v239, v239, 4, v248\n\t" \
  "v_lshrrev_b32 v240, 4, v244\n\t" \
  "v_add_u32 v240, 512, v240\n\t" \
  "v_mul_u32_u24 v248, 0xe39, v240\n\t" \
  "v_lshrrev_b32 v248, 16, v248\n\t" \
  "v_mul_u32_u24 v248, 0x360, v248\n\t" \
  "v_lshl_add_u32 v240, v240, 4, v248\n\t" \
  "ds_read_b128 v[176:179], v213 offset:0\n\t" \
  "ds_read_b128 v[180:183], v213 offset:512\n\t" \
  "ds_read_b128 v[184:187], v213 offset:1024\n\t" \
  "ds_read_b128 v[188:191], v213 offset:1536\n\t" \
  "s_waitcnt lgkmcnt(3)\n\t" \
  "v_mfma_f32_16x16x32_bf16 v[64:67], v[176:179], a[180:183], v[140:143]\n\t" \
  "v_add_u32 v212, s73, v216\n\t" \
  "v_add_u32 v222, s77, v226\n\t" \
  "v_mfma_f32_16x16x32_bf16 v[72:75], v[176:179], a[196:199], v[144:147]\n\t" \
  "v_add_u32 v223, s77, v227\n\t" \
  "v_cvt_pk_bf16_f32 v136, v96, v97\n\t" \
  "v_mfma_f32_16x16x32_bf16 v[80:83], v[176:179], a[212:215], v[148:151]\n\t" \
  "v_cvt_pk_bf16_f32 v137, v98, v99\n\t" \
  "v_cvt_pk_bf16_f32 v138, v100, v101\n\t" \
  "v_mfma_f32_16x16x32_bf16 v[88:91], v[176:179], a[228:231], v[152:155]\n\t" \
  "ds_read_b128 v[176:179], v213 offset:4096\n\t" \
  "v_cvt_pk_bf16_f32 v139, v102, v103\n\t" \
  "s_waitcnt lgkmcnt(3)\n\t" \
  "v_mfma_f32_16x16x32_bf16 v[64:67], v[180:183], a[184:187], v[64:67]\n\t" \
  "v_exp_f32 v32, v32\n\t" \
  "v_mfma_f32_16x16x32_bf16 v[72:75], v[180:183], a[200:203], v[72:75]\n\t" \
  "v_exp_f32 v33, v33\n\t" \
  "v_mfma_f32_16x16x32_bf16 v[80:83], v[180:183], a[216:219], v[80:83]\n\t" \
  "ds_read_b64_tr_b16 v[192:193], v222 offset:0\n\t" \
  "ds_read_b64_tr_b16 v[194:195], v222 offset:4096\n\t" \
  "v_mfma_f32_16x16x32_bf16 v[88:91], v[180:183], a[232:235], v[88:91]\n\t" \
  "ds_read_b128 v[180:183], v213 offset:4608\n\t" \
  "s_waitcnt lgkmcnt(5)\n\t" \
  "v_mfma_f32_16x16x32_bf16 v[64:67], v[184:187], a[188:191], v[64:67]\n\t" \
  "v_exp_f32 v34, v34\n\t" \
  "v_mfma_f32_16x16x32_bf16 v[72:75], v[184:187], a[204:207], v[72:75]\n\t" \
  "v_exp_f32 v35, v35\n\t" \
  "v_mfma_f32_16x16x32_bf16 v[80:83], v[184:187], a[220:223], v[80:83]\n\t" \
  "ds_read_b64_tr_b16 v[196:197], v223 offset:0\n\t" \
  "ds_read_b64_tr_b16 v[198:199], v223 offset:4096\n\t" \
  "v_mfma_f32_16x16x32_bf16 v[88:91], v[184:187], a[236:239], v[88:91]\n\t" \
  "ds_read_b128 v[184:187], v213 offset:5120\n\t" \
  "v_cvt_pk_bf16_f32 v104, v32, v33\n\t" \
  "s_waitcnt lgkmcnt(7)\n\t" \
  "v_mfma_f32_16x16x32_bf16 v[64:67], v[188:191], a[192:195], v[64:67]\n\t" \
  "v_cvt_pk_bf16_f32 v105, v34, v35\n\t" \
  "v_mfma_f32_16x16x32_bf16 v[72:75], v[188:191], a[208:211], v[72:75]\n\t" \
  "v_exp_f32 v36, v36\n\t" \
  "v_mfma_f32_16x16x32_bf16 v[80:83], v[188:191], a[224:227], v[80:83]\n\t" \
  "ds_read_b64_tr_b16 v[200:201], v222 offset:512\n\t" \
  "ds_read_b64_tr_b16 v[202:203], v222 offset:4608\n\t" \
  "v_mfma_f32_16x16x32_bf16 v[88:91], v[188:191], a[240:243], v[88:91]\n\t" \
  "ds_read_b128 v[188:191], v213 offset:5632\n\t" \
  "s_waitcnt lgkmcnt(9)\n\t" \
  "v_mfma_f32_16x16x32_bf16 v[68:71], v[176:179], a[180:183], v[140:143]\n\t" \
  "v_exp_f32 v37, v37\n\t" \
  "v_mfma_f32_16x16x32_bf16 v[76:79], v[176:179], a[196:199], v[144:147]\n\t" \
  "v_exp_f32 v38, v38\n\t" \
  "v_mfma_f32_16x16x32_bf16 v[84:87], v[176:179], a[212:215], v[148:151]\n\t" \
  "ds_read_b64_tr_b16 v[204:205], v223 offset:512\n\t" \
  "ds_read_b64_tr_b16 v[206:207], v223 offset:4608\n\t" \
  "v_mfma_f32_16x16x32_bf16 v[92:95], v[176:179], a[228:231], v[152:155]\n\t" \
  "ds_read_b128 v[176:179], v212 offset:0\n\t" \
  "s_waitcnt lgkmcnt(9)\n\t" \
  "v_mfma_f32_16x16x32_bf16 v[68:71], v[180:183], a[184:187], v[68:71]\n\t" \
  "v_exp_f32 v39, v39\n\t" \
  "v_mfma_f32_16x16x32_bf16 v[76:79], v[180:183], a[200:203], v[76:79]\n\t" \
  "v_cvt_pk_bf16_f32 v106, v36, v37\n\t" \
  "v_cvt_pk_bf16_f32 v107, v38, v39\n\t" \
  "v_mfma_f32_16x16x32_bf16 v[84:87], v[180:183], a[216:219], v[84:87]\n\t" \
  "v_exp_f32 v40, v40\n\t" \
  "v_mfma_f32_16x16x32_bf16 v[92:95], v[180:183], a[232:235], v[92:95]\n\t" \
  "ds_read_b128 v[180:183], v212 offset:512\n\t" \
  "s_waitcnt lgkmcnt(7)\n\t" \
  "v_mfma_f32_16x16x32_bf16 v[68:71], v[184:187], a[188:191], v[68:71]\n\t" \
  "v_exp_f32 v41, v41\n\t" \
  "v_mfma_f32_16x16x32_bf16 v[76:79], v[184:187], a[204:207], v[76:79]\n\t" \
  "v_exp_f32 v42, v42\n\t" \
  "v_mfma_f32_16x16x32_bf16 v[84:87], v[184:187], a[220:223], v[84:87]\n\t" \
  "v_exp_f32 v43, v43\n\t" \
  "v_mfma_f32_16x16x32_bf16 v[92:95], v[184:187], a[236:239], v[92:95]\n\t" \
  "ds_read_b128 v[184:187], v212 offset:1024\n\t" \
  "v_cvt_pk_bf16_f32 v108, v40, v41\n\t" \
  "s_waitcnt lgkmcnt(5)\n\t" \
  "v_mfma_f32_16x16x32_bf16 v[68:71], v[188:191], a[192:195], v[68:71]\n\t" \
  "v_cvt_pk_bf16_f32 v109, v42, v43\n\t" \
  "v_mfma_f32_16x16x32_bf16 v[76:79], v[188:191], a[208:211], v[76:79]\n\t" \
  "v_exp_f32 v44, v44\n\t" \
  "v_mfma_f32_16x16x32_bf16 v[84:87], v[188:191], a[224:227], v[84:87]\n\t" \
  "v_exp_f32 v45, v45\n\t" \
  "v_mfma_f32_16x16x32_bf16 v[92:95], v[188:191], a[240:243], v[92:95]\n\t" \
  "ds_read_b128 v[188:191], v212 offset:1536\n\t" \
  "v_exp_f32 v46, v46\n\t" \
  "v_exp_f32 v47, v47\n\t" \
  "v_cvt_pk_bf16_f32 v110, v44, v45\n\t" \
  "v_cvt_pk_bf16_f32 v111, v46, v47\n\t" \
  "v_exp_f32 v48, v48\n\t" \
  "v_exp_f32 v49, v49\n\t" \
  "v_exp_f32 v50, v50\n\t" \
  "v_exp_f32 v51, v51\n\t" \
  "v_cvt_pk_bf16_f32 v112, v48, v49\n\t" \
  "v_cvt_pk_bf16_f32 v113, v50, v51\n\t" \
  "v_exp_f32 v52, v52\n\t" \
  "v_exp_f32 v53, v53\n\t" \
  "v_exp_f32 v54, v54\n\t" \
  "v_exp_f32 v55, v55\n\t" \
  "v_cvt_pk_bf16_f32 v114, v52, v53\n\t" \
  "v_cvt_pk_bf16_f32 v115, v54, v55\n\t" \
  "v_exp_f32 v56, v56\n\t" \
  "v_exp_f32 v57, v57\n\t" \
  "v_exp_f32 v58, v58\n\t" \
  "v_exp_f32 v59, v59\n\t" \
  "v_cvt_pk_bf16_f32 v116, v56, v57\n\t" \
  "v_cvt_pk_bf16_f32 v117, v58, v59\n\t" \
  "v_exp_f32 v60, v60\n\t" \
  "v_exp_f32 v61, v61\n\t" \
  "v_exp_f32 v62, v62\n\t" \
  "v_exp_f32 v63, v63\n\t" \
  "v_cvt_pk_bf16_f32 v118, v60, v61\n\t" \
  "v_cvt_pk_bf16_f32 v119, v62, v63\n\t" \
  "s_mov_b32 s61, s72\n\t" \
  "s_mov_b32 s72, s73\n\t" \
  "s_mov_b32 s73, s74\n\t" \
  "s_mov_b32 s74, s75\n\t" \
  "s_add_u32 s80, s80, 16384\n\t" \
  "s_mov_b32 s75, s61\n\t" \
  "s_mov_b32 s61, s76\n\t" \
  "s_mov_b32 s76, s77\n\t" \
  "s_mov_b32 s77, s78\n\t" \
  "s_mov_b32 s78, s79\n\t" \
  "s_add_u32 s81, s81, 16384\n\t" \
  "s_mov_b32 s79, s61\n\t" \
  "s_mov_b32 s60, 15\n\t" \
  "s_waitcnt lgkmcnt(0)\n\t" \
  "s_waitcnt vmcnt(11)\n\t" \
  "ALOOP_%=:\n\t" \
  "s_barrier\n\t" \
  "s_waitcnt lgkmcnt(11)\n\t" \
  "v_mfma_f32_16x16x32_bf16 v[96:99], v[176:179], v[160:163], v[156:159]\n\t" \
  "v_add_u32 v213, s72, v217\n\t" \
  "v_add_u32 v224, s76, v228\n\t" \
  "v_mfma_f32_16x16x32_bf16 v[32:35], v[176:179], a[180:183], v[140:143]\n\t" \
  "v_add_u32 v225, s76, v229\n\t" \
  "v_mfma_f32_16x16x32_bf16 v[40:43], v[176:179], a[196:199], v[144:147]\n\t" \
  "v_exp_f32 v64, v64\n\t" \
  "v_mfma_f32_16x16x32_bf16 v[48:51], v[176:179], a[212:215], v[148:151]\n\t" \
  "s_add_u32 m0, s82, s75\n\t" \
  "v_mfma_f32_16x16x32_bf16 v[56:59], v[176:179], a[228:231], v[152:155]\n\t" \
  "ds_read_b128 v[176:179], v212 offset:4096\n\t" \
  "s_waitcnt lgkmcnt(9)\n\t" \
  "v_mfma_f32_16x16x32_bf16 v[96:99], v[180:183], v[164:167], v[96:99]\n\t" \
  "v_exp_f32 v65, v65\n\t" \
  "v_mfma_f32_16x16x32_bf16 v[32:35], v[180:183], a[184:187], v[32:35]\n\t" \
  "v_exp_f32 v66, v66\n\t" \
  "v_mfma_f32_16x16x32_bf16 v[40:43], v[180:183], a[200:203], v[40:43]\n\t" \
  "buffer_load_dwordx4 v220, s[48:51], s80 offen nt lds\n\t" \
  "v_mfma_f32_16x16x32_bf16 v[48:51], v[180:183], a[216:219], v[48:51]\n\t" \
  "v_exp_f32 v67, v67\n\t" \
  "v_mfma_f32_16x16x32_bf16 v[56:59], v[180:183], a[232:235], v[56:59]\n\t" \
  "ds_read_b128 v[180:183], v212 offset:4608\n\t" \
  "v_cvt_pk_bf16_f32 v120, v64, v65\n\t" \
  "v_mfma_f32_16x16x32_bf16 a[0:3], v[192:195], v[104:107], a[0:3]\n\t" \
  "v_cvt_pk_bf16_f32 v121, v66, v67\n\t" \
  "v_mfma_f32_16x16x32_bf16 a[32:35], v[192:195], v[108:111], a[32:35]\n\t" \
  "s_add_u32 s63, s80, 4096\n\t" \
  "v_mfma_f32_16x16x32_bf16 a[64:67], v[192:195], v[112:115], a[64:67]\n\t" \
  "v_exp_f32 v68, v68\n\t" \
  "v_mfma_f32_16x16x32_bf16 a[96:99], v[192:195], v[116:119], a[96:99]\n\t" \
  "v_exp_f32 v69, v69\n\t" \
  "v_mfma_f32_16x16x32_bf16 a[128:131], v[192:195], v[136:139], a[128:131]\n\t" \
  "ds_read_b64_tr_b16 v[192:193], v222 offset:1024\n\t" \
  "ds_read_b64_tr_b16 v[194:195], v222 offset:5120\n\t" \
  "s_waitcnt lgkmcnt(9)\n\t" \
  "v_mfma_f32_16x16x32_bf16 v[96:99], v[184:187], v[168:171], v[96:99]\n\t" \
  "buffer_load_dwordx4 v220, s[48:51], s80 offen offset:2048 nt lds\n\t" \
  "v_mfma_f32_16x16x32_bf16 v[32:35], v[184:187], a[188:191], v[32:35]\n\t" \
  "v_exp_f32 v70, v70\n\t" \
  "v_mfma_f32_16x16x32_bf16 v[40:43], v[184:187], a[204:207], v[40:43]\n\t" \
  "v_exp_f32 v71, v71\n\t" \
  "v_mfma_f32_16x16x32_bf16 v[48:51], v[184:187], a[220:223], v[48:51]\n\t" \
  "v_cvt_pk_bf16_f32 v122, v68, v69\n\t" \
  "v_cvt_pk_bf16_f32 v123, v70, v71\n\t" \
  "v_mfma_f32_16x16x32_bf16 v[56:59], v[184:187], a[236:239], v[56:59]\n\t" \
  "s_add_u32 m0, m0, 4096\n\t" \
  "ds_read_b128 v[184:187], v212 offset:5120\n\t" \
  "s_waitcnt lgkmcnt(7)\n\t" \
  "v_mfma_f32_16x16x32_bf16 v[96:99], v[188:191], v[172:175], v[96:99]\n\t" \
  "v_exp_f32 v72, v72\n\t" \
  "v_mfma_f32_16x16x32_bf16 v[32:35], v[188:191], a[192:195], v[32:35]\n\t" \
  "v_exp_f32 v73, v73\n\t" \
  "v_mfma_f32_16x16x32_bf16 v[40:43], v[188:191], a[208:211], v[40:43]\n\t" \
  "v_exp_f32 v74, v74\n\t" \
  "v_mfma_f32_16x16x32_bf16 v[48:51], v[188:191], a[224:227], v[48:51]\n\t" \
  "buffer_load_dwordx4 v220, s[48:51], s63 offen nt lds\n\t" \
  "v_mfma_f32_16x16x32_bf16 v[56:59], v[188:191], a[240:243], v[56:59]\n\t" \
  "ds_read_b128 v[188:191], v212 offset:5632\n\t" \
  "v_mfma_f32_16x16x32_bf16 a[4:7], v[196:199], v[104:107], a[4:7]\n\t" \
  "v_exp_f32 v75, v75\n\t" \
  "v_mfma_f32_16x16x32_bf16 a[36:39], v[196:199], v[108:111], a[36:39]\n\t" \
  "v_cvt_pk_bf16_f32 v124, v72, v73\n\t" \
  "v_cvt_pk_bf16_f32 v125, v74, v75\n\t" \
  "v_mfma_f32_16x16x32_bf16 a[68:71], v[196:199], v[112:115], a[68:71]\n\t" \
  "buffer_load_dwordx4 v220, s[48:51], s63 offen offset:2048 nt lds\n\t" \
  "v_mfma_f32_16x16x32_bf16 a[100:103], v[196:199], v[116:119], a[100:103]\n\t" \
  "v_exp_f32 v76, v76\n\t" \
  "v_mfma_f32_16x16x32_bf16 a[132:135], v[196:199], v[136:139], a[132:135]\n\t" \
  "ds_read_b64_tr_b16 v[196:197], v223 offset:1024\n\t" \
  "ds_read_b64_tr_b16 v[198:199], v223 offset:5120\n\t" \
  "s_waitcnt lgkmcnt(7)\n\t" \
  "v_mfma_f32_16x16x32_bf16 v[100:103], v[176:179], v[160:163], v[156:159]\n\t" \
  "v_exp_f32 v77, v77\n\t" \
  "v_mfma_f32_16x16x32_bf16 v[36:39], v[176:179], a[180:183], v[140:143]\n\t" \
  "v_exp_f32 v78, v78\n\t" \
  "v_mfma_f32_16x16x32_bf16 v[44:47], v[176:179], a[196:199], v[144:147]\n\t" \
  "v_exp_f32 v79, v79\n\t" \
  "v_mfma_f32_16x16x32_bf16 v[52:55], v[176:179], a[212:215], v[148:151]\n\t" \
  "v_cvt_pk_bf16_f32 v126, v76, v77\n\t" \
  "v_cvt_pk_bf16_f32 v127, v78, v79\n\t" \
  "v_mfma_f32_16x16x32_bf16 v[60:63], v[176:179], a[228:231], v[152:155]\n\t" \
  "ds_read_b128 v[176:179], v213 offset:0\n\t" \
  "s_waitcnt lgkmcnt(7)\n\t" \
  "v_mfma_f32_16x16x32_bf16 v[100:103], v[180:183], v[164:167], v[100:103]\n\t" \
  "v_exp_f32 v80, v80\n\t" \
  "v_mfma_f32_16x16x32_bf16 v[36:39], v[180:183], a[184:187], v[36:39]\n\t" \
  "v_exp_f32 v81, v81\n\t" \
  "v_mfma_f32_16x16x32_bf16 v[44:47], v[180:183], a[200:203], v[44:47]\n\t" \
  "v_exp_f32 v82, v82\n\t" \
  "v_mfma_f32_16x16x32_bf16 v[52:55], v[180:183], a[216:219], v[52:55]\n\t" \
  "v_exp_f32 v83, v83\n\t" \
  "v_mfma_f32_16x16x32_bf16 v[60:63], v[180:183], a[232:235], v[60:63]\n\t" \
  "ds_read_b128 v[180:183], v213 offset:512\n\t" \
  "v_cvt_pk_bf16_f32 v128, v80, v81\n\t" \
  "v_mfma_f32_16x16x32_bf16 a[8:11], v[200:203], v[104:107], a[8:11]\n\t" \
  "v_cvt_pk_bf16_f32 v129, v82, v83\n\t" \
  "v_mfma_f32_16x16x32_bf16 a[40:43], v[200:203], v[108:111], a[40:43]\n\t" \
  "v_exp_f32 v84, v84\n\t" \
  "v_mfma_f32_16x16x32_bf16 a[72:75], v[200:203], v[112:115], a[72:75]\n\t" \
  "v_mfma_f32_16x16x32_bf16 a[104:107], v[200:203], v[116:119], a[104:107]\n\t" \
  "v_exp_f32 v85, v85\n\t" \
  "v_mfma_f32_16x16x32_bf16 a[136:139], v[200:203], v[136:139], a[136:139]\n\t" \
  "ds_read_b64_tr_b16 v[200:201], v222 offset:1536\n\t" \
  "ds_read_b64_tr_b16 v[202:203], v222 offset:5632\n\t" \
  "s_waitcnt lgkmcnt(7)\n\t" \
  "v_mfma_f32_16x16x32_bf16 v[100:103], v[184:187], v[168:171], v[100:103]\n\t" \
  "v_exp_f32 v86, v86\n\t" \
  "v_mfma_f32_16x16x32_bf16 v[36:39], v[184:187], a[188:191], v[36:39]\n\t" \
  "v_exp_f32 v87, v87\n\t" \
  "v_mfma_f32_16x16x32_bf16 v[44:47], v[184:187], a[204:207], v[44:47]\n\t" \
  "v_mfma_f32_16x16x32_bf16 v[52:55], v[184:187], a[220:223], v[52:55]\n\t" \
  "v_cvt_pk_bf16_f32 v130, v84, v85\n\t" \
  "v_mfma_f32_16x16x32_bf16 v[60:63], v[184:187], a[236:239], v[60:63]\n\t" \
  "ds_read_b128 v[184:187], v213 offset:1024\n\t" \
  "v_cvt_pk_bf16_f32 v131, v86, v87\n\t" \
  "s_waitcnt lgkmcnt(7)\n\t" \
  "v_mfma_f32_16x16x32_bf16 v[100:103], v[188:191], v[172:175], v[100:103]\n\t" \
  "v_exp_f32 v88, v88\n\t" \
  "v_mfma_f32_16x16x32_bf16 v[36:39], v[188:191], a[192:195], v[36:39]\n\t" \
  "v_exp_f32 v89, v89\n\t" \
  "v_mfma_f32_16x16x32_bf16 v[44:47], v[188:191], a[208:211], v[44:47]\n\t" \
  "v_mfma_f32_16x16x32_bf16 v[52:55], v[188:191], a[224:227], v[52:55]\n\t" \
  "v_exp_f32 v90, v90\n\t" \
  "v_mfma_f32_16x16x32_bf16 v[60:63], v[188:191], a[240:243], v[60:63]\n\t" \
  "ds_read_b128 v[188:191], v213 offset:1536\n\t" \
  "v_mfma_f32_16x16x32_bf16 a[12:15], v[204:207], v[104:107], a[12:15]\n\t" \
  "v_exp_f32 v91, v91\n\t" \
  "v_mfma_f32_16x16x32_bf16 a[44:47], v[204:207], v[108:111], a[44:47]\n\t" \
  "v_mfma_f32_16x16x32_bf16 a[76:79], v[204:207], v[112:115], a[76:79]\n\t" \
  "v_cvt_pk_bf16_f32 v132, v88, v89\n\t" \
  "v_cvt_pk_bf16_f32 v133, v90, v91\n\t" \
  "v_mfma_f32_16x16x32_bf16 a[108:111], v[204:207], v[116:119], a[108:111]\n\t" \
  "v_exp_f32 v92, v92\n\t" \
  "v_mfma_f32_16x16x32_bf16 a[140:143], v[204:207], v[136:139], a[140:143]\n\t" \
  "ds_read_b64_tr_b16 v[204:205], v223 offset:1536\n\t" \
  "ds_read_b64_tr_b16 v[206:207], v223 offset:5632\n\t" \
  "v_mfma_f32_16x16x32_bf16 a[16:19], v[192:195], v[104:107], a[16:19]\n\t" \
  "v_exp_f32 v93, v93\n\t" \
  "v_mfma_f32_16x16x32_bf16 a[48:51], v[192:195], v[108:111], a[48:51]\n\t" \
  "v_mfma_f32_16x16x32_bf16 a[80:83], v[192:195], v[112:115], a[80:83]\n\t" \
  "v_exp_f32 v94, v94\n\t" \
  "v_mfma_f32_16x16x32_bf16 a[112:115], v[192:195], v[116:119], a[112:115]\n\t" \
  "v_exp_f32 v95, v95\n\t" \
  "v_mfma_f32_16x16x32_bf16 a[144:147], v[192:195], v[136:139], a[144:147]\n\t" \
  "ds_read_b64_tr_b16 v[192:193], v224 offset:0\n\t" \
  "ds_read_b64_tr_b16 v[194:195], v224 offset:4096\n\t" \
  "s_waitcnt lgkmcnt(10)\n\t" \
  "v_mfma_f32_16x16x32_bf16 a[20:23], v[196:199], v[104:107], a[20:23]\n\t" \
  "v_cvt_pk_bf16_f32 v134, v92, v93\n\t" \
  "v_mfma_f32_16x16x32_bf16 a[52:55], v[196:199], v[108:111], a[52:55]\n\t" \
  "v_cvt_pk_bf16_f32 v135, v94, v95\n\t" \
  "v_mfma_f32_16x16x32_bf16 a[84:87], v[196:199], v[112:115], a[84:87]\n\t" \
  "v_exp_f32 v96, v96\n\t" \
  "v_mfma_f32_16x16x32_bf16 a[116:119], v[196:199], v[116:119], a[116:119]\n\t" \
  "v_mfma_f32_16x16x32_bf16 a[148:151], v[196:199], v[136:139], a[148:151]\n\t" \
  "ds_read_b64_tr_b16 v[196:197], v225 offset:0\n\t" \
  "ds_read_b64_tr_b16 v[198:199], v225 offset:4096\n\t" \
  "s_waitcnt lgkmcnt(8)\n\t" \
  "v_mfma_f32_16x16x32_bf16 a[24:27], v[200:203], v[104:107], a[24:27]\n\t" \
  "v_exp_f32 v97, v97\n\t" \
  "v_mfma_f32_16x16x32_bf16 a[56:59], v[200:203], v[108:111], a[56:59]\n\t" \
  "v_exp_f32 v98, v98\n\t" \
  "v_mfma_f32_16x16x32_bf16 a[88:91], v[200:203], v[112:115], a[88:91]\n\t" \
  "v_exp_f32 v99, v99\n\t" \
  "v_mfma_f32_16x16x32_bf16 a[120:123], v[200:203], v[116:119], a[120:123]\n\t" \
  "v_mfma_f32_16x16x32_bf16 a[152:155], v[200:203], v[136:139], a[152:155]\n\t" \
  "ds_read_b64_tr_b16 v[200:201], v224 offset:512\n\t" \
  "ds_read_b64_tr_b16 v[202:203], v224 offset:4608\n\t" \
  "s_waitcnt lgkmcnt(6)\n\t" \
  "v_mfma_f32_16x16x32_bf16 a[28:31], v[204:207], v[104:107], a[28:31]\n\t" \
  "v_exp_f32 v100, v100\n\t" \
  "v_mfma_f32_16x16x32_bf16 a[60:63], v[204:207], v[108:111], a[60:63]\n\t" \
  "v_exp_f32 v101, v101\n\t" \
  "v_mfma_f32_16x16x32_bf16 a[92:95], v[204:207], v[112:115], a[92:95]\n\t" \
  "v_exp_f32 v102, v102\n\t" \
  "v_mfma_f32_16x16x32_bf16 a[124:127], v[204:207], v[116:119], a[124:127]\n\t" \
  "v_mfma_f32_16x16x32_bf16 a[156:159], v[204:207], v[136:139], a[156:159]\n\t" \
  "ds_read_b64_tr_b16 v[204:205], v225 offset:512\n\t" \
  "ds_read_b64_tr_b16 v[206:207], v225 offset:4608\n\t" \
  "v_mfma_f32_16x16x32_bf16 a[160:163], v[208:211], v[104:107], a[160:163]\n\t" \
  "v_exp_f32 v103, v103\n\t" \
  "v_mfma_f32_16x16x32_bf16 a[164:167], v[208:211], v[108:111], a[164:167]\n\t" \
  "v_mfma_f32_16x16x32_bf16 a[168:171], v[208:211], v[112:115], a[168:171]\n\t" \
  "v_mfma_f32_16x16x32_bf16 a[172:175], v[208:211], v[116:119], a[172:175]\n\t" \
  "v_mfma_f32_16x16x32_bf16 a[176:179], v[208:211], v[136:139], a[176:179]\n\t" \
  "v_mfma_f32_16x16x32_bf16 a[160:163], v[208:211], v[120:123], a[160:163]\n\t" \
  "v_add_u32 v212, s73, v216\n\t" \
  "v_add_u32 v222, s77, v226\n\t" \
  "v_mfma_f32_16x16x32_bf16 a[164:167], v[208:211], v[124:127], a[164:167]\n\t" \
  "v_add_u32 v223, s77, v227\n\t" \
  "v_cvt_pk_bf16_f32 v136, v96, v97\n\t" \
  "v_mfma_f32_16x16x32_bf16 a[168:171], v[208:211], v[128:131], a[168:171]\n\t" \
  "v_cvt_pk_bf16_f32 v137, v98, v99\n\t" \
  "v_cvt_pk_bf16_f32 v138, v100, v101\n\t" \
  "v_mfma_f32_16x16x32_bf16 a[172:175], v[208:211], v[132:135], a[172:175]\n\t" \
  "s_add_u32 m0, s82, s79\n\t" \
  "v_cvt_pk_bf16_f32 v139, v102, v103\n\t" \
  "v_mfma_f32_16x16x32_bf16 v[64:67], v[176:179], a[180:183], v[140:143]\n\t" \
  "v_exp_f32 v32, v32\n\t" \
  "v_mfma_f32_16x16x32_bf16 v[72:75], v[176:179], a[196:199], v[144:147]\n\t" \
  "v_exp_f32 v33, v33\n\t" \
  "v_mfma_f32_16x16x32_bf16 v[80:83], v[176:179], a[212:215], v[148:151]\n\t" \
  "v_exp_f32 v34, v34\n\t" \
  "v_mfma_f32_16x16x32_bf16 v[88:91], v[176:179], a[228:231], v[152:155]\n\t" \
  "buffer_load_dwordx4 v221, s[52:55], s81 offen nt lds\n\t" \
  "ds_read_b128 v[176:179], v213 offset:4096\n\t" \
  "s_waitcnt lgkmcnt(7)\n\t" \
  "v_mfma_f32_16x16x32_bf16 a[0:3], v[192:195], v[120:123], a[0:3]\n\t" \
  "v_exp_f32 v35, v35\n\t" \
  "v_mfma_f32_16x16x32_bf16 a[32:35], v[192:195], v[124:127], a[32:35]\n\t" \
  "v_cvt_pk_bf16_f32 v104, v32, v33\n\t" \
  "v_cvt_pk_bf16_f32 v105, v34, v35\n\t" \
  "v_mfma_f32_16x16x32_bf16 a[64:67], v[192:195], v[128:131], a[64:67]\n\t" \
  "v_exp_f32 v36, v36\n\t" \
  "v_mfma_f32_16x16x32_bf16 a[96:99], v[192:195], v[132:135], a[96:99]\n\t" \
  "s_add_u32 s63, s81, 4096\n\t" \
  "ds_read_b64_tr_b16 v[192:193], v224 offset:1024\n\t" \
  "v_mfma_f32_16x16x32_bf16 v[64:67], v[180:183], a[184:187], v[64:67]\n\t" \
  "ds_read_b64_tr_b16 v[194:195], v224 offset:5120\n\t" \
  "v_mfma_f32_16x16x32_bf16 v[72:75], v[180:183], a[200:203], v[72:75]\n\t" \
  "v_exp_f32 v37, v37\n\t" \
  "v_mfma_f32_16x16x32_bf16 v[80:83], v[180:183], a[216:219], v[80:83]\n\t" \
  "v_exp_f32 v38, v38\n\t" \
  "v_mfma_f32_16x16x32_bf16 v[88:91], v[180:183], a[232:235], v[88:91]\n\t" \
  "buffer_load_dwordx4 v221, s[52:55], s81 offen offset:2048 nt lds\n\t" \
  "ds_read_b128 v[180:183], v213 offset:4608\n\t" \
  "s_waitcnt lgkmcnt(8)\n\t" \
  "v_mfma_f32_16x16x32_bf16 a[4:7], v[196:199], v[120:123], a[4:7]\n\t" \
  "v_exp_f32 v39, v39\n\t" \
  "v_mfma_f32_16x16x32_bf16 a[36:39], v[196:199], v[124:127], a[36:39]\n\t" \
  "v_cvt_pk_bf16_f32 v106, v36, v37\n\t" \
  "v_cvt_pk_bf16_f32 v107, v38, v39\n\t" \
  "v_mfma_f32_16x16x32_bf16 a[68:71], v[196:199], v[128:131], a[68:71]\n\t" \
  "v_exp_f32 v40, v40\n\t" \
  "v_mfma_f32_16x16x32_bf16 a[100:103], v[196:199], v[132:135], a[100:103]\n\t" \
  "s_add_u32 m0, m0, 4096\n\t" \
  "ds_read_b64_tr_b16 v[196:197], v225 offset:1024\n\t" \
  "v_mfma_f32_16x16x32_bf16 v[64:67], v[184:187], a[188:191], v[64:67]\n\t" \
  "ds_read_b64_tr_b16 v[198:199], v225 offset:5120\n\t" \
  "v_mfma_f32_16x16x32_bf16 v[72:75], v[184:187], a[204:207], v[72:75]\n\t" \
  "v_exp_f32 v41, v41\n\t" \
  "v_mfma_f32_16x16x32_bf16 v[80:83], v[184:187], a[220:223], v[80:83]\n\t" \
  "v_exp_f32 v42, v42\n\t" \
  "v_mfma_f32_16x16x32_bf16 v[88:91], v[184:187], a[236:239], v[88:91]\n\t" \
  "buffer_load_dwordx4 v221, s[52:55], s63 offen nt lds\n\t" \
  "ds_read_b128 v[184:187], v213 offset:5120\n\t" \
  "s_waitcnt lgkmcnt(9)\n\t" \
  "v_mfma_f32_16x16x32_bf16 a[8:11], v[200:203], v[120:123], a[8:11]\n\t" \
  "v_exp_f32 v43, v43\n\t" \
  "v_mfma_f32_16x16x32_bf16 a[40:43], v[200:203], v[124:127], a[40:43]\n\t" \
  "v_cvt_pk_bf16_f32 v108, v40, v41\n\t" \
  "v_cvt_pk_bf16_f32 v109, v42, v43\n\t" \
  "v_mfma_f32_16x16x32_bf16 a[72:75], v[200:203], v[128:131], a[72:75]\n\t" \
  "v_exp_f32 v44, v44\n\t" \
  "v_mfma_f32_16x16x32_bf16 a[104:107], v[200:203], v[132:135], a[104:107]\n\t" \
  "buffer_load_dwordx4 v221, s[52:55], s63 offen offset:2048 nt lds\n\t" \
  "ds_read_b64_tr_b16 v[200:201], v224 offset:1536\n\t" \
  "v_mfma_f32_16x16x32_bf16 v[64:67], v[188:191], a[192:195], v[64:67]\n\t" \
  "ds_read_b64_tr_b16 v[202:203], v224 offset:5632\n\t" \
  "v_mfma_f32_16x16x32_bf16 v[72:75], v[188:191], a[208:211], v[72:75]\n\t" \
  "v_exp_f32 v45, v45\n\t" \
  "v_mfma_f32_16x16x32_bf16 v[80:83], v[188:191], a[224:227], v[80:83]\n\t" \
  "v_exp_f32 v46, v46\n\t" \
  "v_mfma_f32_16x16x32_bf16 v[88:91], v[188:191], a[240:243], v[88:91]\n\t" \
  "ds_read_b128 v[188:191], v213 offset:5632\n\t" \
  "s_waitcnt lgkmcnt(10)\n\t" \
  "v_mfma_f32_16x16x32_bf16 a[12:15], v[204:207], v[120:123], a[12:15]\n\t" \
  "v_exp_f32 v47, v47\n\t" \
  "v_mfma_f32_16x16x32_bf16 a[44:47], v[204:207], v[124:127], a[44:47]\n\t" \
  "v_cvt_pk_bf16_f32 v110, v44, v45\n\t" \
  "v_cvt_pk_bf16_f32 v111, v46, v47\n\t" \
  "v_mfma_f32_16x16x32_bf16 a[76:79], v[204:207], v[128:131], a[76:79]\n\t" \
  "v_exp_f32 v48, v48\n\t" \
  "v_mfma_f32_16x16x32_bf16 a[108:111], v[204:207], v[132:135], a[108:111]\n\t" \
  "ds_read_b64_tr_b16 v[204:205], v225 offset:1536\n\t" \
  "ds_read_b64_tr_b16 v[206:207], v225 offset:5632\n\t" \
  "s_waitcnt lgkmcnt(11)\n\t" \
  "v_mfma_f32_16x16x32_bf16 v[68:71], v[176:179], a[180:183], v[140:143]\n\t" \
  "v_exp_f32 v49, v49\n\t" \
  "v_mfma_f32_16x16x32_bf16 v[76:79], v[176:179], a[196:199], v[144:147]\n\t" \
  "v_exp_f32 v50, v50\n\t" \
  "v_mfma_f32_16x16x32_bf16 v[84:87], v[176:179], a[212:215], v[148:151]\n\t" \
  "v_exp_f32 v51, v51\n\t" \
  "v_mfma_f32_16x16x32_bf16 v[92:95], v[176:179], a[228:231], v[152:155]\n\t" \
  "ds_read_b128 v[176:179], v212 offset:0\n\t" \
  "v_cvt_pk_bf16_f32 v112, v48, v49\n\t" \
  "s_waitcnt lgkmcnt(10)\n\t" \
  "v_mfma_f32_16x16x32_bf16 a[16:19], v[192:195], v[120:123], a[16:19]\n\t" \
  "v_cvt_pk_bf16_f32 v113, v50, v51\n\t" \
  "v_mfma_f32_16x16x32_bf16 a[48:51], v[192:195], v[124:127], a[48:51]\n\t" \
  "v_exp_f32 v52, v52\n\t" \
  "v_mfma_f32_16x16x32_bf16 a[80:83], v[192:195], v[128:131], a[80:83]\n\t" \
  "v_exp_f32 v53, v53\n\t" \
  "v_mfma_f32_16x16x32_bf16 a[112:115], v[192:195], v[132:135], a[112:115]\n\t" \
  "ds_read_b64_tr_b16 v[192:193], v222 offset:0\n\t" \
  "ds_read_b64_tr_b16 v[194:195], v222 offset:4096\n\t" \
  "s_waitcnt lgkmcnt(9)\n\t" \
  "v_mfma_f32_16x16x32_bf16 v[68:71], v[180:183], a[184:187], v[68:71]\n\t" \
  "s_mov_b32 s61, s72\n\t" \
  "v_mfma_f32_16x16x32_bf16 v[76:79], v[180:183], a[200:203], v[76:79]\n\t" \
  "s_mov_b32 s72, s73\n\t" \
  "v_mfma_f32_16x16x32_bf16 v[84:87], v[180:183], a[216:219], v[84:87]\n\t" \
  "s_mov_b32 s73, s74\n\t" \
  "v_mfma_f32_16x16x32_bf16 v[92:95], v[180:183], a[232:235], v[92:95]\n\t" \
  "ds_read_b128 v[180:183], v212 offset:512\n\t" \
  "v_mfma_f32_16x16x32_bf16 a[20:23], v[196:199], v[120:123], a[20:23]\n\t" \
  "s_mov_b32 s74, s75\n\t" \
  "v_mfma_f32_16x16x32_bf16 a[52:55], v[196:199], v[124:127], a[52:55]\n\t" \
  "s_add_u32 s80, s80, 16384\n\t" \
  "v_mfma_f32_16x16x32_bf16 a[84:87], v[196:199], v[128:131], a[84:87]\n\t" \
  "v_exp_f32 v54, v54\n\t" \
  "v_mfma_f32_16x16x32_bf16 a[116:119], v[196:199], v[132:135], a[116:119]\n\t" \
  "s_mov_b32 s75, s61\n\t" \
  "ds_read_b64_tr_b16 v[196:197], v223 offset:0\n\t" \
  "s_waitcnt lgkmcnt(10)\n\t" \
  "v_mfma_f32_16x16x32_bf16 v[68:71], v[184:187], a[188:191], v[68:71]\n\t" \
  "s_mov_b32 s61, s76\n\t" \
  "ds_read_b64_tr_b16 v[198:199], v223 offset:4096\n\t" \
  "v_mfma_f32_16x16x32_bf16 v[76:79], v[184:187], a[204:207], v[76:79]\n\t" \
  "s_mov_b32 s76, s77\n\t" \
  "v_mfma_f32_16x16x32_bf16 v[84:87], v[184:187], a[220:223], v[84:87]\n\t" \
  "v_exp_f32 v55, v55\n\t" \
  "v_mfma_f32_16x16x32_bf16 v[92:95], v[184:187], a[236:239], v[92:95]\n\t" \
  "s_mov_b32 s77, s78\n\t" \
  "ds_read_b128 v[184:187], v212 offset:1024\n\t" \
  "s_waitcnt lgkmcnt(10)\n\t" \
  "v_mfma_f32_16x16x32_bf16 a[24:27], v[200:203], v[120:123], a[24:27]\n\t" \
  "s_mov_b32 s78, s79\n\t" \
  "v_cvt_pk_bf16_f32 v114, v52, v53\n\t" \
  "v_mfma_f32_16x16x32_bf16 a[56:59], v[200:203], v[124:127], a[56:59]\n\t" \
  "v_cvt_pk_bf16_f32 v115, v54, v55\n\t" \
  "v_mfma_f32_16x16x32_bf16 a[88:91], v[200:203], v[128:131], a[88:91]\n\t" \
  "s_add_u32 s81, s81, 16384\n\t" \
  "v_mfma_f32_16x16x32_bf16 a[120:123], v[200:203], v[132:135], a[120:123]\n\t" \
  "s_mov_b32 s79, s61\n\t" \
  "ds_read_b64_tr_b16 v[200:201], v222 offset:512\n\t" \
  "s_waitcnt lgkmcnt(10)\n\t" \
  "v_mfma_f32_16x16x32_bf16 v[68:71], v[188:191], a[192:195], v[68:71]\n\t" \
  "s_sub_u32 s60, s60, 1\n\t" \
  "ds_read_b64_tr_b16 v[202:203], v222 offset:4608\n\t" \
  "v_mfma_f32_16x16x32_bf16 v[76:79], v[188:191], a[208:211], v[76:79]\n\t" \
  "v_exp_f32 v56, v56\n\t" \
  "v_mfma_f32_16x16x32_bf16 v[84:87], v[188:191], a[224:227], v[84:87]\n\t" \
  "v_exp_f32 v57, v57\n\t" \
  "v_mfma_f32_16x16x32_bf16 v[92:95], v[188:191], a[240:243], v[92:95]\n\t" \
  "ds_read_b128 v[188:191], v212 offset:1536\n\t" \
  "s_waitcnt lgkmcnt(10)\n\t" \
  "v_mfma_f32_16x16x32_bf16 a[28:31], v[204:207], v[120:123], a[28:31]\n\t" \
  "v_exp_f32 v58, v58\n\t" \
  "v_mfma_f32_16x16x32_bf16 a[60:63], v[204:207], v[124:127], a[60:63]\n\t" \
  "v_exp_f32 v59, v59\n\t" \
  "v_mfma_f32_16x16x32_bf16 a[92:95], v[204:207], v[128:131], a[92:95]\n\t" \
  "v_cvt_pk_bf16_f32 v116, v56, v57\n\t" \
  "v_cvt_pk_bf16_f32 v117, v58, v59\n\t" \
  "v_mfma_f32_16x16x32_bf16 a[124:127], v[204:207], v[132:135], a[124:127]\n\t" \
  "ds_read_b64_tr_b16 v[204:205], v223 offset:512\n\t" \
  "ds_read_b64_tr_b16 v[206:207], v223 offset:4608\n\t" \
  "v_exp_f32 v60, v60\n\t" \
  "v_exp_f32 v61, v61\n\t" \
  "v_exp_f32 v62, v62\n\t" \
  "v_exp_f32 v63, v63\n\t" \
  "v_cvt_pk_bf16_f32 v118, v60, v61\n\t" \
  "v_cvt_pk_bf16_f32 v119, v62, v63\n\t" \
  "s_waitcnt vmcnt(11)\n\t" \
  "s_cmp_eq_u32 s60, 14\n\t" \
  "s_cbranch_scc1 ALOOP_%=\n\t" \
  "s_waitcnt vmcnt(8)\n\t" \
  "s_cmp_lg_u32 s60, 0\n\t" \
  "s_cbranch_scc1 ALOOP_%=\n\t" \
  "s_waitcnt vmcnt(0)\n\t" \
  "s_barrier\n\t" \
  "s_mov_b64 s[40:41], %21\n\t" \
  "s_mov_b64 s[64:65], %25\n\t" \
  "s_mul_i32 s61, s65, 288\n\t" \
  "s_sub_u32 s42, 4718592, s61\n\t" \
  "s_mov_b32 s43, 0x20000\n\t" \
  "s_lshr_b32 s73, s90, 10\n\t" \
  "s_lshr_b32 s66, s73, 1\n\t" \
  "s_lshl_b32 s61, s66, 2\n\t" \
  "s_lshl_b32 s62, s85, 1\n\t" \
  "s_add_u32 s61, s61, s62\n\t" \
  "s_mul_i32 s67, s61, 9216\n\t" \
  "s_mov_b32 s72, s65\n\t" \
  "s_lshl_b32 s61, s85, 11\n\t" \
  "s_sub_u32 s75, s84, s61\n\t" \
  "s_mov_b32 s74, 0\n\t" \
  "s_add_u32 s72, s72, 0\n\t" \
  "s_and_b32 s72, s72, 3\n\t" \
  "s_lshl_b32 s61, s64, 2\n\t" \
  "s_add_u32 s61, s61, s72\n\t" \
  "s_lshl_b32 s61, s61, 1\n\t" \
  "s_add_u32 s61, s61, s66\n\t" \
  "s_mul_i32 s68, s61, 147456\n\t" \
  "s_mul_i32 s61, s85, 73728\n\t" \
  "s_add_u32 s68, s68, s61\n\t" \
  "s_sub_i32 s69, s72, s65\n\t" \
  "s_lshl_b32 s69, s69, 13\n\t" \
  "s_waitcnt lgkmcnt(9)\n\t" \
  "v_mfma_f32_16x16x32_bf16 a[0:3], v[192:195], v[104:107], a[0:3]\n\t" \
  "v_add_u32 v213, s72, v217\n\t" \
  "v_add_u32 v224, s76, v228\n\t" \
  "v_mfma_f32_16x16x32_bf16 a[32:35], v[192:195], v[108:111], a[32:35]\n\t" \
  "v_add_u32 v225, s76, v229\n\t" \
  "v_mfma_f32_16x16x32_bf16 a[64:67], v[192:195], v[112:115], a[64:67]\n\t" \
  "s_add_u32 s62, s67, 0\n\t" \
  "v_mfma_f32_16x16x32_bf16 a[96:99], v[192:195], v[116:119], a[96:99]\n\t" \
  "v_exp_f32 v64, v64\n\t" \
  "v_mfma_f32_16x16x32_bf16 a[128:131], v[192:195], v[136:139], a[128:131]\n\t" \
  "ds_read_b64_tr_b16 v[192:193], v222 offset:1024\n\t" \
  "ds_read_b64_tr_b16 v[194:195], v222 offset:5120\n\t" \
  "s_waitcnt lgkmcnt(8)\n\t" \
  "v_mfma_f32_16x16x32_bf16 a[4:7], v[196:199], v[104:107], a[4:7]\n\t" \
  "v_exp_f32 v65, v65\n\t" \
  "v_mfma_f32_16x16x32_bf16 a[36:39], v[196:199], v[108:111], a[36:39]\n\t" \
  "s_add_u32 m0, s62, 0\n\t" \
  "v_mfma_f32_16x16x32_bf16 a[68:71], v[196:199], v[112:115], a[68:71]\n\t" \
  "v_exp_f32 v66, v66\n\t" \
  "v_mfma_f32_16x16x32_bf16 a[100:103], v[196:199], v[116:119], a[100:103]\n\t" \
  "v_exp_f32 v67, v67\n\t" \
  "v_mfma_f32_16x16x32_bf16 a[132:135], v[196:199], v[136:139], a[132:135]\n\t" \
  "ds_read_b64_tr_b16 v[196:197], v223 offset:1024\n\t" \
  "ds_read_b64_tr_b16 v[198:199], v223 offset:5120\n\t" \
  "s_waitcnt lgkmcnt(7)\n\t" \
  "v_mfma_f32_16x16x32_bf16 a[8:11], v[200:203], v[104:107], a[8:11]\n\t" \
  "buffer_load_dwordx4 v232, s[40:43], s68 offen lds\n\t" \
  "v_cvt_pk_bf16_f32 v120, v64, v65\n\t" \
  "v_mfma_f32_16x16x32_bf16 a[40:43], v[200:203], v[108:111], a[40:43]\n\t" \
  "v_cvt_pk_bf16_f32 v121, v66, v67\n\t" \
  "v_mfma_f32_16x16x32_bf16 a[72:75], v[200:203], v[112:115], a[72:75]\n\t" \
  "v_exp_f32 v68, v68\n\t" \
  "v_mfma_f32_16x16x32_bf16 a[104:107], v[200:203], v[116:119], a[104:107]\n\t" \
  "v_exp_f32 v69, v69\n\t" \
  "v_mfma_f32_16x16x32_bf16 a[136:139], v[200:203], v[136:139], a[136:139]\n\t" \
  "s_add_u32 m0, s62, 1024\n\t" \
  "ds_read_b64_tr_b16 v[200:201], v222 offset:1536\n\t" \
  "s_waitcnt lgkmcnt(5)\n\t" \
  "v_mfma_f32_16x16x32_bf16 a[12:15], v[204:207], v[104:107], a[12:15]\n\t" \
  "ds_read_b64_tr_b16 v[202:203], v222 offset:5632\n\t" \
  "v_mfma_f32_16x16x32_bf16 a[44:47], v[204:207], v[108:111], a[44:47]\n\t" \
  "v_exp_f32 v70, v70\n\t" \
  "v_mfma_f32_16x16x32_bf16 a[76:79], v[204:207], v[112:115], a[76:79]\n\t" \
  "v_exp_f32 v71, v71\n\t" \
  "v_mfma_f32_16x16x32_bf16 a[108:111], v[204:207], v[116:119], a[108:111]\n\t" \
  "buffer_load_dwordx4 v233, s[40:43], s68 offen lds\n\t" \
  "v_cvt_pk_bf16_f32 v122, v68, v69\n\t" \
  "v_mfma_f32_16x16x32_bf16 a[140:143], v[204:207], v[136:139], a[140:143]\n\t" \
  "ds_read_b64_tr_b16 v[204:205], v223 offset:1536\n\t" \
  "ds_read_b64_tr_b16 v[206:207], v223 offset:5632\n\t" \
  "s_waitcnt lgkmcnt(6)\n\t" \
  "v_mfma_f32_16x16x32_bf16 a[16:19], v[192:195], v[104:107], a[16:19]\n\t" \
  "v_cvt_pk_bf16_f32 v123, v70, v71\n\t" \
  "v_mfma_f32_16x16x32_bf16 a[48:51], v[192:195], v[108:111], a[48:51]\n\t" \
  "v_exp_f32 v72, v72\n\t" \
  "v_mfma_f32_16x16x32_bf16 a[80:83], v[192:195], v[112:115], a[80:83]\n\t" \
  "s_add_u32 m0, s62, 2048\n\t" \
  "v_mfma_f32_16x16x32_bf16 a[112:115], v[192:195], v[116:119], a[112:115]\n\t" \
  "v_exp_f32 v73, v73\n\t" \
  "v_mfma_f32_16x16x32_bf16 a[144:147], v[192:195], v[136:139], a[144:147]\n\t" \
  "ds_read_b64_tr_b16 v[192:193], v224 offset:0\n\t" \
  "ds_read_b64_tr_b16 v[194:195], v224 offset:4096\n\t" \
  "s_waitcnt lgkmcnt(6)\n\t" \
  "v_mfma_f32_16x16x32_bf16 a[20:23], v[196:199], v[104:107], a[20:23]\n\t" \
  "v_exp_f32 v74, v74\n\t" \
  "v_mfma_f32_16x16x32_bf16 a[52:55], v[196:199], v[108:111], a[52:55]\n\t" \
  "buffer_load_dwordx4 v234, s[40:43], s68 offen lds\n\t" \
  "v_mfma_f32_16x16x32_bf16 a[84:87], v[196:199], v[112:115], a[84:87]\n\t" \
  "v_exp_f32 v75, v75\n\t" \
  "v_mfma_f32_16x16x32_bf16 a[116:119], v[196:199], v[116:119], a[116:119]\n\t" \
  "v_cvt_pk_bf16_f32 v124, v72, v73\n\t" \
  "v_cvt_pk_bf16_f32 v125, v74, v75\n\t" \
  "v_mfma_f32_16x16x32_bf16 a[148:151], v[196:199], v[136:139], a[148:151]\n\t" \
  "ds_read_b64_tr_b16 v[196:197], v225 offset:0\n\t" \
  "ds_read_b64_tr_b16 v[198:199], v225 offset:4096\n\t" \
  "s_waitcnt lgkmcnt(6)\n\t" \
  "v_mfma_f32_16x16x32_bf16 a[24:27], v[200:203], v[104:107], a[24:27]\n\t" \
  "s_add_u32 m0, s62, 3072\n\t" \
  "v_mfma_f32_16x16x32_bf16 a[56:59], v[200:203], v[108:111], a[56:59]\n\t" \
  "v_exp_f32 v76, v76\n\t" \
  "v_mfma_f32_16x16x32_bf16 a[88:91], v[200:203], v[112:115], a[88:91]\n\t" \
  "v_exp_f32 v77, v77\n\t" \
  "v_mfma_f32_16x16x32_bf16 a[120:123], v[200:203], v[116:119], a[120:123]\n\t" \
  "v_exp_f32 v78, v78\n\t" \
  "v_mfma_f32_16x16x32_bf16 a[152:155], v[200:203], v[136:139], a[152:155]\n\t" \
  "buffer_load_dwordx4 v235, s[40:43], s68 offen lds\n\t" \
  "ds_read_b64_tr_b16 v[200:201], v224 offset:512\n\t" \
  "s_waitcnt lgkmcnt(5)\n\t" \
  "v_mfma_f32_16x16x32_bf16 a[28:31], v[204:207], v[104:107], a[28:31]\n\t" \
  "ds_read_b64_tr_b16 v[202:203], v224 offset:4608\n\t" \
  "v_mfma_f32_16x16x32_bf16 a[60:63], v[204:207], v[108:111], a[60:63]\n\t" \
  "v_exp_f32 v79, v79\n\t" \
  "v_mfma_f32_16x16x32_bf16 a[92:95], v[204:207], v[112:115], a[92:95]\n\t" \
  "v_cvt_pk_bf16_f32 v126, v76, v77\n\t" \
  "v_cvt_pk_bf16_f32 v127, v78, v79\n\t" \
  "v_mfma_f32_16x16x32_bf16 a[124:127], v[204:207], v[116:119], a[124:127]\n\t" \
  "s_add_u32 m0, s62, 4096\n\t" \
  "v_mfma_f32_16x16x32_bf16 a[156:159], v[204:207], v[136:139], a[156:159]\n\t" \
  "ds_read_b64_tr_b16 v[204:205], v225 offset:512\n\t" \
  "ds_read_b64_tr_b16 v[206:207], v225 offset:4608\n\t" \
  "v_mfma_f32_16x16x32_bf16 a[160:163], v[208:211], v[104:107], a[160:163]\n\t" \
  "v_exp_f32 v80, v80\n\t" \
  "v_mfma_f32_16x16x32_bf16 a[164:167], v[208:211], v[108:111], a[164:167]\n\t" \
  "v_exp_f32 v81, v81\n\t" \
  "v_mfma_f32_16x16x32_bf16 a[168:171], v[208:211], v[112:115], a[168:171]\n\t" \
  "buffer_load_dwordx4 v236, s[40:43], s68 offen lds\n\t" \
  "v_mfma_f32_16x16x32_bf16 a[172:175], v[208:211], v[116:119], a[172:175]\n\t" \
  "v_exp_f32 v82, v82\n\t" \
  "v_mfma_f32_16x16x32_bf16 a[176:179], v[208:211], v[136:139], a[176:179]\n\t" \
  "v_exp_f32 v83, v83\n\t" \
  "s_add_u32 m0, s62, 5120\n\t" \
  "s_nop 0\n\t" \
  "buffer_load_dwordx4 v237, s[40:43], s68 offen lds\n\t" \
  "s_add_u32 m0, s62, 6144\n\t" \
  "s_nop 0\n\t" \
  "buffer_load_dwordx4 v238, s[40:43], s68 offen lds\n\t" \
  "s_add_u32 m0, s62, 7168\n\t" \
  "s_nop 0\n\t" \
  "buffer_load_dwordx4 v239, s[40:43], s68 offen lds\n\t" \
  "s_add_u32 m0, s62, 8192\n\t" \
  "s_nop 0\n\t" \
  "buffer_load_dwordx4 v240, s[40:43], s68 offen lds\n\t" \
  "v_cvt_pk_bf16_f32 v128, v80, v81\n\t" \
  "v_cvt_pk_bf16_f32 v129, v82, v83\n\t" \
  "v_exp_f32 v84, v84\n\t" \
  "v_exp_f32 v85, v85\n\t" \
  "v_exp_f32 v86, v86\n\t" \
  "v_exp_f32 v87, v87\n\t" \
  "v_cvt_pk_bf16_f32 v130, v84, v85\n\t" \
  "v_cvt_pk_bf16_f32 v131, v86, v87\n\t" \
  "v_exp_f32 v88, v88\n\t" \
  "v_exp_f32 v89, v89\n\t" \
  "v_exp_f32 v90, v90\n\t" \
  "v_exp_f32 v91, v91\n\t" \
  "v_cvt_pk_bf16_f32 v132, v88, v89\n\t" \
  "v_cvt_pk_bf16_f32 v133, v90, v91\n\t" \
  "v_exp_f32 v92, v92\n\t" \
  "v_exp_f32 v93, v93\n\t" \
  "v_exp_f32 v94, v94\n\t" \
  "v_exp_f32 v95, v95\n\t" \
  "v_cvt_pk_bf16_f32 v134, v92, v93\n\t" \
  "v_cvt_pk_bf16_f32 v135, v94, v95\n\t" \
  "v_mfma_f32_16x16x32_bf16 a[160:163], v[208:211], v[120:123], a[160:163]\n\t" \
  "v_mfma_f32_16x16x32_bf16 a[164:167], v[208:211], v[124:127], a[164:167]\n\t" \
  "s_add_u32 s61, s68, 36864\n\t" \
  "v_mfma_f32_16x16x32_bf16 a[168:171], v[208:211], v[128:131], a[168:171]\n\t" \
  "v_mfma_f32_16x16x32_bf16 a[172:175], v[208:211], v[132:135], a[172:175]\n\t" \
  "s_add_u32 m0, s62, 9216\n\t" \
  "s_waitcnt lgkmcnt(4)\n\t" \
  "v_mfma_f32_16x16x32_bf16 a[0:3], v[192:195], v[120:123], a[0:3]\n\t" \
  "v_mfma_f32_16x16x32_bf16 a[32:35], v[192:195], v[124:127], a[32:35]\n\t" \
  "buffer_load_dwordx4 v232, s[40:43], s61 offen lds\n\t" \
  "v_mfma_f32_16x16x32_bf16 a[64:67], v[192:195], v[128:131], a[64:67]\n\t" \
  "v_mfma_f32_16x16x32_bf16 a[96:99], v[192:195], v[132:135], a[96:99]\n\t" \
  "s_add_u32 m0, s62, 10240\n\t" \
  "ds_read_b64_tr_b16 v[192:193], v224 offset:1024\n\t" \
  "v_mfma_f32_16x16x32_bf16 a[4:7], v[196:199], v[120:123], a[4:7]\n\t" \
  "ds_read_b64_tr_b16 v[194:195], v224 offset:5120\n\t" \
  "v_mfma_f32_16x16x32_bf16 a[36:39], v[196:199], v[124:127], a[36:39]\n\t" \
  "buffer_load_dwordx4 v233, s[40:43], s61 offen lds\n\t" \
  "v_mfma_f32_16x16x32_bf16 a[68:71], v[196:199], v[128:131], a[68:71]\n\t" \
  "v_mfma_f32_16x16x32_bf16 a[100:103], v[196:199], v[132:135], a[100:103]\n\t" \
  "s_add_u32 m0, s62, 11264\n\t" \
  "ds_read_b64_tr_b16 v[196:197], v225 offset:1024\n\t" \
  "s_waitcnt lgkmcnt(3)\n\t" \
  "v_mfma_f32_16x16x32_bf16 a[8:11], v[200:203], v[120:123], a[8:11]\n\t" \
  "ds_read_b64_tr_b16 v[198:199], v225 offset:5120\n\t" \
  "v_mfma_f32_16x16x32_bf16 a[40:43], v[200:203], v[124:127], a[40:43]\n\t" \
  "buffer_load_dwordx4 v234, s[40:43], s61 offen lds\n\t" \
  "v_mfma_f32_16x16x32_bf16 a[72:75], v[200:203], v[128:131], a[72:75]\n\t" \
  "v_mfma_f32_16x16x32_bf16 a[104:107], v[200:203], v[132:135], a[104:107]\n\t" \
  "s_add_u32 m0, s62, 12288\n\t" \
  "ds_read_b64_tr_b16 v[200:201], v224 offset:1536\n\t" \
  "v_mfma_f32_16x16x32_bf16 a[12:15], v[204:207], v[120:123], a[12:15]\n\t" \
  "ds_read_b64_tr_b16 v[202:203], v224 offset:5632\n\t" \
  "v_mfma_f32_16x16x32_bf16 a[44:47], v[204:207], v[124:127], a[44:47]\n\t" \
  "buffer_load_dwordx4 v235, s[40:43], s61 offen lds\n\t" \
  "v_mfma_f32_16x16x32_bf16 a[76:79], v[204:207], v[128:131], a[76:79]\n\t" \
  "v_mfma_f32_16x16x32_bf16 a[108:111], v[204:207], v[132:135], a[108:111]\n\t" \
  "s_add_u32 m0, s62, 13312\n\t" \
  "ds_read_b64_tr_b16 v[204:205], v225 offset:1536\n\t" \
  "s_waitcnt lgkmcnt(5)\n\t" \
  "v_mfma_f32_16x16x32_bf16 a[16:19], v[192:195], v[120:123], a[16:19]\n\t" \
  "ds_read_b64_tr_b16 v[206:207], v225 offset:5632\n\t" \
  "v_mfma_f32_16x16x32_bf16 a[48:51], v[192:195], v[124:127], a[48:51]\n\t" \
  "buffer_load_dwordx4 v236, s[40:43], s61 offen lds\n\t" \
  "v_mfma_f32_16x16x32_bf16 a[80:83], v[192:195], v[128:131], a[80:83]\n\t" \
  "v_mfma_f32_16x16x32_bf16 a[112:115], v[192:195], v[132:135], a[112:115]\n\t" \
  "s_add_u32 m0, s62, 14336\n\t" \
  "s_waitcnt lgkmcnt(4)\n\t" \
  "v_mfma_f32_16x16x32_bf16 a[20:23], v[196:199], v[120:123], a[20:23]\n\t" \
  "v_mfma_f32_16x16x32_bf16 a[52:55], v[196:199], v[124:127], a[52:55]\n\t" \
  "buffer_load_dwordx4 v237, s[40:43], s61 offen lds\n\t" \
  "v_mfma_f32_16x16x32_bf16 a[84:87], v[196:199], v[128:131], a[84:87]\n\t" \
  "v_mfma_f32_16x16x32_bf16 a[116:119], v[196:199], v[132:135], a[116:119]\n\t" \
  "s_add_u32 m0, s62, 15360\n\t" \
  "s_waitcnt lgkmcnt(2)\n\t" \
  "v_mfma_f32_16x16x32_bf16 a[24:27], v[200:203], v[120:123], a[24:27]\n\t" \
  "v_mfma_f32_16x16x32_bf16 a[56:59], v[200:203], v[124:127], a[56:59]\n\t" \
  "buffer_load_dwordx4 v238, s[40:43], s61 offen lds\n\t" \
  "v_mfma_f32_16x16x32_bf16 a[88:91], v[200:203], v[128:131], a[88:91]\n\t" \
  "v_mfma_f32_16x16x32_bf16 a[120:123], v[200:203], v[132:135], a[120:123]\n\t" \
  "s_add_u32 m0, s62, 16384\n\t" \
  "s_waitcnt lgkmcnt(0)\n\t" \
  "v_mfma_f32_16x16x32_bf16 a[28:31], v[204:207], v[120:123], a[28:31]\n\t" \
  "v_mfma_f32_16x16x32_bf16 a[60:63], v[204:207], v[124:127], a[60:63]\n\t" \
  "buffer_load_dwordx4 v239, s[40:43], s61 offen lds\n\t" \
  "v_mfma_f32_16x16x32_bf16 a[92:95], v[204:207], v[128:131], a[92:95]\n\t" \
  "v_mfma_f32_16x16x32_bf16 a[124:127], v[204:207], v[132:135], a[124:127]\n\t" \
  "s_add_u32 m0, s62, 17408\n\t" \
  "s_nop 0\n\t" \
  "buffer_load_dwordx4 v240, s[40:43], s61 offen lds\n\t" \
  "v_mov_b32 v247, s89\n\t" \
  "s_mov_b64 s[86:87], 0\n\t" \
  "v_accvgpr_read_b32 v120, a128\n\t" \
  "v_accvgpr_read_b32 v121, a129\n\t" \
  "v_accvgpr_read_b32 v122, a130\n\t" \
  "v_accvgpr_read_b32 v123, a131\n\t" \
  "v_accvgpr_read_b32 v124, a132\n\t" \
  "v_accvgpr_read_b32 v125, a133\n\t" \
  "v_accvgpr_read_b32 v126, a134\n\t" \
  "v_accvgpr_read_b32 v127, a135\n\t" \
  "v_accvgpr_read_b32 v128, a136\n\t" \
  "v_accvgpr_read_b32 v129, a137\n\t" \
  "v_accvgpr_read_b32 v130, a138\n\t" \
  "v_accvgpr_read_b32 v131, a139\n\t" \
  "v_accvgpr_read_b32 v132, a140\n\t" \
  "v_accvgpr_read_b32 v133, a141\n\t" \
  "v_accvgpr_read_b32 v134, a142\n\t" \
  "v_accvgpr_read_b32 v135, a143\n\t" \
  "v_accvgpr_read_b32 v136, a144\n\t" \
  "v_accvgpr_read_b32 v137, a145\n\t" \
  "v_accvgpr_read_b32 v138, a146\n\t" \
  "v_accvgpr_read_b32 v139, a147\n\t" \
  "v_accvgpr_read_b32 v140, a148\n\t" \
  "v_accvgpr_read_b32 v141, a149\n\t" \
  "v_accvgpr_read_b32 v142, a150\n\t" \
  "v_accvgpr_read_b32 v143, a151\n\t" \
  "v_accvgpr_read_b32 v144, a152\n\t" \
  "v_accvgpr_read_b32 v145, a153\n\t" \
  "v_accvgpr_read_b32 v146, a154\n\t" \
  "v_accvgpr_read_b32 v147, a155\n\t" \
  "v_accvgpr_read_b32 v148, a156\n\t" \
  "v_accvgpr_read_b32 v149, a157\n\t" \
  "v_accvgpr_read_b32 v150, a158\n\t" \
  "v_accvgpr_read_b32 v151, a159\n\t" \
  "v_accvgpr_read_b32 v248, a176\n\t" \
  "ds_write_b128 v245, v[120:123] offset:0\n\t" \
  "ds_write_b128 v245, v[124:127] offset:1024\n\t" \
  "ds_write_b128 v245, v[128:131] offset:2048\n\t" \
  "ds_write_b128 v245, v[132:135] offset:3072\n\t" \
  "ds_write_b128 v245, v[136:139] offset:4096\n\t" \
  "ds_write_b128 v245, v[140:143] offset:5120\n\t" \
  "ds_write_b128 v245, v[144:147] offset:6144\n\t" \
  "ds_write_b128 v245, v[148:151] offset:7168\n\t" \
  "v_accvgpr_read_b32 v152, a160\n\t" \
  "v_cmp_nle_f32 vcc, v152, v247\n\t" \
  "s_or_b64 s[86:87], s[86:87], vcc\n\t" \
  "v_accvgpr_read_b32 v153, a164\n\t" \
  "v_cmp_nle_f32 vcc, v153, v247\n\t" \
  "s_or_b64 s[86:87], s[86:87], vcc\n\t" \
  "v_accvgpr_read_b32 v154, a168\n\t" \
  "v_cmp_nle_f32 vcc, v154, v247\n\t" \
  "s_or_b64 s[86:87], s[86:87], vcc\n\t" \
  "v_accvgpr_read_b32 v155, a172\n\t" \
  "v_cmp_nle_f32 vcc, v155, v247\n\t" \
  "s_or_b64 s[86:87], s[86:87], vcc\n\t" \
  "v_cmp_nle_f32 vcc, v248, v247\n\t" \
  "s_or_b64 s[86:87], s[86:87], vcc\n\t" \
  "s_lshl_b32 s61, s73, 9\n\t" \
  "s_add_u32 s61, s61, 161792\n\t" \
  "v_lshrrev_b32 v249, 1, v244\n\t" \
  "v_add_u32 v249, s61, v249\n\t" \
  "v_mov_b32 v250, v156\n\t" \
  "v_mov_b32 v251, v248\n\t" \
  "ds_write_b64 v249, v[250:251] offset:0\n\t" \
  "s_cmp_lg_u64 s[86:87], 0\n\t" \
  "s_cselect_b32 s62, 1, 0\n\t" \
  "s_lshl_b32 s63, s73, 2\n\t" \
  "s_add_u32 s63, s63, 131072\n\t" \
  "v_mov_b32 v252, s63\n\t" \
  "v_mov_b32 v253, s62\n\t" \
  "ds_write_b32 v252, v253\n\t" \
  "v_rcp_f32 v152, v152\n\t" \
  "v_rcp_f32 v153, v153\n\t" \
  "v_rcp_f32 v154, v154\n\t" \
  "v_rcp_f32 v155, v155\n\t" \
  "s_waitcnt lgkmcnt(0)\n\t" \
  "s_barrier\n\t" \
  "v_mov_b32 v252, 131072\n\t" \
  "ds_read_b128 v[192:195], v252 offset:0\n\t" \
  "s_xor_b32 s61, s73, 1\n\t" \
  "s_sub_i32 s61, s61, s73\n\t" \
  "s_lshl_b32 s61, s61, 9\n\t" \
  "v_add_u32 v249, s61, v249\n\t" \
  "ds_read_b64 v[254:255], v249 offset:0\n\t" \
  "s_lshl_b32 s61, s85, 12\n\t" \
  "v_add_u32 v252, s61, v245\n\t" \
  "v_add_u32 v253, s61, v246\n\t" \
  "ds_read_b128 v[156:159], v252 offset:0\n\t" \
  "ds_read_b128 v[160:163], v252 offset:1024\n\t" \
  "ds_read_b128 v[164:167], v252 offset:2048\n\t" \
  "ds_read_b128 v[168:171], v252 offset:3072\n\t" \
  "ds_read_b128 v[172:175], v253 offset:0\n\t" \
  "ds_read_b128 v[176:179], v253 offset:1024\n\t" \
  "ds_read_b128 v[180:183], v253 offset:2048\n\t" \
  "ds_read_b128 v[184:187], v253 offset:3072\n\t" \
  "s_waitcnt lgkmcnt(9)\n\t" \
  "v_or3_b32 v192, v192, v193, v194\n\t" \
  "v_or_b32 v192, v192, v195\n\t" \
  "s_nop 0\n\t" \
  "v_readfirstlane_b32 s61, v192\n\t" \
  "s_nop 3\n\t" \
  "s_cmp_lg_u32 s61, 0\n\t" \
  "s_cbranch_scc1 FALLBACK_%=\n\t" \
  "s_waitcnt lgkmcnt(8)\n\t" \
  "v_min_f32 v196, v250, v254\n\t" \
  "v_sub_f32 v197, v196, v250\n\t" \
  "v_sub_f32 v198, v196, v254\n\t" \
  "v_exp_f32 v197, v197\n\t" \
  "v_exp_f32 v198, v198\n\t" \
  "v_mul_f32 v196, v248, v197\n\t" \
  "v_fmac_f32 v196, v255, v198\n\t" \
  "v_rcp_f32 v196, v196\n\t" \
  "s_nop 0\n\t" \
  "v_mul_f32 v197, v197, v196\n\t" \
  "v_mul_f32 v198, v198, v196\n\t" \
  "s_waitcnt lgkmcnt(3)\n\t" \
  "v_mul_f32 v156, v156, v197\n\t" \
  "v_fmac_f32 v156, v172, v198\n\t" \
  "v_mul_f32 v157, v157, v197\n\t" \
  "v_fmac_f32 v157, v173, v198\n\t" \
  "v_mul_f32 v158, v158, v197\n\t" \
  "v_fmac_f32 v158, v174, v198\n\t" \
  "v_mul_f32 v159, v159, v197\n\t" \
  "v_fmac_f32 v159, v175, v198\n\t" \
  "v_mul_f32 v160, v160, v197\n\t" \
  "s_waitcnt lgkmcnt(1)\n\t" \
  "v_fmac_f32 v160, v176, v198\n\t" \
  "v_mul_f32 v161, v161, v197\n\t" \
  "v_fmac_f32 v161, v177, v198\n\t" \
  "v_mul_f32 v162, v162, v197\n\t" \
  "v_fmac_f32 v162, v178, v198\n\t" \
  "v_mul_f32 v163, v163, v197\n\t" \
  "v_fmac_f32 v163, v179, v198\n\t" \
  "v_mul_f32 v164, v164, v197\n\t" \
  "v_fmac_f32 v164, v180, v198\n\t" \
  "v_mul_f32 v165, v165, v197\n\t" \
  "v_fmac_f32 v165, v181, v198\n\t" \
  "v_mul_f32 v166, v166, v197\n\t" \
  "v_fmac_f32 v166, v182, v198\n\t" \
  "v_mul_f32 v167, v167, v197\n\t" \
  "v_fmac_f32 v167, v183, v198\n\t" \
  "v_mul_f32 v168, v168, v197\n\t" \
  "s_waitcnt lgkmcnt(0)\n\t" \
  "v_fmac_f32 v168, v184, v198\n\t" \
  "v_mul_f32 v169, v169, v197\n\t" \
  "v_fmac_f32 v169, v185, v198\n\t" \
  "v_mul_f32 v170, v170, v197\n\t" \
  "v_fmac_f32 v170, v186, v198\n\t" \
  "v_mul_f32 v171, v171, v197\n\t" \
  "v_fmac_f32 v171, v187, v198\n\t" \
  "v_cvt_pk_bf16_f32 v200, v156, v157\n\t" \
  "v_cvt_pk_bf16_f32 v201, v158, v159\n\t" \
  "v_cvt_pk_bf16_f32 v202, v160, v161\n\t" \
  "v_cvt_pk_bf16_f32 v203, v162, v163\n\t" \
  "buffer_store_dwordx4 v[200:203], v244, s[56:59], s84 offen sc1\n\t" \
  "ds_write_b128 v242, v[200:203] offset:0\n\t" \
  "v_cvt_pk_bf16_f32 v204, v164, v165\n\t" \
  "v_cvt_pk_bf16_f32 v205, v166, v167\n\t" \
  "v_cvt_pk_bf16_f32 v206, v168, v169\n\t" \
  "v_cvt_pk_bf16_f32 v207, v170, v171\n\t" \
  "buffer_store_dwordx4 v[204:207], v244, s[56:59], s84 offen offset:1024 sc1\n\t" \
  "ds_write_b128 v242, v[204:207] offset:1024\n\t" \
  "s_add_u32 s76, s83, 131072\n\t" \
  "s_add_u32 s77, s83, 262144\n\t" \
  "s_add_u32 s78, s83, 393216\n\t" \
  "v_accvgpr_read_b32 v112, a0\n\t" \
  "v_accvgpr_read_b32 v113, a1\n\t" \
  "v_accvgpr_read_b32 v114, a2\n\t" \
  "v_accvgpr_read_b32 v115, a3\n\t" \
  "v_accvgpr_read_b32 v116, a4\n\t" \
  "v_accvgpr_read_b32 v117, a5\n\t" \
  "v_accvgpr_read_b32 v118, a6\n\t" \
  "v_accvgpr_read_b32 v119, a7\n\t" \
  "v_pk_mul_f32 v[112:113], v[112:113], v[152:153] op_sel_hi:[1,0]\n\t" \
  "v_pk_mul_f32 v[114:115], v[114:115], v[152:153] op_sel_hi:[1,0]\n\t" \
  "v_pk_mul_f32 v[116:117], v[116:117], v[152:153] op_sel_hi:[1,0]\n\t" \
  "v_pk_mul_f32 v[118:119], v[118:119], v[152:153] op_sel_hi:[1,0]\n\t" \
  "v_cvt_pk_bf16_f32 v32, v112, v113\n\t" \
  "v_cvt_pk_bf16_f32 v33, v114, v115\n\t" \
  "v_cvt_pk_bf16_f32 v34, v116, v117\n\t" \
  "v_cvt_pk_bf16_f32 v35, v118, v119\n\t" \
  "buffer_store_dwordx4 v[32:35], v244, s[56:59], s83 offen sc1\n\t" \
  "v_accvgpr_read_b32 v188, a8\n\t" \
  "v_accvgpr_read_b32 v189, a9\n\t" \
  "v_accvgpr_read_b32 v190, a10\n\t" \
  "v_accvgpr_read_b32 v191, a11\n\t" \
  "v_accvgpr_read_b32 v192, a12\n\t" \
  "v_accvgpr_read_b32 v193, a13\n\t" \
  "v_accvgpr_read_b32 v194, a14\n\t" \
  "v_accvgpr_read_b32 v195, a15\n\t" \
  "v_pk_mul_f32 v[188:189], v[188:189], v[152:153] op_sel_hi:[1,0]\n\t" \
  "v_pk_mul_f32 v[190:191], v[190:191], v[152:153] op_sel_hi:[1,0]\n\t" \
  "v_pk_mul_f32 v[192:193], v[192:193], v[152:153] op_sel_hi:[1,0]\n\t" \
  "v_pk_mul_f32 v[194:195], v[194:195], v[152:153] op_sel_hi:[1,0]\n\t" \
  "v_cvt_pk_bf16_f32 v52, v188, v189\n\t" \
  "v_cvt_pk_bf16_f32 v53, v190, v191\n\t" \
  "v_cvt_pk_bf16_f32 v54, v192, v193\n\t" \
  "v_cvt_pk_bf16_f32 v55, v194, v195\n\t" \
  "buffer_store_dwordx4 v[52:55], v244, s[56:59], s83 offen offset:1024 sc1\n\t" \
  "v_accvgpr_read_b32 v112, a16\n\t" \
  "v_accvgpr_read_b32 v113, a17\n\t" \
  "v_accvgpr_read_b32 v114, a18\n\t" \
  "v_accvgpr_read_b32 v115, a19\n\t" \
  "v_accvgpr_read_b32 v116, a20\n\t" \
  "v_accvgpr_read_b32 v117, a21\n\t" \
  "v_accvgpr_read_b32 v118, a22\n\t" \
  "v_accvgpr_read_b32 v119, a23\n\t" \
  "v_pk_mul_f32 v[112:113], v[112:113], v[152:153] op_sel_hi:[1,0]\n\t" \
  "v_pk_mul_f32 v[114:115], v[114:115], v[152:153] op_sel_hi:[1,0]\n\t" \
  "v_pk_mul_f32 v[116:117], v[116:117], v[152:153] op_sel_hi:[1,0]\n\t" \
  "v_pk_mul_f32 v[118:119], v[118:119], v[152:153] op_sel_hi:[1,0]\n\t" \
  "v_cvt_pk_bf16_f32 v72, v112, v113\n\t" \
  "v_cvt_pk_bf16_f32 v73, v114, v115\n\t" \
  "v_cvt_pk_bf16_f32 v74, v116, v117\n\t" \
  "v_cvt_pk_bf16_f32 v75, v118, v119\n\t" \
  "v_accvgpr_read_b32 v188, a24\n\t" \
  "v_accvgpr_read_b32 v189, a25\n\t" \
  "v_accvgpr_read_b32 v190, a26\n\t" \
  "v_accvgpr_read_b32 v191, a27\n\t" \
  "v_accvgpr_read_b32 v192, a28\n\t" \
  "v_accvgpr_read_b32 v193, a29\n\t" \
  "v_accvgpr_read_b32 v194, a30\n\t" \
  "v_accvgpr_read_b32 v195, a31\n\t" \
  "v_pk_mul_f32 v[188:189], v[188:189], v[152:153] op_sel_hi:[1,0]\n\t" \
  "v_pk_mul_f32 v[190:191], v[190:191], v[152:153] op_sel_hi:[1,0]\n\t" \
  "v_pk_mul_f32 v[192:193], v[192:193], v[152:153] op_sel_hi:[1,0]\n\t" \
  "v_pk_mul_f32 v[194:195], v[194:195], v[152:153] op_sel_hi:[1,0]\n\t" \
  "v_cvt_pk_bf16_f32 v92, v188, v189\n\t" \
  "v_cvt_pk_bf16_f32 v93, v190, v191\n\t" \
  "v_cvt_pk_bf16_f32 v94, v192, v193\n\t" \
  "v_cvt_pk_bf16_f32 v95, v194, v195\n\t" \
  "buffer_store_dwordx4 v[72:75], v244, s[56:59], s83 offen offset:2048 sc1\n\t" \
  "v_accvgpr_read_b32 v112, a32\n\t" \
  "v_accvgpr_read_b32 v113, a33\n\t" \
  "v_accvgpr_read_b32 v114, a34\n\t" \
  "v_accvgpr_read_b32 v115, a35\n\t" \
  "v_accvgpr_read_b32 v116, a36\n\t" \
  "v_accvgpr_read_b32 v117, a37\n\t" \
  "v_accvgpr_read_b32 v118, a38\n\t" \
  "v_accvgpr_read_b32 v119, a39\n\t" \
  "v_pk_mul_f32 v[112:113], v[112:113], v[152:153] op_sel:[0,1] op_sel_hi:[1,1]\n\t" \
  "v_pk_mul_f32 v[114:115], v[114:115], v[152:153] op_sel:[0,1] op_sel_hi:[1,1]\n\t" \
  "v_pk_mul_f32 v[116:117], v[116:117], v[152:153] op_sel:[0,1] op_sel_hi:[1,1]\n\t" \
  "v_pk_mul_f32 v[118:119], v[118:119], v[152:153] op_sel:[0,1] op_sel_hi:[1,1]\n\t" \
  "v_cvt_pk_bf16_f32 v36, v112, v113\n\t" \
  "v_cvt_pk_bf16_f32 v37, v114, v115\n\t" \
  "v_cvt_pk_bf16_f32 v38, v116, v117\n\t" \
  "v_cvt_pk_bf16_f32 v39, v118, v119\n\t" \
  "buffer_store_dwordx4 v[92:95], v244, s[56:59], s83 offen offset:3072 sc1\n\t" \
  "v_accvgpr_read_b32 v188, a40\n\t" \
  "v_accvgpr_read_b32 v189, a41\n\t" \
  "v_accvgpr_read_b32 v190, a42\n\t" \
  "v_accvgpr_read_b32 v191, a43\n\t" \
  "v_accvgpr_read_b32 v192, a44\n\t" \
  "v_accvgpr_read_b32 v193, a45\n\t" \
  "v_accvgpr_read_b32 v194, a46\n\t" \
  "v_accvgpr_read_b32 v195, a47\n\t" \
  "v_pk_mul_f32 v[188:189], v[188:189], v[152:153] op_sel:[0,1] op_sel_hi:[1,1]\n\t" \
  "v_pk_mul_f32 v[190:191], v[190:191], v[152:153] op_sel:[0,1] op_sel_hi:[1,1]\n\t" \
  "v_pk_mul_f32 v[192:193], v[192:193], v[152:153] op_sel:[0,1] op_sel_hi:[1,1]\n\t" \
  "v_pk_mul_f32 v[194:195], v[194:195], v[152:153] op_sel:[0,1] op_sel_hi:[1,1]\n\t" \
  "v_cvt_pk_bf16_f32 v56, v188, v189\n\t" \
  "v_cvt_pk_bf16_f32 v57, v190, v191\n\t" \
  "v_cvt_pk_bf16_f32 v58, v192, v193\n\t" \
  "v_cvt_pk_bf16_f32 v59, v194, v195\n\t" \
  "v_accvgpr_read_b32 v112, a48\n\t" \
  "v_accvgpr_read_b32 v113, a49\n\t" \
  "v_accvgpr_read_b32 v114, a50\n\t" \
  "v_accvgpr_read_b32 v115, a51\n\t" \
  "v_accvgpr_read_b32 v116, a52\n\t" \
  "v_accvgpr_read_b32 v117, a53\n\t" \
  "v_accvgpr_read_b32 v118, a54\n\t" \
  "v_accvgpr_read_b32 v119, a55\n\t" \
  "v_pk_mul_f32 v[112:113], v[112:113], v[152:153] op_sel:[0,1] op_sel_hi:[1,1]\n\t" \
  "v_pk_mul_f32 v[114:115], v[114:115], v[152:153] op_sel:[0,1] op_sel_hi:[1,1]\n\t" \
  "v_pk_mul_f32 v[116:117], v[116:117], v[152:153] op_sel:[0,1] op_sel_hi:[1,1]\n\t" \
  "v_pk_mul_f32 v[118:119], v[118:119], v[152:153] op_sel:[0,1] op_sel_hi:[1,1]\n\t" \
  "v_cvt_pk_bf16_f32 v76, v112, v113\n\t" \
  "v_cvt_pk_bf16_f32 v77, v114, v115\n\t" \
  "v_cvt_pk_bf16_f32 v78, v116, v117\n\t" \
  "v_cvt_pk_bf16_f32 v79, v118, v119\n\t" \
  "buffer_store_dwordx4 v[36:39], v244, s[56:59], s76 offen sc1\n\t" \
  "v_accvgpr_read_b32 v188, a56\n\t" \
  "v_accvgpr_read_b32 v189, a57\n\t" \
  "v_accvgpr_read_b32 v190, a58\n\t" \
  "v_accvgpr_read_b32 v191, a59\n\t" \
  "v_accvgpr_read_b32 v192, a60\n\t" \
  "v_accvgpr_read_b32 v193, a61\n\t" \
  "v_accvgpr_read_b32 v194, a62\n\t" \
  "v_accvgpr_read_b32 v195, a63\n\t" \
  "v_pk_mul_f32 v[188:189], v[188:189], v[152:153] op_sel:[0,1] op_sel_hi:[1,1]\n\t" \
  "v_pk_mul_f32 v[190:191], v[190:191], v[152:153] op_sel:[0,1] op_sel_hi:[1,1]\n\t" \
  "v_pk_mul_f32 v[192:193], v[192:193], v[152:153] op_sel:[0,1] op_sel_hi:[1,1]\n\t" \
  "v_pk_mul_f32 v[194:195], v[194:195], v[152:153] op_sel:[0,1] op_sel_hi:[1,1]\n\t" \
  "v_cvt_pk_bf16_f32 v96, v188, v189\n\t" \
  "v_cvt_pk_bf16_f32 v97, v190, v191\n\t" \
  "v_cvt_pk_bf16_f32 v98, v192, v193\n\t" \
  "v_cvt_pk_bf16_f32 v99, v194, v195\n\t" \
  "buffer_store_dwordx4 v[56:59], v244, s[56:59], s76 offen offset:1024 sc1\n\t" \
  "v_accvgpr_read_b32 v112, a64\n\t" \
  "v_accvgpr_read_b32 v113, a65\n\t" \
  "v_accvgpr_read_b32 v114, a66\n\t" \
  "v_accvgpr_read_b32 v115, a67\n\t" \
  "v_accvgpr_read_b32 v116, a68\n\t" \
  "v_accvgpr_read_b32 v117, a69\n\t" \
  "v_accvgpr_read_b32 v118, a70\n\t" \
  "v_accvgpr_read_b32 v119, a71\n\t" \
  "v_pk_mul_f32 v[112:113], v[112:113], v[154:155] op_sel_hi:[1,0]\n\t" \
  "v_pk_mul_f32 v[114:115], v[114:115], v[154:155] op_sel_hi:[1,0]\n\t" \
  "v_pk_mul_f32 v[116:117], v[116:117], v[154:155] op_sel_hi:[1,0]\n\t" \
  "v_pk_mul_f32 v[118:119], v[118:119], v[154:155] op_sel_hi:[1,0]\n\t" \
  "v_cvt_pk_bf16_f32 v40, v112, v113\n\t" \
  "v_cvt_pk_bf16_f32 v41, v114, v115\n\t" \
  "v_cvt_pk_bf16_f32 v42, v116, v117\n\t" \
  "v_cvt_pk_bf16_f32 v43, v118, v119\n\t" \
  "v_accvgpr_read_b32 v188, a72\n\t" \
  "v_accvgpr_read_b32 v189, a73\n\t" \
  "v_accvgpr_read_b32 v190, a74\n\t" \
  "v_accvgpr_read_b32 v191, a75\n\t" \
  "v_accvgpr_read_b32 v192, a76\n\t" \
  "v_accvgpr_read_b32 v193, a77\n\t" \
  "v_accvgpr_read_b32 v194, a78\n\t" \
  "v_accvgpr_read_b32 v195, a79\n\t" \
  "v_pk_mul_f32 v[188:189], v[188:189], v[154:155] op_sel_hi:[1,0]\n\t" \
  "v_pk_mul_f32 v[190:191], v[190:191], v[154:155] op_sel_hi:[1,0]\n\t" \
  "v_pk_mul_f32 v[192:193], v[192:193], v[154:155] op_sel_hi:[1,0]\n\t" \
  "v_pk_mul_f32 v[194:195], v[194:195], v[154:155] op_sel_hi:[1,0]\n\t" \
  "v_cvt_pk_bf16_f32 v60, v188, v189\n\t" \
  "v_cvt_pk_bf16_f32 v61, v190, v191\n\t" \
  "v_cvt_pk_bf16_f32 v62, v192, v193\n\t" \
  "v_cvt_pk_bf16_f32 v63, v194, v195\n\t" \
  "buffer_store_dwordx4 v[76:79], v244, s[56:59], s76 offen offset:2048 sc1\n\t" \
  "v_accvgpr_read_b32 v112, a80\n\t" \
  "v_accvgpr_read_b32 v113, a81\n\t" \
  "v_accvgpr_read_b32 v114, a82\n\t" \
  "v_accvgpr_read_b32 v115, a83\n\t" \
  "v_accvgpr_read_b32 v116, a84\n\t" \
  "v_accvgpr_read_b32 v117, a85\n\t" \
  "v_accvgpr_read_b32 v118, a86\n\t" \
  "v_accvgpr_read_b32 v119, a87\n\t" \
  "v_pk_mul_f32 v[112:113], v[112:113], v[154:155] op_sel_hi:[1,0]\n\t" \
  "v_pk_mul_f32 v[114:115], v[114:115], v[154:155] op_sel_hi:[1,0]\n\t" \
  "v_pk_mul_f32 v[116:117], v[116:117], v[154:155] op_sel_hi:[1,0]\n\t" \
  "v_pk_mul_f32 v[118:119], v[118:119], v[154:155] op_sel_hi:[1,0]\n\t" \
  "v_cvt_pk_bf16_f32 v80, v112, v113\n\t" \
  "v_cvt_pk_bf16_f32 v81, v114, v115\n\t" \
  "v_cvt_pk_bf16_f32 v82, v116, v117\n\t" \
  "v_cvt_pk_bf16_f32 v83, v118, v119\n\t" \
  "buffer_store_dwordx4 v[96:99], v244, s[56:59], s76 offen offset:3072 sc1\n\t" \
  "v_accvgpr_read_b32 v188, a88\n\t" \
  "v_accvgpr_read_b32 v189, a89\n\t" \
  "v_accvgpr_read_b32 v190, a90\n\t" \
  "v_accvgpr_read_b32 v191, a91\n\t" \
  "v_accvgpr_read_b32 v192, a92\n\t" \
  "v_accvgpr_read_b32 v193, a93\n\t" \
  "v_accvgpr_read_b32 v194, a94\n\t" \
  "v_accvgpr_read_b32 v195, a95\n\t" \
  "v_pk_mul_f32 v[188:189], v[188:189], v[154:155] op_sel_hi:[1,0]\n\t" \
  "v_pk_mul_f32 v[190:191], v[190:191], v[154:155] op_sel_hi:[1,0]\n\t" \
  "v_pk_mul_f32 v[192:193], v[192:193], v[154:155] op_sel_hi:[1,0]\n\t" \
  "v_pk_mul_f32 v[194:195], v[194:195], v[154:155] op_sel_hi:[1,0]\n\t" \
  "v_cvt_pk_bf16_f32 v100, v188, v189\n\t" \
  "v_cvt_pk_bf16_f32 v101, v190, v191\n\t" \
  "v_cvt_pk_bf16_f32 v102, v192, v193\n\t" \
  "v_cvt_pk_bf16_f32 v103, v194, v195\n\t" \
  "v_accvgpr_read_b32 v112, a96\n\t" \
  "v_accvgpr_read_b32 v113, a97\n\t" \
  "v_accvgpr_read_b32 v114, a98\n\t" \
  "v_accvgpr_read_b32 v115, a99\n\t" \
  "v_accvgpr_read_b32 v116, a100\n\t" \
  "v_accvgpr_read_b32 v117, a101\n\t" \
  "v_accvgpr_read_b32 v118, a102\n\t" \
  "v_accvgpr_read_b32 v119, a103\n\t" \
  "v_pk_mul_f32 v[112:113], v[112:113], v[154:155] op_sel:[0,1] op_sel_hi:[1,1]\n\t" \
  "v_pk_mul_f32 v[114:115], v[114:115], v[154:155] op_sel:[0,1] op_sel_hi:[1,1]\n\t" \
  "v_pk_mul_f32 v[116:117], v[116:117], v[154:155] op_sel:[0,1] op_sel_hi:[1,1]\n\t" \
  "v_pk_mul_f32 v[118:119], v[118:119], v[154:155] op_sel:[0,1] op_sel_hi:[1,1]\n\t" \
  "v_cvt_pk_bf16_f32 v44, v112, v113\n\t" \
  "v_cvt_pk_bf16_f32 v45, v114, v115\n\t" \
  "v_cvt_pk_bf16_f32 v46, v116, v117\n\t" \
  "v_cvt_pk_bf16_f32 v47, v118, v119\n\t" \
  "buffer_store_dwordx4 v[40:43], v244, s[56:59], s77 offen sc1\n\t" \
  "v_accvgpr_read_b32 v188, a104\n\t" \
  "v_accvgpr_read_b32 v189, a105\n\t" \
  "v_accvgpr_read_b32 v190, a106\n\t" \
  "v_accvgpr_read_b32 v191, a107\n\t" \
  "v_accvgpr_read_b32 v192, a108\n\t" \
  "v_accvgpr_read_b32 v193, a109\n\t" \
  "v_accvgpr_read_b32 v194, a110\n\t" \
  "v_accvgpr_read_b32 v195, a111\n\t" \
  "v_pk_mul_f32 v[188:189], v[188:189], v[154:155] op_sel:[0,1] op_sel_hi:[1,1]\n\t" \
  "v_pk_mul_f32 v[190:191], v[190:191], v[154:155] op_sel:[0,1] op_sel_hi:[1,1]\n\t" \
  "v_pk_mul_f32 v[192:193], v[192:193], v[154:155] op_sel:[0,1] op_sel_hi:[1,1]\n\t" \
  "v_pk_mul_f32 v[194:195], v[194:195], v[154:155] op_sel:[0,1] op_sel_hi:[1,1]\n\t" \
  "v_cvt_pk_bf16_f32 v64, v188, v189\n\t" \
  "v_cvt_pk_bf16_f32 v65, v190, v191\n\t" \
  "v_cvt_pk_bf16_f32 v66, v192, v193\n\t" \
  "v_cvt_pk_bf16_f32 v67, v194, v195\n\t" \
  "buffer_store_dwordx4 v[60:63], v244, s[56:59], s77 offen offset:1024 sc1\n\t" \
  "v_accvgpr_read_b32 v112, a112\n\t" \
  "v_accvgpr_read_b32 v113, a113\n\t" \
  "v_accvgpr_read_b32 v114, a114\n\t" \
  "v_accvgpr_read_b32 v115, a115\n\t" \
  "v_accvgpr_read_b32 v116, a116\n\t" \
  "v_accvgpr_read_b32 v117, a117\n\t" \
  "v_accvgpr_read_b32 v118, a118\n\t" \
  "v_accvgpr_read_b32 v119, a119\n\t" \
  "v_pk_mul_f32 v[112:113], v[112:113], v[154:155] op_sel:[0,1] op_sel_hi:[1,1]\n\t" \
  "v_pk_mul_f32 v[114:115], v[114:115], v[154:155] op_sel:[0,1] op_sel_hi:[1,1]\n\t" \
  "v_pk_mul_f32 v[116:117], v[116:117], v[154:155] op_sel:[0,1] op_sel_hi:[1,1]\n\t" \
  "v_pk_mul_f32 v[118:119], v[118:119], v[154:155] op_sel:[0,1] op_sel_hi:[1,1]\n\t" \
  "v_cvt_pk_bf16_f32 v84, v112, v113\n\t" \
  "v_cvt_pk_bf16_f32 v85, v114, v115\n\t" \
  "v_cvt_pk_bf16_f32 v86, v116, v117\n\t" \
  "v_cvt_pk_bf16_f32 v87, v118, v119\n\t" \
  "v_accvgpr_read_b32 v188, a120\n\t" \
  "v_accvgpr_read_b32 v189, a121\n\t" \
  "v_accvgpr_read_b32 v190, a122\n\t" \
  "v_accvgpr_read_b32 v191, a123\n\t" \
  "v_accvgpr_read_b32 v192, a124\n\t" \
  "v_accvgpr_read_b32 v193, a125\n\t" \
  "v_accvgpr_read_b32 v194, a126\n\t" \
  "v_accvgpr_read_b32 v195, a127\n\t" \
  "v_pk_mul_f32 v[188:189], v[188:189], v[154:155] op_sel:[0,1] op_sel_hi:[1,1]\n\t" \
  "v_pk_mul_f32 v[190:191], v[190:191], v[154:155] op_sel:[0,1] op_sel_hi:[1,1]\n\t" \
  "v_pk_mul_f32 v[192:193], v[192:193], v[154:155] op_sel:[0,1] op_sel_hi:[1,1]\n\t" \
  "v_pk_mul_f32 v[194:195], v[194:195], v[154:155] op_sel:[0,1] op_sel_hi:[1,1]\n\t" \
  "v_cvt_pk_bf16_f32 v104, v188, v189\n\t" \
  "v_cvt_pk_bf16_f32 v105, v190, v191\n\t" \
  "v_cvt_pk_bf16_f32 v106, v192, v193\n\t" \
  "v_cvt_pk_bf16_f32 v107, v194, v195\n\t" \
  "buffer_store_dwordx4 v[80:83], v244, s[56:59], s77 offen offset:2048 sc1\n\t" \
  "v_mov_b64 v[210:211], %26\n\t" \
  "v_readfirstlane_b32 s52, v214\n\t" \
  "v_readfirstlane_b32 s53, v215\n\t" \
  "s_nop 3\n\t" \
  "s_add_u32 s52, s52, 1\n\t" \
  "s_addc_u32 s53, s53, 0\n\t" \
  "s_mov_b32 s74, 0\n\t" \
  "s_mov_b64 s[44:45], %22\n\t" \
  "s_mov_b32 s46, 0x2000000\n\t" \
  "s_mov_b32 s47, 0x20000\n\t" \
  "s_mov_b64 s[48:49], %23\n\t" \
  "s_mov_b32 s50, 2654208\n\t" \
  "s_mov_b32 s51, 0x20000\n\t" \
  "v_lshrrev_b32 v252, 4, v244\n\t" \
  "v_and_b32 v252, 3, v252\n\t" \
  "v_lshlrev_b32 v253, 10, v252\n\t" \
  "v_lshlrev_b32 v252, 8, v252\n\t" \
  "s_lshl_b32 s61, s64, 10\n\t" \
  "s_add_u32 s61, s61, 256\n\t" \
  "v_add_u32 v252, s61, v252\n\t" \
  "s_lshl_b32 s61, s65, 8\n\t" \
  "s_add_u32 s61, s61, 4352\n\t" \
  "v_add_u32 v253, s61, v253\n\t" \
  "s_lshl_b32 s79, s64, 2\n\t" \
  "s_add_u32 s79, s79, s65\n\t" \
  "s_lshl_b32 s79, s79, 8\n\t" \
  "s_add_u32 s79, s79, 256\n\t" \
  "s_waitcnt lgkmcnt(0)\n\t" \
  "s_waitcnt vmcnt(13)\n\t" \
  "s_barrier\n\t" \
  "ds_read_b128 v[48:51], v243 offset:0\n\t" \
  "ds_read_b128 v[68:71], v243 offset:1024\n\t" \
  "ds_read_b128 v[88:91], v243 offset:2048\n\t" \
  "ds_read_b128 v[108:111], v243 offset:3072\n\t" \
  "v_add_u32 v208, 0, v210\n\t" \
  "v_add_u32 v209, 0, v211\n\t" \
  "ds_read_b64_tr_b16 v[192:193], v208 offset:0\n\t" \
  "ds_read_b64_tr_b16 v[194:195], v208 offset:4608\n\t" \
  "ds_read_b64_tr_b16 v[196:197], v208 offset:32\n\t" \
  "ds_read_b64_tr_b16 v[198:199], v208 offset:4640\n\t" \
  "ds_read_b64_tr_b16 v[200:201], v208 offset:64\n\t" \
  "ds_read_b64_tr_b16 v[202:203], v208 offset:4672\n\t" \
  "ds_read_b64_tr_b16 v[204:205], v208 offset:96\n\t" \
  "ds_read_b64_tr_b16 v[206:207], v208 offset:4704\n\t" \
  "s_waitcnt lgkmcnt(6)\n\t" \
  "v_mfma_f32_16x16x32_bf16 a[0:3], v[192:195], v[32:35], 0\n\t" \
  "s_add_u32 s72, s72, 1\n\t" \
  "v_mfma_f32_16x16x32_bf16 a[36:39], v[192:195], v[36:39], 0\n\t" \
  "s_and_b32 s72, s72, 3\n\t" \
  "v_mfma_f32_16x16x32_bf16 a[72:75], v[192:195], v[40:43], 0\n\t" \
  "s_lshl_b32 s61, s64, 2\n\t" \
  "v_mfma_f32_16x16x32_bf16 a[108:111], v[192:195], v[44:47], 0\n\t" \
  "v_mfma_f32_16x16x32_bf16 a[144:147], v[192:195], v[48:51], 0\n\t" \
  "ds_read_b64_tr_b16 v[192:193], v208 offset:128\n\t" \
  "ds_read_b64_tr_b16 v[194:195], v208 offset:4736\n\t" \
  "s_waitcnt lgkmcnt(6)\n\t" \
  "v_mfma_f32_16x16x32_bf16 a[4:7], v[196:199], v[32:35], 0\n\t" \
  "s_add_u32 s61, s61, s72\n\t" \
  "s_lshl_b32 s61, s61, 1\n\t" \
  "v_mfma_f32_16x16x32_bf16 a[40:43], v[196:199], v[36:39], 0\n\t" \
  "v_mfma_f32_16x16x32_bf16 a[76:79], v[196:199], v[40:43], 0\n\t" \
  "s_add_u32 s61, s61, s66\n\t" \
  "v_mfma_f32_16x16x32_bf16 a[112:115], v[196:199], v[44:47], 0\n\t" \
  "s_mul_i32 s68, s61, 147456\n\t" \
  "v_mfma_f32_16x16x32_bf16 a[148:151], v[196:199], v[48:51], 0\n\t" \
  "ds_read_b64_tr_b16 v[196:197], v209 offset:160\n\t" \
  "ds_read_b64_tr_b16 v[198:199], v209 offset:4768\n\t" \
  "s_waitcnt lgkmcnt(6)\n\t" \
  "v_mfma_f32_16x16x32_bf16 a[8:11], v[200:203], v[32:35], 0\n\t" \
  "s_mul_i32 s61, s85, 73728\n\t" \
  "v_mfma_f32_16x16x32_bf16 a[44:47], v[200:203], v[36:39], 0\n\t" \
  "s_add_u32 s68, s68, s61\n\t" \
  "v_mfma_f32_16x16x32_bf16 a[80:83], v[200:203], v[40:43], 0\n\t" \
  "v_mfma_f32_16x16x32_bf16 a[116:119], v[200:203], v[44:47], 0\n\t" \
  "s_sub_i32 s69, s72, s65\n\t" \
  "v_mfma_f32_16x16x32_bf16 a[152:155], v[200:203], v[48:51], 0\n\t" \
  "ds_read_b64_tr_b16 v[200:201], v209 offset:192\n\t" \
  "ds_read_b64_tr_b16 v[202:203], v209 offset:4800\n\t" \
  "s_waitcnt lgkmcnt(6)\n\t" \
  "v_mfma_f32_16x16x32_bf16 a[12:15], v[204:207], v[32:35], 0\n\t" \
  "s_lshl_b32 s69, s69, 13\n\t" \
  "s_add_u32 s62, s67, 73728\n\t" \
  "v_mfma_f32_16x16x32_bf16 a[48:51], v[204:207], v[36:39], 0\n\t" \
  "v_mfma_f32_16x16x32_bf16 a[84:87], v[204:207], v[40:43], 0\n\t" \
  "s_add_u32 m0, s62, 0\n\t" \
  "v_mfma_f32_16x16x32_bf16 a[120:123], v[204:207], v[44:47], 0\n\t" \
  "buffer_load_dwordx4 v232, s[40:43], s68 offen lds\n\t" \
  "v_mfma_f32_16x16x32_bf16 a[156:159], v[204:207], v[48:51], 0\n\t" \
  "ds_read_b64_tr_b16 v[204:205], v209 offset:224\n\t" \
  "ds_read_b64_tr_b16 v[206:207], v209 offset:4832\n\t" \
  "s_waitcnt lgkmcnt(6)\n\t" \
  "v_mfma_f32_16x16x32_bf16 a[16:19], v[192:195], v[32:35], 0\n\t" \
  "s_add_u32 m0, s62, 1024\n\t" \
  "v_mfma_f32_16x16x32_bf16 a[52:55], v[192:195], v[36:39], 0\n\t" \
  "buffer_load_dwordx4 v233, s[40:43], s68 offen lds\n\t" \
  "v_mfma_f32_16x16x32_bf16 a[88:91], v[192:195], v[40:43], 0\n\t" \
  "v_mfma_f32_16x16x32_bf16 a[124:127], v[192:195], v[44:47], 0\n\t" \
  "s_add_u32 m0, s62, 2048\n\t" \
  "v_mfma_f32_16x16x32_bf16 a[160:163], v[192:195], v[48:51], 0\n\t" \
  "ds_read_b64_tr_b16 v[192:193], v209 offset:256\n\t" \
  "ds_read_b64_tr_b16 v[194:195], v209 offset:4864\n\t" \
  "s_waitcnt lgkmcnt(6)\n\t" \
  "v_mfma_f32_16x16x32_bf16 a[20:23], v[196:199], v[32:35], 0\n\t" \
  "buffer_load_dwordx4 v234, s[40:43], s68 offen lds\n\t" \
  "v_mfma_f32_16x16x32_bf16 a[56:59], v[196:199], v[36:39], 0\n\t" \
  "s_add_u32 m0, s62, 3072\n\t" \
  "v_mfma_f32_16x16x32_bf16 a[92:95], v[196:199], v[40:43], 0\n\t" \
  "buffer_load_dwordx4 v235, s[40:43], s68 offen lds\n\t" \
  "v_mfma_f32_16x16x32_bf16 a[128:131], v[196:199], v[44:47], 0\n\t" \
  "ds_read_b64_tr_b16 v[196:197], v208 offset:9216\n\t" \
  "ds_read_b64_tr_b16 v[198:199], v208 offset:13824\n\t" \
  "s_waitcnt lgkmcnt(6)\n\t" \
  "v_mfma_f32_16x16x32_bf16 a[24:27], v[200:203], v[32:35], 0\n\t" \
  "s_add_u32 m0, s62, 4096\n\t" \
  "v_mfma_f32_16x16x32_bf16 a[60:63], v[200:203], v[36:39], 0\n\t" \
  "buffer_load_dwordx4 v236, s[40:43], s68 offen lds\n\t" \
  "v_mfma_f32_16x16x32_bf16 a[96:99], v[200:203], v[40:43], 0\n\t" \
  "s_add_u32 m0, s62, 5120\n\t" \
  "v_mfma_f32_16x16x32_bf16 a[132:135], v[200:203], v[44:47], 0\n\t" \
  "ds_read_b64_tr_b16 v[200:201], v208 offset:9248\n\t" \
  "ds_read_b64_tr_b16 v[202:203], v208 offset:13856\n\t" \
  "s_waitcnt lgkmcnt(4)\n\t" \
  "v_mfma_f32_16x16x32_bf16 a[28:31], v[204:207], v[32:35], 0\n\t" \
  "buffer_load_dwordx4 v237, s[40:43], s68 offen lds\n\t" \
  "v_mfma_f32_16x16x32_bf16 a[64:67], v[204:207], v[36:39], 0\n\t" \
  "s_add_u32 m0, s62, 6144\n\t" \
  "v_mfma_f32_16x16x32_bf16 a[100:103], v[204:207], v[40:43], 0\n\t" \
  "v_mfma_f32_16x16x32_bf16 a[136:139], v[204:207], v[44:47], 0\n\t" \
  "ds_read_b64_tr_b16 v[204:205], v208 offset:9280\n\t" \
  "ds_read_b64_tr_b16 v[206:207], v208 offset:13888\n\t" \
  "v_mfma_f32_16x16x32_bf16 a[32:35], v[192:195], v[32:35], 0\n\t" \
  "buffer_load_dwordx4 v238, s[40:43], s68 offen lds\n\t" \
  "s_add_u32 m0, s62, 7168\n\t" \
  "v_mfma_f32_16x16x32_bf16 a[68:71], v[192:195], v[36:39], 0\n\t" \
  "v_mfma_f32_16x16x32_bf16 a[104:107], v[192:195], v[40:43], 0\n\t" \
  "buffer_load_dwordx4 v239, s[40:43], s68 offen lds\n\t" \
  "v_mfma_f32_16x16x32_bf16 a[140:143], v[192:195], v[44:47], 0\n\t" \
  "ds_read_b64_tr_b16 v[192:193], v208 offset:9312\n\t" \
  "ds_read_b64_tr_b16 v[194:195], v208 offset:13920\n\t" \
  "s_waitcnt lgkmcnt(6)\n\t" \
  "v_mfma_f32_16x16x32_bf16 a[0:3], v[196:199], v[52:55], a[0:3]\n\t" \
  "s_add_u32 m0, s62, 8192\n\t" \
  "v_mfma_f32_16x16x32_bf16 a[36:39], v[196:199], v[56:59], a[36:39]\n\t" \
  "buffer_load_dwordx4 v240, s[40:43], s68 offen lds\n\t" \
  "v_mfma_f32_16x16x32_bf16 a[72:75], v[196:199], v[60:63], a[72:75]\n\t" \
  "s_add_u32 s61, s68, 36864\n\t" \
  "v_mfma_f32_16x16x32_bf16 a[108:111], v[196:199], v[64:67], a[108:111]\n\t" \
  "s_add_u32 m0, s62, 9216\n\t" \
  "v_mfma_f32_16x16x32_bf16 a[144:147], v[196:199], v[68:71], a[144:147]\n\t" \
  "ds_read_b64_tr_b16 v[196:197], v208 offset:9344\n\t" \
  "ds_read_b64_tr_b16 v[198:199], v208 offset:13952\n\t" \
  "s_waitcnt lgkmcnt(6)\n\t" \
  "v_mfma_f32_16x16x32_bf16 a[4:7], v[200:203], v[52:55], a[4:7]\n\t" \
  "buffer_load_dwordx4 v232, s[40:43], s61 offen lds\n\t" \
  "v_mfma_f32_16x16x32_bf16 a[40:43], v[200:203], v[56:59], a[40:43]\n\t" \
  "buffer_store_dwordx4 v[100:103], v244, s[56:59], s77 offen offset:3072 sc1\n\t" \
  "v_mfma_f32_16x16x32_bf16 a[76:79], v[200:203], v[60:63], a[76:79]\n\t" \
  "v_mfma_f32_16x16x32_bf16 a[112:115], v[200:203], v[64:67], a[112:115]\n\t" \
  "s_add_u32 m0, s62, 10240\n\t" \
  "v_mfma_f32_16x16x32_bf16 a[148:151], v[200:203], v[68:71], a[148:151]\n\t" \
  "ds_read_b64_tr_b16 v[200:201], v209 offset:9376\n\t" \
  "ds_read_b64_tr_b16 v[202:203], v209 offset:13984\n\t" \
  "s_waitcnt lgkmcnt(6)\n\t" \
  "v_mfma_f32_16x16x32_bf16 a[8:11], v[204:207], v[52:55], a[8:11]\n\t" \
  "buffer_load_dwordx4 v233, s[40:43], s61 offen lds\n\t" \
  "v_mfma_f32_16x16x32_bf16 a[44:47], v[204:207], v[56:59], a[44:47]\n\t" \
  "s_add_u32 m0, s62, 11264\n\t" \
  "v_mfma_f32_16x16x32_bf16 a[80:83], v[204:207], v[60:63], a[80:83]\n\t" \
  "buffer_load_dwordx4 v234, s[40:43], s61 offen lds\n\t" \
  "v_mfma_f32_16x16x32_bf16 a[116:119], v[204:207], v[64:67], a[116:119]\n\t" \
  "v_mfma_f32_16x16x32_bf16 a[152:155], v[204:207], v[68:71], a[152:155]\n\t" \
  "ds_read_b64_tr_b16 v[204:205], v209 offset:9408\n\t" \
  "ds_read_b64_tr_b16 v[206:207], v209 offset:14016\n\t" \
  "s_waitcnt lgkmcnt(6)\n\t" \
  "v_mfma_f32_16x16x32_bf16 a[12:15], v[192:195], v[52:55], a[12:15]\n\t" \
  "s_add_u32 m0, s62, 12288\n\t" \
  "s_nop 0\n\t" \
  "buffer_load_dwordx4 v235, s[40:43], s61 offen lds\n\t" \
  "v_mfma_f32_16x16x32_bf16 a[48:51], v[192:195], v[56:59], a[48:51]\n\t" \
  "v_mfma_f32_16x16x32_bf16 a[84:87], v[192:195], v[60:63], a[84:87]\n\t" \
  "s_add_u32 m0, s62, 13312\n\t" \
  "v_mfma_f32_16x16x32_bf16 a[120:123], v[192:195], v[64:67], a[120:123]\n\t" \
  "buffer_load_dwordx4 v236, s[40:43], s61 offen lds\n\t" \
  "v_mfma_f32_16x16x32_bf16 a[156:159], v[192:195], v[68:71], a[156:159]\n\t" \
  "ds_read_b64_tr_b16 v[192:193], v209 offset:9440\n\t" \
  "ds_read_b64_tr_b16 v[194:195], v209 offset:14048\n\t" \
  "s_waitcnt lgkmcnt(6)\n\t" \
  "v_mfma_f32_16x16x32_bf16 a[16:19], v[196:199], v[52:55], a[16:19]\n\t" \
  "buffer_store_dwordx4 v[44:47], v244, s[56:59], s78 offen sc1\n\t" \
  "v_mfma_f32_16x16x32_bf16 a[52:55], v[196:199], v[56:59], a[52:55]\n\t" \
  "s_add_u32 m0, s62, 14336\n\t" \
  "v_mfma_f32_16x16x32_bf16 a[88:91], v[196:199], v[60:63], a[88:91]\n\t" \
  "buffer_load_dwordx4 v237, s[40:43], s61 offen lds\n\t" \
  "v_mfma_f32_16x16x32_bf16 a[124:127], v[196:199], v[64:67], a[124:127]\n\t" \
  "v_mfma_f32_16x16x32_bf16 a[160:163], v[196:199], v[68:71], a[160:163]\n\t" \
  "ds_read_b64_tr_b16 v[196:197], v209 offset:9472\n\t" \
  "ds_read_b64_tr_b16 v[198:199], v209 offset:14080\n\t" \
  "s_waitcnt lgkmcnt(6)\n\t" \
  "v_mfma_f32_16x16x32_bf16 a[20:23], v[200:203], v[52:55], a[20:23]\n\t" \
  "s_add_u32 m0, s62, 15360\n\t" \
  "s_nop 0\n\t" \
  "buffer_load_dwordx4 v238, s[40:43], s61 offen lds\n\t" \
  "v_mfma_f32_16x16x32_bf16 a[56:59], v[200:203], v[56:59], a[56:59]\n\t" \
  "v_mfma_f32_16x16x32_bf16 a[92:95], v[200:203], v[60:63], a[92:95]\n\t" \
  "s_add_u32 m0, s62, 16384\n\t" \
  "v_mfma_f32_16x16x32_bf16 a[128:131], v[200:203], v[64:67], a[128:131]\n\t" \
  "ds_read_b64_tr_b16 v[200:201], v208 offset:18432\n\t" \
  "ds_read_b64_tr_b16 v[202:203], v208 offset:23040\n\t" \
  "s_waitcnt lgkmcnt(6)\n\t" \
  "v_mfma_f32_16x16x32_bf16 a[24:27], v[204:207], v[52:55], a[24:27]\n\t" \
  "buffer_load_dwordx4 v239, s[40:43], s61 offen lds\n\t" \
  "v_mfma_f32_16x16x32_bf16 a[60:63], v[204:207], v[56:59], a[60:63]\n\t" \
  "s_add_u32 m0, s62, 17408\n\t" \
  "v_mfma_f32_16x16x32_bf16 a[96:99], v[204:207], v[60:63], a[96:99]\n\t" \
  "buffer_load_dwordx4 v240, s[40:43], s61 offen lds\n\t" \
  "v_mfma_f32_16x16x32_bf16 a[132:135], v[204:207], v[64:67], a[132:135]\n\t" \
  "ds_read_b64_tr_b16 v[204:205], v208 offset:18464\n\t" \
  "ds_read_b64_tr_b16 v[206:207], v208 offset:23072\n\t" \
  "s_waitcnt lgkmcnt(6)\n\t" \
  "v_mfma_f32_16x16x32_bf16 a[28:31], v[192:195], v[52:55], a[28:31]\n\t" \
  "buffer_store_dwordx4 v[64:67], v244, s[56:59], s78 offen offset:1024 sc1\n\t" \
  "v_mfma_f32_16x16x32_bf16 a[64:67], v[192:195], v[56:59], a[64:67]\n\t" \
  "buffer_store_dwordx4 v[84:87], v244, s[56:59], s78 offen offset:2048 sc1\n\t" \
  "v_mfma_f32_16x16x32_bf16 a[100:103], v[192:195], v[60:63], a[100:103]\n\t" \
  "buffer_store_dwordx4 v[104:107], v244, s[56:59], s78 offen offset:3072 sc1\n\t" \
  "v_mfma_f32_16x16x32_bf16 a[136:139], v[192:195], v[64:67], a[136:139]\n\t" \
  "ds_read_b64_tr_b16 v[192:193], v208 offset:18496\n\t" \
  "ds_read_b64_tr_b16 v[194:195], v208 offset:23104\n\t" \
  "s_waitcnt lgkmcnt(6)\n\t" \
  "v_mfma_f32_16x16x32_bf16 a[32:35], v[196:199], v[52:55], a[32:35]\n\t" \
  "v_mfma_f32_16x16x32_bf16 a[68:71], v[196:199], v[56:59], a[68:71]\n\t" \
  "v_mfma_f32_16x16x32_bf16 a[104:107], v[196:199], v[60:63], a[104:107]\n\t" \
  "v_mfma_f32_16x16x32_bf16 a[140:143], v[196:199], v[64:67], a[140:143]\n\t" \
  "ds_read_b64_tr_b16 v[196:197], v208 offset:18528\n\t" \
  "ds_read_b64_tr_b16 v[198:199], v208 offset:23136\n\t" \
  "s_waitcnt vmcnt(0)\n\t" \
  "s_barrier\n\t" \
  "s_cmp_lg_u32 s73, 0\n\t" \
  "s_cbranch_scc1 PB0_%=\n\t" \
  "v_mov_b32 v250, s52\n\t" \
  "v_mov_b32 v251, s53\n\t" \
  "v_mov_b32 v254, 0\n\t" \
  "s_mov_b64 exec, 1\n\t" \
  "s_nop 1\n\t" \
  "buffer_store_dwordx2 v[250:251], v254, s[96:99], s79 offen sc1\n\t" \
  "s_mov_b64 exec, -1\n\t" \
  "s_nop 4\n\t" \
  "PB0_%=:\n\t" \
  "s_waitcnt lgkmcnt(4)\n\t" \
  "v_mfma_f32_16x16x32_bf16 a[0:3], v[200:203], v[72:75], a[0:3]\n\t" \
  "v_mfma_f32_16x16x32_bf16 a[36:39], v[200:203], v[76:79], a[36:39]\n\t" \
  "v_mfma_f32_16x16x32_bf16 a[72:75], v[200:203], v[80:83], a[72:75]\n\t" \
  "v_mfma_f32_16x16x32_bf16 a[108:111], v[200:203], v[84:87], a[108:111]\n\t" \
  "v_mfma_f32_16x16x32_bf16 a[144:147], v[200:203], v[88:91], a[144:147]\n\t" \
  "ds_read_b64_tr_b16 v[200:201], v208 offset:18560\n\t" \
  "ds_read_b64_tr_b16 v[202:203], v208 offset:23168\n\t" \
  "v_mfma_f32_16x16x32_bf16 a[4:7], v[204:207], v[72:75], a[4:7]\n\t" \
  "v_mfma_f32_16x16x32_bf16 a[40:43], v[204:207], v[76:79], a[40:43]\n\t" \
  "v_mfma_f32_16x16x32_bf16 a[76:79], v[204:207], v[80:83], a[76:79]\n\t" \
  "v_mfma_f32_16x16x32_bf16 a[112:115], v[204:207], v[84:87], a[112:115]\n\t" \
  "v_mfma_f32_16x16x32_bf16 a[148:151], v[204:207], v[88:91], a[148:151]\n\t" \
  "ds_read_b64_tr_b16 v[204:205], v209 offset:18592\n\t" \
  "ds_read_b64_tr_b16 v[206:207], v209 offset:23200\n\t" \
  "s_waitcnt lgkmcnt(4)\n\t" \
  "v_mfma_f32_16x16x32_bf16 a[8:11], v[192:195], v[72:75], a[8:11]\n\t" \
  "v_mfma_f32_16x16x32_bf16 a[44:47], v[192:195], v[76:79], a[44:47]\n\t" \
  "v_mfma_f32_16x16x32_bf16 a[80:83], v[192:195], v[80:83], a[80:83]\n\t" \
  "v_mfma_f32_16x16x32_bf16 a[116:119], v[192:195], v[84:87], a[116:119]\n\t" \
  "v_mfma_f32_16x16x32_bf16 a[152:155], v[192:195], v[88:91], a[152:155]\n\t" \
  "ds_read_b64_tr_b16 v[192:193], v209 offset:18624\n\t" \
  "ds_read_b64_tr_b16 v[194:195], v209 offset:23232\n\t" \
  "v_mfma_f32_16x16x32_bf16 a[12:15], v[196:199], v[72:75], a[12:15]\n\t" \
  "v_mfma_f32_16x16x32_bf16 a[48:51], v[196:199], v[76:79], a[48:51]\n\t" \
  "v_mfma_f32_16x16x32_bf16 a[84:87], v[196:199], v[80:83], a[84:87]\n\t" \
  "v_mfma_f32_16x16x32_bf16 a[120:123], v[196:199], v[84:87], a[120:123]\n\t" \
  "v_mfma_f32_16x16x32_bf16 a[156:159], v[196:199], v[88:91], a[156:159]\n\t" \
  "ds_read_b64_tr_b16 v[196:197], v209 offset:18656\n\t" \
  "ds_read_b64_tr_b16 v[198:199], v209 offset:23264\n\t" \
  "s_waitcnt lgkmcnt(6)\n\t" \
  "v_mfma_f32_16x16x32_bf16 a[16:19], v[200:203], v[72:75], a[16:19]\n\t" \
  "v_mfma_f32_16x16x32_bf16 a[52:55], v[200:203], v[76:79], a[52:55]\n\t" \
  "v_mfma_f32_16x16x32_bf16 a[88:91], v[200:203], v[80:83], a[88:91]\n\t" \
  "v_mfma_f32_16x16x32_bf16 a[124:127], v[200:203], v[84:87], a[124:127]\n\t" \
  "v_mfma_f32_16x16x32_bf16 a[160:163], v[200:203], v[88:91], a[160:163]\n\t" \
  "ds_read_b64_tr_b16 v[200:201], v209 offset:18688\n\t" \
  "ds_read_b64_tr_b16 v[202:203], v209 offset:23296\n\t" \
  "s_waitcnt lgkmcnt(6)\n\t" \
  "v_mfma_f32_16x16x32_bf16 a[20:23], v[204:207], v[72:75], a[20:23]\n\t" \
  "v_mfma_f32_16x16x32_bf16 a[56:59], v[204:207], v[76:79], a[56:59]\n\t" \
  "v_mfma_f32_16x16x32_bf16 a[92:95], v[204:207], v[80:83], a[92:95]\n\t" \
  "v_mfma_f32_16x16x32_bf16 a[128:131], v[204:207], v[84:87], a[128:131]\n\t" \
  "ds_read_b64_tr_b16 v[204:205], v208 offset:27648\n\t" \
  "ds_read_b64_tr_b16 v[206:207], v208 offset:32256\n\t" \
  "s_waitcnt lgkmcnt(6)\n\t" \
  "v_mfma_f32_16x16x32_bf16 a[24:27], v[192:195], v[72:75], a[24:27]\n\t" \
  "v_mfma_f32_16x16x32_bf16 a[60:63], v[192:195], v[76:79], a[60:63]\n\t" \
  "v_mfma_f32_16x16x32_bf16 a[96:99], v[192:195], v[80:83], a[96:99]\n\t" \
  "v_mfma_f32_16x16x32_bf16 a[132:135], v[192:195], v[84:87], a[132:135]\n\t" \
  "ds_read_b64_tr_b16 v[192:193], v208 offset:27680\n\t" \
  "ds_read_b64_tr_b16 v[194:195], v208 offset:32288\n\t" \
  "s_waitcnt lgkmcnt(6)\n\t" \
  "v_mfma_f32_16x16x32_bf16 a[28:31], v[196:199], v[72:75], a[28:31]\n\t" \
  "v_mfma_f32_16x16x32_bf16 a[64:67], v[196:199], v[76:79], a[64:67]\n\t" \
  "v_mfma_f32_16x16x32_bf16 a[100:103], v[196:199], v[80:83], a[100:103]\n\t" \
  "v_mfma_f32_16x16x32_bf16 a[136:139], v[196:199], v[84:87], a[136:139]\n\t" \
  "ds_read_b64_tr_b16 v[196:197], v208 offset:27712\n\t" \
  "ds_read_b64_tr_b16 v[198:199], v208 offset:32320\n\t" \
  "s_waitcnt lgkmcnt(6)\n\t" \
  "v_mfma_f32_16x16x32_bf16 a[32:35], v[200:203], v[72:75], a[32:35]\n\t" \
  "v_mfma_f32_16x16x32_bf16 a[68:71], v[200:203], v[76:79], a[68:71]\n\t" \
  "v_mfma_f32_16x16x32_bf16 a[104:107], v[200:203], v[80:83], a[104:107]\n\t" \
  "v_mfma_f32_16x16x32_bf16 a[140:143], v[200:203], v[84:87], a[140:143]\n\t" \
  "ds_read_b64_tr_b16 v[200:201], v208 offset:27744\n\t" \
  "ds_read_b64_tr_b16 v[202:203], v208 offset:32352\n\t" \
  "s_waitcnt lgkmcnt(6)\n\t" \
  "v_mfma_f32_16x16x32_bf16 a[0:3], v[204:207], v[92:95], a[0:3]\n\t" \
  "v_mfma_f32_16x16x32_bf16 a[36:39], v[204:207], v[96:99], a[36:39]\n\t" \
  "v_mfma_f32_16x16x32_bf16 a[72:75], v[204:207], v[100:103], a[72:75]\n\t" \
  "v_mfma_f32_16x16x32_bf16 a[108:111], v[204:207], v[104:107], a[108:111]\n\t" \
  "v_mfma_f32_16x16x32_bf16 a[144:147], v[204:207], v[108:111], a[144:147]\n\t" \
  "ds_read_b64_tr_b16 v[204:205], v208 offset:27776\n\t" \
  "ds_read_b64_tr_b16 v[206:207], v208 offset:32384\n\t" \
  "s_waitcnt lgkmcnt(6)\n\t" \
  "v_mfma_f32_16x16x32_bf16 a[4:7], v[192:195], v[92:95], a[4:7]\n\t" \
  "v_mfma_f32_16x16x32_bf16 a[40:43], v[192:195], v[96:99], a[40:43]\n\t" \
  "v_mfma_f32_16x16x32_bf16 a[76:79], v[192:195], v[100:103], a[76:79]\n\t" \
  "v_mfma_f32_16x16x32_bf16 a[112:115], v[192:195], v[104:107], a[112:115]\n\t" \
  "v_mfma_f32_16x16x32_bf16 a[148:151], v[192:195], v[108:111], a[148:151]\n\t" \
  "ds_read_b64_tr_b16 v[192:193], v209 offset:27808\n\t" \
  "ds_read_b64_tr_b16 v[194:195], v209 offset:32416\n\t" \
  "s_waitcnt lgkmcnt(6)\n\t" \
  "v_mfma_f32_16x16x32_bf16 a[8:11], v[196:199], v[92:95], a[8:11]\n\t" \
  "v_mfma_f32_16x16x32_bf16 a[44:47], v[196:199], v[96:99], a[44:47]\n\t" \
  "v_mfma_f32_16x16x32_bf16 a[80:83], v[196:199], v[100:103], a[80:83]\n\t" \
  "v_mfma_f32_16x16x32_bf16 a[116:119], v[196:199], v[104:107], a[116:119]\n\t" \
  "v_mfma_f32_16x16x32_bf16 a[152:155], v[196:199], v[108:111], a[152:155]\n\t" \
  "ds_read_b64_tr_b16 v[196:197], v209 offset:27840\n\t" \
  "ds_read_b64_tr_b16 v[198:199], v209 offset:32448\n\t" \
  "s_waitcnt lgkmcnt(6)\n\t" \
  "v_mfma_f32_16x16x32_bf16 a[12:15], v[200:203], v[92:95], a[12:15]\n\t" \
  "v_mfma_f32_16x16x32_bf16 a[48:51], v[200:203], v[96:99], a[48:51]\n\t" \
  "v_mfma_f32_16x16x32_bf16 a[84:87], v[200:203], v[100:103], a[84:87]\n\t" \
  "v_mfma_f32_16x16x32_bf16 a[120:123], v[200:203], v[104:107], a[120:123]\n\t" \
  "v_mfma_f32_16x16x32_bf16 a[156:159], v[200:203], v[108:111], a[156:159]\n\t" \
  "ds_read_b64_tr_b16 v[200:201], v209 offset:27872\n\t" \
  "ds_read_b64_tr_b16 v[202:203], v209 offset:32480\n\t" \
  "s_waitcnt lgkmcnt(6)\n\t" \
  "v_mfma_f32_16x16x32_bf16 a[16:19], v[204:207], v[92:95], a[16:19]\n\t" \
  "v_mfma_f32_16x16x32_bf16 a[52:55], v[204:207], v[96:99], a[52:55]\n\t" \
  "v_mfma_f32_16x16x32_bf16 a[88:91], v[204:207], v[100:103], a[88:91]\n\t" \
  "v_mfma_f32_16x16x32_bf16 a[124:127], v[204:207], v[104:107], a[124:127]\n\t" \
  "v_mfma_f32_16x16x32_bf16 a[160:163], v[204:207], v[108:111], a[160:163]\n\t" \
  "ds_read_b64_tr_b16 v[204:205], v209 offset:27904\n\t" \
  "ds_read_b64_tr_b16 v[206:207], v209 offset:32512\n\t" \
  "s_waitcnt lgkmcnt(6)\n\t" \
  "v_mfma_f32_16x16x32_bf16 a[20:23], v[192:195], v[92:95], a[20:23]\n\t" \
  "v_mfma_f32_16x16x32_bf16 a[56:59], v[192:195], v[96:99], a[56:59]\n\t" \
  "v_mfma_f32_16x16x32_bf16 a[92:95], v[192:195], v[100:103], a[92:95]\n\t" \
  "v_mfma_f32_16x16x32_bf16 a[128:131], v[192:195], v[104:107], a[128:131]\n\t" \
  "s_waitcnt lgkmcnt(4)\n\t" \
  "v_mfma_f32_16x16x32_bf16 a[24:27], v[196:199], v[92:95], a[24:27]\n\t" \
  "v_mfma_f32_16x16x32_bf16 a[60:63], v[196:199], v[96:99], a[60:63]\n\t" \
  "v_mfma_f32_16x16x32_bf16 a[96:99], v[196:199], v[100:103], a[96:99]\n\t" \
  "v_mfma_f32_16x16x32_bf16 a[132:135], v[196:199], v[104:107], a[132:135]\n\t" \
  "s_waitcnt lgkmcnt(2)\n\t" \
  "v_mfma_f32_16x16x32_bf16 a[28:31], v[200:203], v[92:95], a[28:31]\n\t" \
  "v_mfma_f32_16x16x32_bf16 a[64:67], v[200:203], v[96:99], a[64:67]\n\t" \
  "v_mfma_f32_16x16x32_bf16 a[100:103], v[200:203], v[100:103], a[100:103]\n\t" \
  "v_mfma_f32_16x16x32_bf16 a[136:139], v[200:203], v[104:107], a[136:139]\n\t" \
  "s_waitcnt lgkmcnt(0)\n\t" \
  "v_mfma_f32_16x16x32_bf16 a[32:35], v[204:207], v[92:95], a[32:35]\n\t" \
  "v_mfma_f32_16x16x32_bf16 a[68:71], v[204:207], v[96:99], a[68:71]\n\t" \
  "v_mfma_f32_16x16x32_bf16 a[104:107], v[204:207], v[100:103], a[104:107]\n\t" \
  "v_mfma_f32_16x16x32_bf16 a[140:143], v[204:207], v[104:107], a[140:143]\n\t" \
  "s_cmp_lg_u32 s73, 0\n\t" \
  "s_cbranch_scc1 PL0_%=\n\t" \
  "P2L_%=:\n\t" \
  "buffer_load_dwordx2 v[250:251], v252, s[96:99], 0 offen sc1\n\t" \
  "s_waitcnt vmcnt(0)\n\t" \
  "v_cmp_eq_u64 vcc, s[52:53], v[250:251]\n\t" \
  "s_nop 4\n\t" \
  "s_cmp_eq_u64 vcc, exec\n\t" \
  "s_cbranch_scc1 P2D_%=\n\t" \
  "s_sleep 2\n\t" \
  "s_add_u32 s74, s74, 1\n\t" \
  "s_cmp_lt_u32 s74, 0x40000\n\t" \
  "s_cbranch_scc1 P2L_%=\n\t" \
  "P2D_%=:\n\t" \
  "PL0_%=:\n\t" \
  "s_barrier\n\t" \
  "s_add_u32 s80, s83, s69\n\t" \
  "s_add_u32 s81, s83, s69\n\t" \
  "s_add_u32 s81, s81, 131072\n\t" \
  "s_add_u32 s82, s83, s69\n\t" \
  "s_add_u32 s82, s82, 262144\n\t" \
  "s_add_u32 s87, s83, s69\n\t" \
  "s_add_u32 s87, s87, 393216\n\t" \
  "s_add_u32 s95, s75, s69\n\t" \
  "buffer_load_dwordx4 v[112:115], v244, s[56:59], s80 offen sc1\n\t" \
  "buffer_load_dwordx4 v[116:119], v244, s[56:59], s81 offen sc1\n\t" \
  "buffer_load_dwordx4 v[120:123], v244, s[56:59], s82 offen sc1\n\t" \
  "buffer_load_dwordx4 v[124:127], v244, s[56:59], s87 offen sc1\n\t" \
  "buffer_load_dwordx4 v[128:131], v244, s[56:59], s95 offen sc1\n\t" \
  "buffer_load_dwordx4 v[132:135], v244, s[56:59], s80 offen offset:1024 sc1\n\t" \
  "buffer_load_dwordx4 v[136:139], v244, s[56:59], s81 offen offset:1024 sc1\n\t" \
  "buffer_load_dwordx4 v[140:143], v244, s[56:59], s82 offen offset:1024 sc1\n\t" \
  "buffer_load_dwordx4 v[144:147], v244, s[56:59], s87 offen offset:1024 sc1\n\t" \
  "buffer_load_dwordx4 v[148:151], v244, s[56:59], s95 offen offset:1024 sc1\n\t" \
  "buffer_load_dwordx4 v[152:155], v244, s[56:59], s80 offen offset:2048 sc1\n\t" \
  "buffer_load_dwordx4 v[156:159], v244, s[56:59], s81 offen offset:2048 sc1\n\t" \
  "buffer_load_dwordx4 v[160:163], v244, s[56:59], s82 offen offset:2048 sc1\n\t" \
  "buffer_load_dwordx4 v[164:167], v244, s[56:59], s87 offen offset:2048 sc1\n\t" \
  "buffer_load_dwordx4 v[168:171], v244, s[56:59], s95 offen offset:2048 sc1\n\t" \
  "buffer_load_dwordx4 v[172:175], v244, s[56:59], s80 offen offset:3072 sc1\n\t" \
  "buffer_load_dwordx4 v[176:179], v244, s[56:59], s81 offen offset:3072 sc1\n\t" \
  "buffer_load_dwordx4 v[180:183], v244, s[56:59], s82 offen offset:3072 sc1\n\t" \
  "buffer_load_dwordx4 v[184:187], v244, s[56:59], s87 offen offset:3072 sc1\n\t" \
  "buffer_load_dwordx4 v[188:191], v244, s[56:59], s95 offen offset:3072 sc1\n\t" \
  "s_waitcnt vmcnt(20)\n\t" \
  "s_barrier\n\t" \
  "v_add_u32 v208, 73728, v210\n\t" \
  "v_add_u32 v209, 73728, v211\n\t" \
  "ds_read_b64_tr_b16 v[192:193], v208 offset:0\n\t" \
  "ds_read_b64_tr_b16 v[194:195], v208 offset:4608\n\t" \
  "ds_read_b64_tr_b16 v[196:197], v208 offset:32\n\t" \
  "ds_read_b64_tr_b16 v[198:199], v208 offset:4640\n\t" \
  "ds_read_b64_tr_b16 v[200:201], v208 offset:64\n\t" \
  "ds_read_b64_tr_b16 v[202:203], v208 offset:4672\n\t" \
  "ds_read_b64_tr_b16 v[204:205], v208 offset:96\n\t" \
  "ds_read_b64_tr_b16 v[206:207], v208 offset:4704\n\t" \
  "s_waitcnt vmcnt(15)\n\t" \
  "s_waitcnt lgkmcnt(6)\n\t" \
  "v_mfma_f32_16x16x32_bf16 a[0:3], v[192:195], v[112:115], a[0:3]\n\t" \
  "s_add_u32 s72, s72, 1\n\t" \
  "v_mfma_f32_16x16x32_bf16 a[36:39], v[192:195], v[116:119], a[36:39]\n\t" \
  "s_and_b32 s72, s72, 3\n\t" \
  "v_mfma_f32_16x16x32_bf16 a[72:75], v[192:195], v[120:123], a[72:75]\n\t" \
  "v_mfma_f32_16x16x32_bf16 a[108:111], v[192:195], v[124:127], a[108:111]\n\t" \
  "s_lshl_b32 s61, s64, 2\n\t" \
  "v_mfma_f32_16x16x32_bf16 a[144:147], v[192:195], v[128:131], a[144:147]\n\t" \
  "ds_read_b64_tr_b16 v[192:193], v208 offset:128\n\t" \
  "ds_read_b64_tr_b16 v[194:195], v208 offset:4736\n\t" \
  "s_waitcnt lgkmcnt(6)\n\t" \
  "v_mfma_f32_16x16x32_bf16 a[4:7], v[196:199], v[112:115], a[4:7]\n\t" \
  "s_add_u32 s61, s61, s72\n\t" \
  "v_mfma_f32_16x16x32_bf16 a[40:43], v[196:199], v[116:119], a[40:43]\n\t" \
  "s_lshl_b32 s61, s61, 1\n\t" \
  "v_mfma_f32_16x16x32_bf16 a[76:79], v[196:199], v[120:123], a[76:79]\n\t" \
  "s_add_u32 s61, s61, s66\n\t" \
  "v_mfma_f32_16x16x32_bf16 a[112:115], v[196:199], v[124:127], a[112:115]\n\t" \
  "v_mfma_f32_16x16x32_bf16 a[148:151], v[196:199], v[128:131], a[148:151]\n\t" \
  "ds_read_b64_tr_b16 v[196:197], v209 offset:160\n\t" \
  "ds_read_b64_tr_b16 v[198:199], v209 offset:4768\n\t" \
  "s_waitcnt lgkmcnt(6)\n\t" \
  "v_mfma_f32_16x16x32_bf16 a[8:11], v[200:203], v[112:115], a[8:11]\n\t" \
  "s_mul_i32 s68, s61, 147456\n\t" \
  "s_mul_i32 s61, s85, 73728\n\t" \
  "v_mfma_f32_16x16x32_bf16 a[44:47], v[200:203], v[116:119], a[44:47]\n\t" \
  "v_mfma_f32_16x16x32_bf16 a[80:83], v[200:203], v[120:123], a[80:83]\n\t" \
  "s_add_u32 s68, s68, s61\n\t" \
  "v_mfma_f32_16x16x32_bf16 a[116:119], v[200:203], v[124:127], a[116:119]\n\t" \
  "s_sub_i32 s69, s72, s65\n\t" \
  "v_mfma_f32_16x16x32_bf16 a[152:155], v[200:203], v[128:131], a[152:155]\n\t" \
  "ds_read_b64_tr_b16 v[200:201], v209 offset:192\n\t" \
  "ds_read_b64_tr_b16 v[202:203], v209 offset:4800\n\t" \
  "s_waitcnt lgkmcnt(6)\n\t" \
  "v_mfma_f32_16x16x32_bf16 a[12:15], v[204:207], v[112:115], a[12:15]\n\t" \
  "s_lshl_b32 s69, s69, 13\n\t" \
  "v_mfma_f32_16x16x32_bf16 a[48:51], v[204:207], v[116:119], a[48:51]\n\t" \
  "s_add_u32 s62, s67, 0\n\t" \
  "v_mfma_f32_16x16x32_bf16 a[84:87], v[204:207], v[120:123], a[84:87]\n\t" \
  "v_mfma_f32_16x16x32_bf16 a[120:123], v[204:207], v[124:127], a[120:123]\n\t" \
  "s_add_u32 m0, s62, 0\n\t" \
  "v_mfma_f32_16x16x32_bf16 a[156:159], v[204:207], v[128:131], a[156:159]\n\t" \
  "ds_read_b64_tr_b16 v[204:205], v209 offset:224\n\t" \
  "ds_read_b64_tr_b16 v[206:207], v209 offset:4832\n\t" \
  "s_waitcnt lgkmcnt(6)\n\t" \
  "v_mfma_f32_16x16x32_bf16 a[16:19], v[192:195], v[112:115], a[16:19]\n\t" \
  "buffer_load_dwordx4 v232, s[40:43], s68 offen lds\n\t" \
  "v_mfma_f32_16x16x32_bf16 a[52:55], v[192:195], v[116:119], a[52:55]\n\t" \
  "s_add_u32 m0, s62, 1024\n\t" \
  "v_mfma_f32_16x16x32_bf16 a[88:91], v[192:195], v[120:123], a[88:91]\n\t" \
  "buffer_load_dwordx4 v233, s[40:43], s68 offen lds\n\t" \
  "v_mfma_f32_16x16x32_bf16 a[124:127], v[192:195], v[124:127], a[124:127]\n\t" \
  "v_mfma_f32_16x16x32_bf16 a[160:163], v[192:195], v[128:131], a[160:163]\n\t" \
  "ds_read_b64_tr_b16 v[192:193], v209 offset:256\n\t" \
  "ds_read_b64_tr_b16 v[194:195], v209 offset:4864\n\t" \
  "s_waitcnt lgkmcnt(6)\n\t" \
  "v_mfma_f32_16x16x32_bf16 a[20:23], v[196:199], v[112:115], a[20:23]\n\t" \
  "s_add_u32 m0, s62, 2048\n\t" \
  "s_nop 0\n\t" \
  "buffer_load_dwordx4 v234, s[40:43], s68 offen lds\n\t" \
  "v_mfma_f32_16x16x32_bf16 a[56:59], v[196:199], v[116:119], a[56:59]\n\t" \
  "v_mfma_f32_16x16x32_bf16 a[92:95], v[196:199], v[120:123], a[92:95]\n\t" \
  "s_add_u32 m0, s62, 3072\n\t" \
  "v_mfma_f32_16x16x32_bf16 a[128:131], v[196:199], v[124:127], a[128:131]\n\t" \
  "ds_read_b64_tr_b16 v[196:197], v208 offset:9216\n\t" \
  "ds_read_b64_tr_b16 v[198:199], v208 offset:13824\n\t" \
  "s_waitcnt lgkmcnt(6)\n\t" \
  "v_mfma_f32_16x16x32_bf16 a[24:27], v[200:203], v[112:115], a[24:27]\n\t" \
  "buffer_load_dwordx4 v235, s[40:43], s68 offen lds\n\t" \
  "v_mfma_f32_16x16x32_bf16 a[60:63], v[200:203], v[116:119], a[60:63]\n\t" \
  "s_add_u32 m0, s62, 4096\n\t" \
  "v_mfma_f32_16x16x32_bf16 a[96:99], v[200:203], v[120:123], a[96:99]\n\t" \
  "buffer_load_dwordx4 v236, s[40:43], s68 offen lds\n\t" \
  "v_mfma_f32_16x16x32_bf16 a[132:135], v[200:203], v[124:127], a[132:135]\n\t" \
  "ds_read_b64_tr_b16 v[200:201], v208 offset:9248\n\t" \
  "ds_read_b64_tr_b16 v[202:203], v208 offset:13856\n\t" \
  "s_waitcnt lgkmcnt(4)\n\t" \
  "v_mfma_f32_16x16x32_bf16 a[28:31], v[204:207], v[112:115], a[28:31]\n\t" \
  "s_add_u32 m0, s62, 5120\n\t" \
  "v_mfma_f32_16x16x32_bf16 a[64:67], v[204:207], v[116:119], a[64:67]\n\t" \
  "buffer_load_dwordx4 v237, s[40:43], s68 offen lds\n\t" \
  "v_mfma_f32_16x16x32_bf16 a[100:103], v[204:207], v[120:123], a[100:103]\n\t" \
  "v_mfma_f32_16x16x32_bf16 a[136:139], v[204:207], v[124:127], a[136:139]\n\t" \
  "ds_read_b64_tr_b16 v[204:205], v208 offset:9280\n\t" \
  "ds_read_b64_tr_b16 v[206:207], v208 offset:13888\n\t" \
  "v_mfma_f32_16x16x32_bf16 a[32:35], v[192:195], v[112:115], a[32:35]\n\t" \
  "s_add_u32 m0, s62, 6144\n\t" \
  "s_nop 0\n\t" \
  "buffer_load_dwordx4 v238, s[40:43], s68 offen lds\n\t" \
  "v_mfma_f32_16x16x32_bf16 a[68:71], v[192:195], v[116:119], a[68:71]\n\t" \
  "v_mfma_f32_16x16x32_bf16 a[104:107], v[192:195], v[120:123], a[104:107]\n\t" \
  "s_add_u32 m0, s62, 7168\n\t" \
  "v_mfma_f32_16x16x32_bf16 a[140:143], v[192:195], v[124:127], a[140:143]\n\t" \
  "ds_read_b64_tr_b16 v[192:193], v208 offset:9312\n\t" \
  "ds_read_b64_tr_b16 v[194:195], v208 offset:13920\n\t" \
  "s_waitcnt vmcnt(17)\n\t" \
  "s_waitcnt lgkmcnt(6)\n\t" \
  "v_mfma_f32_16x16x32_bf16 a[0:3], v[196:199], v[132:135], a[0:3]\n\t" \
  "buffer_load_dwordx4 v239, s[40:43], s68 offen lds\n\t" \
  "v_mfma_f32_16x16x32_bf16 a[36:39], v[196:199], v[136:139], a[36:39]\n\t" \
  "s_add_u32 m0, s62, 8192\n\t" \
  "v_mfma_f32_16x16x32_bf16 a[72:75], v[196:199], v[140:143], a[72:75]\n\t" \
  "buffer_load_dwordx4 v240, s[40:43], s68 offen lds\n\t" \
  "v_mfma_f32_16x16x32_bf16 a[108:111], v[196:199], v[144:147], a[108:111]\n\t" \
  "v_mfma_f32_16x16x32_bf16 a[144:147], v[196:199], v[148:151], a[144:147]\n\t" \
  "ds_read_b64_tr_b16 v[196:197], v208 offset:9344\n\t" \
  "ds_read_b64_tr_b16 v[198:199], v208 offset:13952\n\t" \
  "s_waitcnt lgkmcnt(6)\n\t" \
  "v_mfma_f32_16x16x32_bf16 a[4:7], v[200:203], v[132:135], a[4:7]\n\t" \
  "s_add_u32 s61, s68, 36864\n\t" \
  "s_add_u32 m0, s62, 9216\n\t" \
  "v_mfma_f32_16x16x32_bf16 a[40:43], v[200:203], v[136:139], a[40:43]\n\t" \
  "v_mfma_f32_16x16x32_bf16 a[76:79], v[200:203], v[140:143], a[76:79]\n\t" \
  "buffer_load_dwordx4 v232, s[40:43], s61 offen lds\n\t" \
  "v_mfma_f32_16x16x32_bf16 a[112:115], v[200:203], v[144:147], a[112:115]\n\t" \
  "s_add_u32 m0, s62, 10240\n\t" \
  "v_mfma_f32_16x16x32_bf16 a[148:151], v[200:203], v[148:151], a[148:151]\n\t" \
  "ds_read_b64_tr_b16 v[200:201], v209 offset:9376\n\t" \
  "ds_read_b64_tr_b16 v[202:203], v209 offset:13984\n\t" \
  "s_waitcnt lgkmcnt(6)\n\t" \
  "v_mfma_f32_16x16x32_bf16 a[8:11], v[204:207], v[132:135], a[8:11]\n\t" \
  "buffer_load_dwordx4 v233, s[40:43], s61 offen lds\n\t" \
  "v_mfma_f32_16x16x32_bf16 a[44:47], v[204:207], v[136:139], a[44:47]\n\t" \
  "s_add_u32 m0, s62, 11264\n\t" \
  "v_mfma_f32_16x16x32_bf16 a[80:83], v[204:207], v[140:143], a[80:83]\n\t" \
  "v_mfma_f32_16x16x32_bf16 a[116:119], v[204:207], v[144:147], a[116:119]\n\t" \
  "buffer_load_dwordx4 v234, s[40:43], s61 offen lds\n\t" \
  "v_mfma_f32_16x16x32_bf16 a[152:155], v[204:207], v[148:151], a[152:155]\n\t" \
  "ds_read_b64_tr_b16 v[204:205], v209 offset:9408\n\t" \
  "ds_read_b64_tr_b16 v[206:207], v209 offset:14016\n\t" \
  "s_waitcnt lgkmcnt(6)\n\t" \
  "v_mfma_f32_16x16x32_bf16 a[12:15], v[192:195], v[132:135], a[12:15]\n\t" \
  "s_add_u32 m0, s62, 12288\n\t" \
  "v_mfma_f32_16x16x32_bf16 a[48:51], v[192:195], v[136:139], a[48:51]\n\t" \
  "buffer_load_dwordx4 v235, s[40:43], s61 offen lds\n\t" \
  "v_mfma_f32_16x16x32_bf16 a[84:87], v[192:195], v[140:143], a[84:87]\n\t" \
  "s_add_u32 m0, s62, 13312\n\t" \
  "v_mfma_f32_16x16x32_bf16 a[120:123], v[192:195], v[144:147], a[120:123]\n\t" \
  "v_mfma_f32_16x16x32_bf16 a[156:159], v[192:195], v[148:151], a[156:159]\n\t" \
  "ds_read_b64_tr_b16 v[192:193], v209 offset:9440\n\t" \
  "ds_read_b64_tr_b16 v[194:195], v209 offset:14048\n\t" \
  "s_waitcnt lgkmcnt(6)\n\t" \
  "v_mfma_f32_16x16x32_bf16 a[16:19], v[196:199], v[132:135], a[16:19]\n\t" \
  "buffer_load_dwordx4 v236, s[40:43], s61 offen lds\n\t" \
  "s_add_u32 m0, s62, 14336\n\t" \
  "v_mfma_f32_16x16x32_bf16 a[52:55], v[196:199], v[136:139], a[52:55]\n\t" \
  "v_mfma_f32_16x16x32_bf16 a[88:91], v[196:199], v[140:143], a[88:91]\n\t" \
  "buffer_load_dwordx4 v237, s[40:43], s61 offen lds\n\t" \
  "v_mfma_f32_16x16x32_bf16 a[124:127], v[196:199], v[144:147], a[124:127]\n\t" \
  "s_add_u32 m0, s62, 15360\n\t" \
  "v_mfma_f32_16x16x32_bf16 a[160:163], v[196:199], v[148:151], a[160:163]\n\t" \
  "ds_read_b64_tr_b16 v[196:197], v209 offset:9472\n\t" \
  "ds_read_b64_tr_b16 v[198:199], v209 offset:14080\n\t" \
  "s_waitcnt lgkmcnt(4)\n\t" \
  "v_mfma_f32_16x16x32_bf16 a[20:23], v[200:203], v[132:135], a[20:23]\n\t" \
  "buffer_load_dwordx4 v238, s[40:43], s61 offen lds\n\t" \
  "v_mfma_f32_16x16x32_bf16 a[56:59], v[200:203], v[136:139], a[56:59]\n\t" \
  "s_add_u32 m0, s62, 16384\n\t" \
  "v_mfma_f32_16x16x32_bf16 a[92:95], v[200:203], v[140:143], a[92:95]\n\t" \
  "v_mfma_f32_16x16x32_bf16 a[128:131], v[200:203], v[144:147], a[128:131]\n\t" \
  "ds_read_b64_tr_b16 v[200:201], v208 offset:18432\n\t" \
  "ds_read_b64_tr_b16 v[202:203], v208 offset:23040\n\t" \
  "v_mfma_f32_16x16x32_bf16 a[24:27], v[204:207], v[132:135], a[24:27]\n\t" \
  "buffer_load_dwordx4 v239, s[40:43], s61 offen lds\n\t" \
  "s_add_u32 m0, s62, 17408\n\t" \
  "v_mfma_f32_16x16x32_bf16 a[60:63], v[204:207], v[136:139], a[60:63]\n\t" \
  "v_mfma_f32_16x16x32_bf16 a[96:99], v[204:207], v[140:143], a[96:99]\n\t" \
  "buffer_load_dwordx4 v240, s[40:43], s61 offen lds\n\t" \
  "v_mfma_f32_16x16x32_bf16 a[132:135], v[204:207], v[144:147], a[132:135]\n\t" \
  "ds_read_b64_tr_b16 v[204:205], v208 offset:18464\n\t" \
  "ds_read_b64_tr_b16 v[206:207], v208 offset:23072\n\t" \
  "s_waitcnt lgkmcnt(5)\n\t" \
  "v_mfma_f32_16x16x32_bf16 a[28:31], v[192:195], v[132:135], a[28:31]\n\t" \
  "v_mfma_f32_16x16x32_bf16 a[64:67], v[192:195], v[136:139], a[64:67]\n\t" \
  "s_add_u32 s80, s83, s69\n\t" \
  "v_mfma_f32_16x16x32_bf16 a[100:103], v[192:195], v[140:143], a[100:103]\n\t" \
  "v_mfma_f32_16x16x32_bf16 a[136:139], v[192:195], v[144:147], a[136:139]\n\t" \
  "ds_read_b64_tr_b16 v[192:193], v208 offset:18496\n\t" \
  "ds_read_b64_tr_b16 v[194:195], v208 offset:23104\n\t" \
  "s_waitcnt lgkmcnt(6)\n\t" \
  "v_mfma_f32_16x16x32_bf16 a[32:35], v[196:199], v[132:135], a[32:35]\n\t" \
  "s_add_u32 s81, s83, s69\n\t" \
  "v_mfma_f32_16x16x32_bf16 a[68:71], v[196:199], v[136:139], a[68:71]\n\t" \
  "v_mfma_f32_16x16x32_bf16 a[104:107], v[196:199], v[140:143], a[104:107]\n\t" \
  "s_add_u32 s81, s81, 131072\n\t" \
  "v_mfma_f32_16x16x32_bf16 a[140:143], v[196:199], v[144:147], a[140:143]\n\t" \
  "ds_read_b64_tr_b16 v[196:197], v208 offset:18528\n\t" \
  "ds_read_b64_tr_b16 v[198:199], v208 offset:23136\n\t" \
  "s_waitcnt vmcnt(23)\n\t" \
  "s_waitcnt lgkmcnt(6)\n\t" \
  "v_mfma_f32_16x16x32_bf16 a[0:3], v[200:203], v[152:155], a[0:3]\n\t" \
  "s_add_u32 s82, s83, s69\n\t" \
  "v_mfma_f32_16x16x32_bf16 a[36:39], v[200:203], v[156:159], a[36:39]\n\t" \
  "v_mfma_f32_16x16x32_bf16 a[72:75], v[200:203], v[160:163], a[72:75]\n\t" \
  "v_mfma_f32_16x16x32_bf16 a[108:111], v[200:203], v[164:167], a[108:111]\n\t" \
  "s_add_u32 s82, s82, 262144\n\t" \
  "v_mfma_f32_16x16x32_bf16 a[144:147], v[200:203], v[168:171], a[144:147]\n\t" \
  "ds_read_b64_tr_b16 v[200:201], v208 offset:18560\n\t" \
  "ds_read_b64_tr_b16 v[202:203], v208 offset:23168\n\t" \
  "s_waitcnt lgkmcnt(6)\n\t" \
  "v_mfma_f32_16x16x32_bf16 a[4:7], v[204:207], v[152:155], a[4:7]\n\t" \
  "s_add_u32 s87, s83, s69\n\t" \
  "v_mfma_f32_16x16x32_bf16 a[40:43], v[204:207], v[156:159], a[40:43]\n\t" \
  "v_mfma_f32_16x16x32_bf16 a[76:79], v[204:207], v[160:163], a[76:79]\n\t" \
  "v_mfma_f32_16x16x32_bf16 a[112:115], v[204:207], v[164:167], a[112:115]\n\t" \
  "s_add_u32 s87, s87, 393216\n\t" \
  "v_mfma_f32_16x16x32_bf16 a[148:151], v[204:207], v[168:171], a[148:151]\n\t" \
  "ds_read_b64_tr_b16 v[204:205], v209 offset:18592\n\t" \
  "ds_read_b64_tr_b16 v[206:207], v209 offset:23200\n\t" \
  "s_waitcnt lgkmcnt(6)\n\t" \
  "v_mfma_f32_16x16x32_bf16 a[8:11], v[192:195], v[152:155], a[8:11]\n\t" \
  "s_add_u32 s95, s75, s69\n\t" \
  "v_mfma_f32_16x16x32_bf16 a[44:47], v[192:195], v[156:159], a[44:47]\n\t" \
  "v_mfma_f32_16x16x32_bf16 a[80:83], v[192:195], v[160:163], a[80:83]\n\t" \
  "v_mfma_f32_16x16x32_bf16 a[116:119], v[192:195], v[164:167], a[116:119]\n\t" \
  "buffer_load_dwordx4 v[32:35], v244, s[56:59], s80 offen sc1\n\t" \
  "v_mfma_f32_16x16x32_bf16 a[152:155], v[192:195], v[168:171], a[152:155]\n\t" \
  "ds_read_b64_tr_b16 v[192:193], v209 offset:18624\n\t" \
  "ds_read_b64_tr_b16 v[194:195], v209 offset:23232\n\t" \
  "s_waitcnt lgkmcnt(6)\n\t" \
  "v_mfma_f32_16x16x32_bf16 a[12:15], v[196:199], v[152:155], a[12:15]\n\t" \
  "buffer_load_dwordx4 v[36:39], v244, s[56:59], s81 offen sc1\n\t" \
  "v_mfma_f32_16x16x32_bf16 a[48:51], v[196:199], v[156:159], a[48:51]\n\t" \
  "v_mfma_f32_16x16x32_bf16 a[84:87], v[196:199], v[160:163], a[84:87]\n\t" \
  "v_mfma_f32_16x16x32_bf16 a[120:123], v[196:199], v[164:167], a[120:123]\n\t" \
  "buffer_load_dwordx4 v[40:43], v244, s[56:59], s82 offen sc1\n\t" \
  "v_mfma_f32_16x16x32_bf16 a[156:159], v[196:199], v[168:171], a[156:159]\n\t" \
  "ds_read_b64_tr_b16 v[196:197], v209 offset:18656\n\t" \
  "ds_read_b64_tr_b16 v[198:199], v209 offset:23264\n\t" \
  "s_waitcnt lgkmcnt(6)\n\t" \
  "v_mfma_f32_16x16x32_bf16 a[16:19], v[200:203], v[152:155], a[16:19]\n\t" \
  "buffer_load_dwordx4 v[44:47], v244, s[56:59], s87 offen sc1\n\t" \
  "v_mfma_f32_16x16x32_bf16 a[52:55], v[200:203], v[156:159], a[52:55]\n\t" \
  "v_mfma_f32_16x16x32_bf16 a[88:91], v[200:203], v[160:163], a[88:91]\n\t" \
  "v_mfma_f32_16x16x32_bf16 a[124:127], v[200:203], v[164:167], a[124:127]\n\t" \
  "buffer_load_dwordx4 v[48:51], v244, s[56:59], s95 offen sc1\n\t" \
  "v_mfma_f32_16x16x32_bf16 a[160:163], v[200:203], v[168:171], a[160:163]\n\t" \
  "ds_read_b64_tr_b16 v[200:201], v209 offset:18688\n\t" \
  "ds_read_b64_tr_b16 v[202:203], v209 offset:23296\n\t" \
  "s_waitcnt lgkmcnt(4)\n\t" \
  "v_mfma_f32_16x16x32_bf16 a[20:23], v[204:207], v[152:155], a[20:23]\n\t" \
  "buffer_load_dwordx4 v[52:55], v244, s[56:59], s80 offen offset:1024 sc1\n\t" \
  "v_mfma_f32_16x16x32_bf16 a[56:59], v[204:207], v[156:159], a[56:59]\n\t" \
  "v_mfma_f32_16x16x32_bf16 a[92:95], v[204:207], v[160:163], a[92:95]\n\t" \
  "v_mfma_f32_16x16x32_bf16 a[128:131], v[204:207], v[164:167], a[128:131]\n\t" \
  "ds_read_b64_tr_b16 v[204:205], v208 offset:27648\n\t" \
  "ds_read_b64_tr_b16 v[206:207], v208 offset:32256\n\t" \
  "v_mfma_f32_16x16x32_bf16 a[24:27], v[192:195], v[152:155], a[24:27]\n\t" \
  "buffer_load_dwordx4 v[56:59], v244, s[56:59], s81 offen offset:1024 sc1\n\t" \
  "v_mfma_f32_16x16x32_bf16 a[60:63], v[192:195], v[156:159], a[60:63]\n\t" \
  "buffer_load_dwordx4 v[60:63], v244, s[56:59], s82 offen offset:1024 sc1\n\t" \
  "v_mfma_f32_16x16x32_bf16 a[96:99], v[192:195], v[160:163], a[96:99]\n\t" \
  "v_mfma_f32_16x16x32_bf16 a[132:135], v[192:195], v[164:167], a[132:135]\n\t" \
  "ds_read_b64_tr_b16 v[192:193], v208 offset:27680\n\t" \
  "ds_read_b64_tr_b16 v[194:195], v208 offset:32288\n\t" \
  "s_waitcnt lgkmcnt(6)\n\t" \
  "v_mfma_f32_16x16x32_bf16 a[28:31], v[196:199], v[152:155], a[28:31]\n\t" \
  "buffer_load_dwordx4 v[64:67], v244, s[56:59], s87 offen offset:1024 sc1\n\t" \
  "v_mfma_f32_16x16x32_bf16 a[64:67], v[196:199], v[156:159], a[64:67]\n\t" \
  "v_mfma_f32_16x16x32_bf16 a[100:103], v[196:199], v[160:163], a[100:103]\n\t" \
  "buffer_load_dwordx4 v[68:71], v244, s[56:59], s95 offen offset:1024 sc1\n\t" \
  "v_mfma_f32_16x16x32_bf16 a[136:139], v[196:199], v[164:167], a[136:139]\n\t" \
  "ds_read_b64_tr_b16 v[196:197], v208 offset:27712\n\t" \
  "ds_read_b64_tr_b16 v[198:199], v208 offset:32320\n\t" \
  "s_waitcnt lgkmcnt(6)\n\t" \
  "v_mfma_f32_16x16x32_bf16 a[32:35], v[200:203], v[152:155], a[32:35]\n\t" \
  "v_mfma_f32_16x16x32_bf16 a[68:71], v[200:203], v[156:159], a[68:71]\n\t" \
  "buffer_load_dwordx4 v[72:75], v244, s[56:59], s80 offen offset:2048 sc1\n\t" \
  "v_mfma_f32_16x16x32_bf16 a[104:107], v[200:203], v[160:163], a[104:107]\n\t" \
  "v_mfma_f32_16x16x32_bf16 a[140:143], v[200:203], v[164:167], a[140:143]\n\t" \
  "ds_read_b64_tr_b16 v[200:201], v208 offset:27744\n\t" \
  "ds_read_b64_tr_b16 v[202:203], v208 offset:32352\n\t" \
  "s_waitcnt vmcnt(29)\n\t" \
  "s_waitcnt lgkmcnt(6)\n\t" \
  "v_mfma_f32_16x16x32_bf16 a[0:3], v[204:207], v[172:175], a[0:3]\n\t" \
  "buffer_load_dwordx4 v[76:79], v244, s[56:59], s81 offen offset:2048 sc1\n\t" \
  "v_mfma_f32_16x16x32_bf16 a[36:39], v[204:207], v[176:179], a[36:39]\n\t" \
  "v_mfma_f32_16x16x32_bf16 a[72:75], v[204:207], v[180:183], a[72:75]\n\t" \
  "buffer_load_dwordx4 v[80:83], v244, s[56:59], s82 offen offset:2048 sc1\n\t" \
  "v_mfma_f32_16x16x32_bf16 a[108:111], v[204:207], v[184:187], a[108:111]\n\t" \
  "v_mfma_f32_16x16x32_bf16 a[144:147], v[204:207], v[188:191], a[144:147]\n\t" \
  "ds_read_b64_tr_b16 v[204:205], v208 offset:27776\n\t" \
  "ds_read_b64_tr_b16 v[206:207], v208 offset:32384\n\t" \
  "s_waitcnt lgkmcnt(6)\n\t" \
  "v_mfma_f32_16x16x32_bf16 a[4:7], v[192:195], v[172:175], a[4:7]\n\t" \
  "buffer_load_dwordx4 v[84:87], v244, s[56:59], s87 offen offset:2048 sc1\n\t" \
  "v_mfma_f32_16x16x32_bf16 a[40:43], v[192:195], v[176:179], a[40:43]\n\t" \
  "v_mfma_f32_16x16x32_bf16 a[76:79], v[192:195], v[180:183], a[76:79]\n\t" \
  "buffer_load_dwordx4 v[88:91], v244, s[56:59], s95 offen offset:2048 sc1\n\t" \
  "v_mfma_f32_16x16x32_bf16 a[112:115], v[192:195], v[184:187], a[112:115]\n\t" \
  "v_mfma_f32_16x16x32_bf16 a[148:151], v[192:195], v[188:191], a[148:151]\n\t" \
  "ds_read_b64_tr_b16 v[192:193], v209 offset:27808\n\t" \
  "ds_read_b64_tr_b16 v[194:195], v209 offset:32416\n\t" \
  "s_waitcnt lgkmcnt(6)\n\t" \
  "v_mfma_f32_16x16x32_bf16 a[8:11], v[196:199], v[172:175], a[8:11]\n\t" \
  "buffer_load_dwordx4 v[92:95], v244, s[56:59], s80 offen offset:3072 sc1\n\t" \
  "v_mfma_f32_16x16x32_bf16 a[44:47], v[196:199], v[176:179], a[44:47]\n\t" \
  "v_mfma_f32_16x16x32_bf16 a[80:83], v[196:199], v[180:183], a[80:83]\n\t" \
  "buffer_load_dwordx4 v[96:99], v244, s[56:59], s81 offen offset:3072 sc1\n\t" \
  "v_mfma_f32_16x16x32_bf16 a[116:119], v[196:199], v[184:187], a[116:119]\n\t" \
  "v_mfma_f32_16x16x32_bf16 a[152:155], v[196:199], v[188:191], a[152:155]\n\t" \
  "ds_read_b64_tr_b16 v[196:197], v209 offset:27840\n\t" \
  "ds_read_b64_tr_b16 v[198:199], v209 offset:32448\n\t" \
  "s_waitcnt lgkmcnt(6)\n\t" \
  "v_mfma_f32_16x16x32_bf16 a[12:15], v[200:203], v[172:175], a[12:15]\n\t" \
  "buffer_load_dwordx4 v[100:103], v244, s[56:59], s82 offen offset:3072 sc1\n\t" \
  "v_mfma_f32_16x16x32_bf16 a[48:51], v[200:203], v[176:179], a[48:51]\n\t" \
  "v_mfma_f32_16x16x32_bf16 a[84:87], v[200:203], v[180:183], a[84:87]\n\t" \
  "buffer_load_dwordx4 v[104:107], v244, s[56:59], s87 offen offset:3072 sc1\n\t" \
  "v_mfma_f32_16x16x32_bf16 a[120:123], v[200:203], v[184:187], a[120:123]\n\t" \
  "v_mfma_f32_16x16x32_bf16 a[156:159], v[200:203], v[188:191], a[156:159]\n\t" \
  "ds_read_b64_tr_b16 v[200:201], v209 offset:27872\n\t" \
  "ds_read_b64_tr_b16 v[202:203], v209 offset:32480\n\t" \
  "s_waitcnt lgkmcnt(4)\n\t" \
  "v_mfma_f32_16x16x32_bf16 a[16:19], v[204:207], v[172:175], a[16:19]\n\t" \
  "buffer_load_dwordx4 v[108:111], v244, s[56:59], s95 offen offset:3072 sc1\n\t" \
  "v_mfma_f32_16x16x32_bf16 a[52:55], v[204:207], v[176:179], a[52:55]\n\t" \
  "v_mfma_f32_16x16x32_bf16 a[88:91], v[204:207], v[180:183], a[88:91]\n\t" \
  "v_mfma_f32_16x16x32_bf16 a[124:127], v[204:207], v[184:187], a[124:127]\n\t" \
  "v_mfma_f32_16x16x32_bf16 a[160:163], v[204:207], v[188:191], a[160:163]\n\t" \
  "ds_read_b64_tr_b16 v[204:205], v209 offset:27904\n\t" \
  "ds_read_b64_tr_b16 v[206:207], v209 offset:32512\n\t" \
  "v_mfma_f32_16x16x32_bf16 a[20:23], v[192:195], v[172:175], a[20:23]\n\t" \
  "v_mfma_f32_16x16x32_bf16 a[56:59], v[192:195], v[176:179], a[56:59]\n\t" \
  "v_mfma_f32_16x16x32_bf16 a[92:95], v[192:195], v[180:183], a[92:95]\n\t" \
  "v_mfma_f32_16x16x32_bf16 a[128:131], v[192:195], v[184:187], a[128:131]\n\t" \
  "s_waitcnt lgkmcnt(4)\n\t" \
  "v_mfma_f32_16x16x32_bf16 a[24:27], v[196:199], v[172:175], a[24:27]\n\t" \
  "v_mfma_f32_16x16x32_bf16 a[60:63], v[196:199], v[176:179], a[60:63]\n\t" \
  "v_mfma_f32_16x16x32_bf16 a[96:99], v[196:199], v[180:183], a[96:99]\n\t" \
  "v_mfma_f32_16x16x32_bf16 a[132:135], v[196:199], v[184:187], a[132:135]\n\t" \
  "s_waitcnt lgkmcnt(2)\n\t" \
  "v_mfma_f32_16x16x32_bf16 a[28:31], v[200:203], v[172:175], a[28:31]\n\t" \
  "v_mfma_f32_16x16x32_bf16 a[64:67], v[200:203], v[176:179], a[64:67]\n\t" \
  "v_mfma_f32_16x16x32_bf16 a[100:103], v[200:203], v[180:183], a[100:103]\n\t" \
  "v_mfma_f32_16x16x32_bf16 a[136:139], v[200:203], v[184:187], a[136:139]\n\t" \
  "s_waitcnt lgkmcnt(0)\n\t" \
  "v_mfma_f32_16x16x32_bf16 a[32:35], v[204:207], v[172:175], a[32:35]\n\t" \
  "v_mfma_f32_16x16x32_bf16 a[68:71], v[204:207], v[176:179], a[68:71]\n\t" \
  "v_mfma_f32_16x16x32_bf16 a[104:107], v[204:207], v[180:183], a[104:107]\n\t" \
  "v_mfma_f32_16x16x32_bf16 a[140:143], v[204:207], v[184:187], a[140:143]\n\t" \
  "s_waitcnt vmcnt(20)\n\t" \
  "s_barrier\n\t" \
  "v_add_u32 v208, 0, v210\n\t" \
  "v_add_u32 v209, 0, v211\n\t" \
  "ds_read_b64_tr_b16 v[192:193], v208 offset:0\n\t" \
  "ds_read_b64_tr_b16 v[194:195], v208 offset:4608\n\t" \
  "ds_read_b64_tr_b16 v[196:197], v208 offset:32\n\t" \
  "ds_read_b64_tr_b16 v[198:199], v208 offset:4640\n\t" \
  "ds_read_b64_tr_b16 v[200:201], v208 offset:64\n\t" \
  "ds_read_b64_tr_b16 v[202:203], v208 offset:4672\n\t" \
  "ds_read_b64_tr_b16 v[204:205], v208 offset:96\n\t" \
  "ds_read_b64_tr_b16 v[206:207], v208 offset:4704\n\t" \
  "s_waitcnt vmcnt(15)\n\t" \
  "s_waitcnt lgkmcnt(6)\n\t" \
  "v_mfma_f32_16x16x32_bf16 a[0:3], v[192:195], v[32:35], a[0:3]\n\t" \
  "s_add_u32 s72, s72, 1\n\t" \
  "v_mfma_f32_16x16x32_bf16 a[36:39], v[192:195], v[36:39], a[36:39]\n\t" \
  "s_and_b32 s72, s72, 3\n\t" \
  "v_mfma_f32_16x16x32_bf16 a[72:75], v[192:195], v[40:43], a[72:75]\n\t" \
  "v_mfma_f32_16x16x32_bf16 a[108:111], v[192:195], v[44:47], a[108:111]\n\t" \
  "s_lshl_b32 s61, s64, 2\n\t" \
  "v_mfma_f32_16x16x32_bf16 a[144:147], v[192:195], v[48:51], a[144:147]\n\t" \
  "ds_read_b64_tr_b16 v[192:193], v208 offset:128\n\t" \
  "ds_read_b64_tr_b16 v[194:195], v208 offset:4736\n\t" \
  "s_waitcnt lgkmcnt(6)\n\t" \
  "v_mfma_f32_16x16x32_bf16 a[4:7], v[196:199], v[32:35], a[4:7]\n\t" \
  "s_add_u32 s61, s61, s72\n\t" \
  "v_mfma_f32_16x16x32_bf16 a[40:43], v[196:199], v[36:39], a[40:43]\n\t" \
  "s_lshl_b32 s61, s61, 1\n\t" \
  "v_mfma_f32_16x16x32_bf16 a[76:79], v[196:199], v[40:43], a[76:79]\n\t" \
  "s_add_u32 s61, s61, s66\n\t" \
  "v_mfma_f32_16x16x32_bf16 a[112:115], v[196:199], v[44:47], a[112:115]\n\t" \
  "v_mfma_f32_16x16x32_bf16 a[148:151], v[196:199], v[48:51], a[148:151]\n\t" \
  "ds_read_b64_tr_b16 v[196:197], v209 offset:160\n\t" \
  "ds_read_b64_tr_b16 v[198:199], v209 offset:4768\n\t" \
  "s_waitcnt lgkmcnt(6)\n\t" \
  "v_mfma_f32_16x16x32_bf16 a[8:11], v[200:203], v[32:35], a[8:11]\n\t" \
  "s_mul_i32 s68, s61, 147456\n\t" \
  "s_mul_i32 s61, s85, 73728\n\t" \
  "v_mfma_f32_16x16x32_bf16 a[44:47], v[200:203], v[36:39], a[44:47]\n\t" \
  "v_mfma_f32_16x16x32_bf16 a[80:83], v[200:203], v[40:43], a[80:83]\n\t" \
  "s_add_u32 s68, s68, s61\n\t" \
  "v_mfma_f32_16x16x32_bf16 a[116:119], v[200:203], v[44:47], a[116:119]\n\t" \
  "s_sub_i32 s69, s72, s65\n\t" \
  "v_mfma_f32_16x16x32_bf16 a[152:155], v[200:203], v[48:51], a[152:155]\n\t" \
  "ds_read_b64_tr_b16 v[200:201], v209 offset:192\n\t" \
  "ds_read_b64_tr_b16 v[202:203], v209 offset:4800\n\t" \
  "s_waitcnt lgkmcnt(6)\n\t" \
  "v_mfma_f32_16x16x32_bf16 a[12:15], v[204:207], v[32:35], a[12:15]\n\t" \
  "s_lshl_b32 s69, s69, 13\n\t" \
  "v_mfma_f32_16x16x32_bf16 a[48:51], v[204:207], v[36:39], a[48:51]\n\t" \
  "s_add_u32 s62, s67, 73728\n\t" \
  "v_mfma_f32_16x16x32_bf16 a[84:87], v[204:207], v[40:43], a[84:87]\n\t" \
  "v_mfma_f32_16x16x32_bf16 a[120:123], v[204:207], v[44:47], a[120:123]\n\t" \
  "s_add_u32 m0, s62, 0\n\t" \
  "v_mfma_f32_16x16x32_bf16 a[156:159], v[204:207], v[48:51], a[156:159]\n\t" \
  "ds_read_b64_tr_b16 v[204:205], v209 offset:224\n\t" \
  "ds_read_b64_tr_b16 v[206:207], v209 offset:4832\n\t" \
  "s_waitcnt lgkmcnt(6)\n\t" \
  "v_mfma_f32_16x16x32_bf16 a[16:19], v[192:195], v[32:35], a[16:19]\n\t" \
  "buffer_load_dwordx4 v232, s[40:43], s68 offen lds\n\t" \
  "v_mfma_f32_16x16x32_bf16 a[52:55], v[192:195], v[36:39], a[52:55]\n\t" \
  "s_add_u32 m0, s62, 1024\n\t" \
  "v_mfma_f32_16x16x32_bf16 a[88:91], v[192:195], v[40:43], a[88:91]\n\t" \
  "buffer_load_dwordx4 v233, s[40:43], s68 offen lds\n\t" \
  "v_mfma_f32_16x16x32_bf16 a[124:127], v[192:195], v[44:47], a[124:127]\n\t" \
  "v_mfma_f32_16x16x32_bf16 a[160:163], v[192:195], v[48:51], a[160:163]\n\t" \
  "ds_read_b64_tr_b16 v[192:193], v209 offset:256\n\t" \
  "ds_read_b64_tr_b16 v[194:195], v209 offset:4864\n\t" \
  "s_waitcnt lgkmcnt(6)\n\t" \
  "v_mfma_f32_16x16x32_bf16 a[20:23], v[196:199], v[32:35], a[20:23]\n\t" \
  "s_add_u32 m0, s62, 2048\n\t" \
  "s_nop 0\n\t" \
  "buffer_load_dwordx4 v234, s[40:43], s68 offen lds\n\t" \
  "v_mfma_f32_16x16x32_bf16 a[56:59], v[196:199], v[36:39], a[56:59]\n\t" \
  "v_mfma_f32_16x16x32_bf16 a[92:95], v[196:199], v[40:43], a[92:95]\n\t" \
  "s_add_u32 m0, s62, 3072\n\t" \
  "v_mfma_f32_16x16x32_bf16 a[128:131], v[196:199], v[44:47], a[128:131]\n\t" \
  "ds_read_b64_tr_b16 v[196:197], v208 offset:9216\n\t" \
  "ds_read_b64_tr_b16 v[198:199], v208 offset:13824\n\t" \
  "s_waitcnt lgkmcnt(6)\n\t" \
  "v_mfma_f32_16x16x32_bf16 a[24:27], v[200:203], v[32:35], a[24:27]\n\t" \
  "buffer_load_dwordx4 v235, s[40:43], s68 offen lds\n\t" \
  "v_mfma_f32_16x16x32_bf16 a[60:63], v[200:203], v[36:39], a[60:63]\n\t" \
  "s_add_u32 m0, s62, 4096\n\t" \
  "v_mfma_f32_16x16x32_bf16 a[96:99], v[200:203], v[40:43], a[96:99]\n\t" \
  "buffer_load_dwordx4 v236, s[40:43], s68 offen lds\n\t" \
  "v_mfma_f32_16x16x32_bf16 a[132:135], v[200:203], v[44:47], a[132:135]\n\t" \
  "ds_read_b64_tr_b16 v[200:201], v208 offset:9248\n\t" \
  "ds_read_b64_tr_b16 v[202:203], v208 offset:13856\n\t" \
  "s_waitcnt lgkmcnt(4)\n\t" \
  "v_mfma_f32_16x16x32_bf16 a[28:31], v[204:207], v[32:35], a[28:31]\n\t" \
  "s_add_u32 m0, s62, 5120\n\t" \
  "v_mfma_f32_16x16x32_bf16 a[64:67], v[204:207], v[36:39], a[64:67]\n\t" \
  "buffer_load_dwordx4 v237, s[40:43], s68 offen lds\n\t" \
  "v_mfma_f32_16x16x32_bf16 a[100:103], v[204:207], v[40:43], a[100:103]\n\t" \
  "v_mfma_f32_16x16x32_bf16 a[136:139], v[204:207], v[44:47], a[136:139]\n\t" \
  "ds_read_b64_tr_b16 v[204:205], v208 offset:9280\n\t" \
  "ds_read_b64_tr_b16 v[206:207], v208 offset:13888\n\t" \
  "v_mfma_f32_16x16x32_bf16 a[32:35], v[192:195], v[32:35], a[32:35]\n\t" \
  "s_add_u32 m0, s62, 6144\n\t" \
  "s_nop 0\n\t" \
  "buffer_load_dwordx4 v238, s[40:43], s68 offen lds\n\t" \
  "v_mfma_f32_16x16x32_bf16 a[68:71], v[192:195], v[36:39], a[68:71]\n\t" \
  "v_mfma_f32_16x16x32_bf16 a[104:107], v[192:195], v[40:43], a[104:107]\n\t" \
  "s_add_u32 m0, s62, 7168\n\t" \
  "v_mfma_f32_16x16x32_bf16 a[140:143], v[192:195], v[44:47], a[140:143]\n\t" \
  "ds_read_b64_tr_b16 v[192:193], v208 offset:9312\n\t" \
  "ds_read_b64_tr_b16 v[194:195], v208 offset:13920\n\t" \
  "s_waitcnt vmcnt(17)\n\t" \
  "s_waitcnt lgkmcnt(6)\n\t" \
  "v_mfma_f32_16x16x32_bf16 a[0:3], v[196:199], v[52:55], a[0:3]\n\t" \
  "buffer_load_dwordx4 v239, s[40:43], s68 offen lds\n\t" \
  "v_mfma_f32_16x16x32_bf16 a[36:39], v[196:199], v[56:59], a[36:39]\n\t" \
  "s_add_u32 m0, s62, 8192\n\t" \
  "v_mfma_f32_16x16x32_bf16 a[72:75], v[196:199], v[60:63], a[72:75]\n\t" \
  "buffer_load_dwordx4 v240, s[40:43], s68 offen lds\n\t" \
  "v_mfma_f32_16x16x32_bf16 a[108:111], v[196:199], v[64:67], a[108:111]\n\t" \
  "v_mfma_f32_16x16x32_bf16 a[144:147], v[196:199], v[68:71], a[144:147]\n\t" \
  "ds_read_b64_tr_b16 v[196:197], v208 offset:9344\n\t" \
  "ds_read_b64_tr_b16 v[198:199], v208 offset:13952\n\t" \
  "s_waitcnt lgkmcnt(6)\n\t" \
  "v_mfma_f32_16x16x32_bf16 a[4:7], v[200:203], v[52:55], a[4:7]\n\t" \
  "s_add_u32 s61, s68, 36864\n\t" \
  "s_add_u32 m0, s62, 9216\n\t" \
  "v_mfma_f32_16x16x32_bf16 a[40:43], v[200:203], v[56:59], a[40:43]\n\t" \
  "v_mfma_f32_16x16x32_bf16 a[76:79], v[200:203], v[60:63], a[76:79]\n\t" \
  "buffer_load_dwordx4 v232, s[40:43], s61 offen lds\n\t" \
  "v_mfma_f32_16x16x32_bf16 a[112:115], v[200:203], v[64:67], a[112:115]\n\t" \
  "s_add_u32 m0, s62, 10240\n\t" \
  "v_mfma_f32_16x16x32_bf16 a[148:151], v[200:203], v[68:71], a[148:151]\n\t" \
  "ds_read_b64_tr_b16 v[200:201], v209 offset:9376\n\t" \
  "ds_read_b64_tr_b16 v[202:203], v209 offset:13984\n\t" \
  "s_waitcnt lgkmcnt(6)\n\t" \
  "v_mfma_f32_16x16x32_bf16 a[8:11], v[204:207], v[52:55], a[8:11]\n\t" \
  "buffer_load_dwordx4 v233, s[40:43], s61 offen lds\n\t" \
  "v_mfma_f32_16x16x32_bf16 a[44:47], v[204:207], v[56:59], a[44:47]\n\t" \
  "s_add_u32 m0, s62, 11264\n\t" \
  "v_mfma_f32_16x16x32_bf16 a[80:83], v[204:207], v[60:63], a[80:83]\n\t" \
  "v_mfma_f32_16x16x32_bf16 a[116:119], v[204:207], v[64:67], a[116:119]\n\t" \
  "buffer_load_dwordx4 v234, s[40:43], s61 offen lds\n\t" \
  "v_mfma_f32_16x16x32_bf16 a[152:155], v[204:207], v[68:71], a[152:155]\n\t" \
  "ds_read_b64_tr_b16 v[204:205], v209 offset:9408\n\t" \
  "ds_read_b64_tr_b16 v[206:207], v209 offset:14016\n\t" \
  "s_waitcnt lgkmcnt(6)\n\t" \
  "v_mfma_f32_16x16x32_bf16 a[12:15], v[192:195], v[52:55], a[12:15]\n\t" \
  "s_add_u32 m0, s62, 12288\n\t" \
  "v_mfma_f32_16x16x32_bf16 a[48:51], v[192:195], v[56:59], a[48:51]\n\t" \
  "buffer_load_dwordx4 v235, s[40:43], s61 offen lds\n\t" \
  "v_mfma_f32_16x16x32_bf16 a[84:87], v[192:195], v[60:63], a[84:87]\n\t" \
  "s_add_u32 m0, s62, 13312\n\t" \
  "v_mfma_f32_16x16x32_bf16 a[120:123], v[192:195], v[64:67], a[120:123]\n\t" \
  "v_mfma_f32_16x16x32_bf16 a[156:159], v[192:195], v[68:71], a[156:159]\n\t" \
  "ds_read_b64_tr_b16 v[192:193], v209 offset:9440\n\t" \
  "ds_read_b64_tr_b16 v[194:195], v209 offset:14048\n\t" \
  "s_waitcnt lgkmcnt(6)\n\t" \
  "v_mfma_f32_16x16x32_bf16 a[16:19], v[196:199], v[52:55], a[16:19]\n\t" \
  "buffer_load_dwordx4 v236, s[40:43], s61 offen lds\n\t" \
  "s_add_u32 m0, s62, 14336\n\t" \
  "v_mfma_f32_16x16x32_bf16 a[52:55], v[196:199], v[56:59], a[52:55]\n\t" \
  "v_mfma_f32_16x16x32_bf16 a[88:91], v[196:199], v[60:63], a[88:91]\n\t" \
  "buffer_load_dwordx4 v237, s[40:43], s61 offen lds\n\t" \
  "v_mfma_f32_16x16x32_bf16 a[124:127], v[196:199], v[64:67], a[124:127]\n\t" \
  "s_add_u32 m0, s62, 15360\n\t" \
  "v_mfma_f32_16x16x32_bf16 a[160:163], v[196:199], v[68:71], a[160:163]\n\t" \
  "ds_read_b64_tr_b16 v[196:197], v209 offset:9472\n\t" \
  "ds_read_b64_tr_b16 v[198:199], v209 offset:14080\n\t" \
  "s_waitcnt lgkmcnt(4)\n\t" \
  "v_mfma_f32_16x16x32_bf16 a[20:23], v[200:203], v[52:55], a[20:23]\n\t" \
  "buffer_load_dwordx4 v238, s[40:43], s61 offen lds\n\t" \
  "v_mfma_f32_16x16x32_bf16 a[56:59], v[200:203], v[56:59], a[56:59]\n\t" \
  "s_add_u32 m0, s62, 16384\n\t" \
  "v_mfma_f32_16x16x32_bf16 a[92:95], v[200:203], v[60:63], a[92:95]\n\t" \
  "v_mfma_f32_16x16x32_bf16 a[128:131], v[200:203], v[64:67], a[128:131]\n\t" \
  "ds_read_b64_tr_b16 v[200:201], v208 offset:18432\n\t" \
  "ds_read_b64_tr_b16 v[202:203], v208 offset:23040\n\t" \
  "v_mfma_f32_16x16x32_bf16 a[24:27], v[204:207], v[52:55], a[24:27]\n\t" \
  "buffer_load_dwordx4 v239, s[40:43], s61 offen lds\n\t" \
  "s_add_u32 m0, s62, 17408\n\t" \
  "v_mfma_f32_16x16x32_bf16 a[60:63], v[204:207], v[56:59], a[60:63]\n\t" \
  "v_mfma_f32_16x16x32_bf16 a[96:99], v[204:207], v[60:63], a[96:99]\n\t" \
  "buffer_load_dwordx4 v240, s[40:43], s61 offen lds\n\t" \
  "v_mfma_f32_16x16x32_bf16 a[132:135], v[204:207], v[64:67], a[132:135]\n\t" \
  "ds_read_b64_tr_b16 v[204:205], v208 offset:18464\n\t" \
  "ds_read_b64_tr_b16 v[206:207], v208 offset:23072\n\t" \
  "s_waitcnt lgkmcnt(5)\n\t" \
  "v_mfma_f32_16x16x32_bf16 a[28:31], v[192:195], v[52:55], a[28:31]\n\t" \
  "v_mfma_f32_16x16x32_bf16 a[64:67], v[192:195], v[56:59], a[64:67]\n\t" \
  "s_add_u32 s80, s83, s69\n\t" \
  "v_mfma_f32_16x16x32_bf16 a[100:103], v[192:195], v[60:63], a[100:103]\n\t" \
  "v_mfma_f32_16x16x32_bf16 a[136:139], v[192:195], v[64:67], a[136:139]\n\t" \
  "ds_read_b64_tr_b16 v[192:193], v208 offset:18496\n\t" \
  "ds_read_b64_tr_b16 v[194:195], v208 offset:23104\n\t" \
  "s_waitcnt lgkmcnt(6)\n\t" \
  "v_mfma_f32_16x16x32_bf16 a[32:35], v[196:199], v[52:55], a[32:35]\n\t" \
  "s_add_u32 s81, s83, s69\n\t" \
  "v_mfma_f32_16x16x32_bf16 a[68:71], v[196:199], v[56:59], a[68:71]\n\t" \
  "v_mfma_f32_16x16x32_bf16 a[104:107], v[196:199], v[60:63], a[104:107]\n\t" \
  "s_add_u32 s81, s81, 131072\n\t" \
  "v_mfma_f32_16x16x32_bf16 a[140:143], v[196:199], v[64:67], a[140:143]\n\t" \
  "ds_read_b64_tr_b16 v[196:197], v208 offset:18528\n\t" \
  "ds_read_b64_tr_b16 v[198:199], v208 offset:23136\n\t" \
  "s_waitcnt vmcnt(23)\n\t" \
  "s_waitcnt lgkmcnt(6)\n\t" \
  "v_mfma_f32_16x16x32_bf16 a[0:3], v[200:203], v[72:75], a[0:3]\n\t" \
  "s_add_u32 s82, s83, s69\n\t" \
  "v_mfma_f32_16x16x32_bf16 a[36:39], v[200:203], v[76:79], a[36:39]\n\t" \
  "v_mfma_f32_16x16x32_bf16 a[72:75], v[200:203], v[80:83], a[72:75]\n\t" \
  "v_mfma_f32_16x16x32_bf16 a[108:111], v[200:203], v[84:87], a[108:111]\n\t" \
  "s_add_u32 s82, s82, 262144\n\t" \
  "v_mfma_f32_16x16x32_bf16 a[144:147], v[200:203], v[88:91], a[144:147]\n\t" \
  "ds_read_b64_tr_b16 v[200:201], v208 offset:18560\n\t" \
  "ds_read_b64_tr_b16 v[202:203], v208 offset:23168\n\t" \
  "s_waitcnt lgkmcnt(6)\n\t" \
  "v_mfma_f32_16x16x32_bf16 a[4:7], v[204:207], v[72:75], a[4:7]\n\t" \
  "s_add_u32 s87, s83, s69\n\t" \
  "v_mfma_f32_16x16x32_bf16 a[40:43], v[204:207], v[76:79], a[40:43]\n\t" \
  "v_mfma_f32_16x16x32_bf16 a[76:79], v[204:207], v[80:83], a[76:79]\n\t" \
  "v_mfma_f32_16x16x32_bf16 a[112:115], v[204:207], v[84:87], a[112:115]\n\t" \
  "s_add_u32 s87, s87, 393216\n\t" \
  "v_mfma_f32_16x16x32_bf16 a[148:151], v[204:207], v[88:91], a[148:151]\n\t" \
  "ds_read_b64_tr_b16 v[204:205], v209 offset:18592\n\t" \
  "ds_read_b64_tr_b16 v[206:207], v209 offset:23200\n\t" \
  "s_waitcnt lgkmcnt(6)\n\t" \
  "v_mfma_f32_16x16x32_bf16 a[8:11], v[192:195], v[72:75], a[8:11]\n\t" \
  "s_add_u32 s95, s75, s69\n\t" \
  "v_mfma_f32_16x16x32_bf16 a[44:47], v[192:195], v[76:79], a[44:47]\n\t" \
  "v_mfma_f32_16x16x32_bf16 a[80:83], v[192:195], v[80:83], a[80:83]\n\t" \
  "v_mfma_f32_16x16x32_bf16 a[116:119], v[192:195], v[84:87], a[116:119]\n\t" \
  "buffer_load_dwordx4 v[112:115], v244, s[56:59], s80 offen sc1\n\t" \
  "v_mfma_f32_16x16x32_bf16 a[152:155], v[192:195], v[88:91], a[152:155]\n\t" \
  "ds_read_b64_tr_b16 v[192:193], v209 offset:18624\n\t" \
  "ds_read_b64_tr_b16 v[194:195], v209 offset:23232\n\t" \
  "s_waitcnt lgkmcnt(6)\n\t" \
  "v_mfma_f32_16x16x32_bf16 a[12:15], v[196:199], v[72:75], a[12:15]\n\t" \
  "buffer_load_dwordx4 v[116:119], v244, s[56:59], s81 offen sc1\n\t" \
  "v_mfma_f32_16x16x32_bf16 a[48:51], v[196:199], v[76:79], a[48:51]\n\t" \
  "v_mfma_f32_16x16x32_bf16 a[84:87], v[196:199], v[80:83], a[84:87]\n\t" \
  "v_mfma_f32_16x16x32_bf16 a[120:123], v[196:199], v[84:87], a[120:123]\n\t" \
  "buffer_load_dwordx4 v[120:123], v244, s[56:59], s82 offen sc1\n\t" \
  "v_mfma_f32_16x16x32_bf16 a[156:159], v[196:199], v[88:91], a[156:159]\n\t" \
  "ds_read_b64_tr_b16 v[196:197], v209 offset:18656\n\t" \
  "ds_read_b64_tr_b16 v[198:199], v209 offset:23264\n\t" \
  "s_waitcnt lgkmcnt(6)\n\t" \
  "v_mfma_f32_16x16x32_bf16 a[16:19], v[200:203], v[72:75], a[16:19]\n\t" \
  "buffer_load_dwordx4 v[124:127], v244, s[56:59], s87 offen sc1\n\t" \
  "v_mfma_f32_16x16x32_bf16 a[52:55], v[200:203], v[76:79], a[52:55]\n\t" \
  "v_mfma_f32_16x16x32_bf16 a[88:91], v[200:203], v[80:83], a[88:91]\n\t" \
  "v_mfma_f32_16x16x32_bf16 a[124:127], v[200:203], v[84:87], a[124:127]\n\t" \
  "buffer_load_dwordx4 v[128:131], v244, s[56:59], s95 offen sc1\n\t" \
  "v_mfma_f32_16x16x32_bf16 a[160:163], v[200:203], v[88:91], a[160:163]\n\t" \
  "ds_read_b64_tr_b16 v[200:201], v209 offset:18688\n\t" \
  "ds_read_b64_tr_b16 v[202:203], v209 offset:23296\n\t" \
  "s_waitcnt lgkmcnt(4)\n\t" \
  "v_mfma_f32_16x16x32_bf16 a[20:23], v[204:207], v[72:75], a[20:23]\n\t" \
  "buffer_load_dwordx4 v[132:135], v244, s[56:59], s80 offen offset:1024 sc1\n\t" \
  "v_mfma_f32_16x16x32_bf16 a[56:59], v[204:207], v[76:79], a[56:59]\n\t" \
  "v_mfma_f32_16x16x32_bf16 a[92:95], v[204:207], v[80:83], a[92:95]\n\t" \
  "v_mfma_f32_16x16x32_bf16 a[128:131], v[204:207], v[84:87], a[128:131]\n\t" \
  "ds_read_b64_tr_b16 v[204:205], v208 offset:27648\n\t" \
  "ds_read_b64_tr_b16 v[206:207], v208 offset:32256\n\t" \
  "v_mfma_f32_16x16x32_bf16 a[24:27], v[192:195], v[72:75], a[24:27]\n\t" \
  "buffer_load_dwordx4 v[136:139], v244, s[56:59], s81 offen offset:1024 sc1\n\t" \
  "v_mfma_f32_16x16x32_bf16 a[60:63], v[192:195], v[76:79], a[60:63]\n\t" \
  "buffer_load_dwordx4 v[140:143], v244, s[56:59], s82 offen offset:1024 sc1\n\t" \
  "v_mfma_f32_16x16x32_bf16 a[96:99], v[192:195], v[80:83], a[96:99]\n\t" \
  "v_mfma_f32_16x16x32_bf16 a[132:135], v[192:195], v[84:87], a[132:135]\n\t" \
  "ds_read_b64_tr_b16 v[192:193], v208 offset:27680\n\t" \
  "ds_read_b64_tr_b16 v[194:195], v208 offset:32288\n\t" \
  "s_waitcnt lgkmcnt(6)\n\t" \
  "v_mfma_f32_16x16x32_bf16 a[28:31], v[196:199], v[72:75], a[28:31]\n\t" \
  "buffer_load_dwordx4 v[144:147], v244, s[56:59], s87 offen offset:1024 sc1\n\t" \
  "v_mfma_f32_16x16x32_bf16 a[64:67], v[196:199], v[76:79], a[64:67]\n\t" \
  "v_mfma_f32_16x16x32_bf16 a[100:103], v[196:199], v[80:83], a[100:103]\n\t" \
  "buffer_load_dwordx4 v[148:151], v244, s[56:59], s95 offen offset:1024 sc1\n\t" \
  "v_mfma_f32_16x16x32_bf16 a[136:139], v[196:199], v[84:87], a[136:139]\n\t" \
  "ds_read_b64_tr_b16 v[196:197], v208 offset:27712\n\t" \
  "ds_read_b64_tr_b16 v[198:199], v208 offset:32320\n\t" \
  "s_waitcnt lgkmcnt(6)\n\t" \
  "v_mfma_f32_16x16x32_bf16 a[32:35], v[200:203], v[72:75], a[32:35]\n\t" \
  "v_mfma_f32_16x16x32_bf16 a[68:71], v[200:203], v[76:79], a[68:71]\n\t" \
  "buffer_load_dwordx4 v[152:155], v244, s[56:59], s80 offen offset:2048 sc1\n\t" \
  "v_mfma_f32_16x16x32_bf16 a[104:107], v[200:203], v[80:83], a[104:107]\n\t" \
  "v_mfma_f32_16x16x32_bf16 a[140:143], v[200:203], v[84:87], a[140:143]\n\t" \
  "ds_read_b64_tr_b16 v[200:201], v208 offset:27744\n\t" \
  "ds_read_b64_tr_b16 v[202:203], v208 offset:32352\n\t" \
  "s_waitcnt vmcnt(29)\n\t" \
  "s_waitcnt lgkmcnt(6)\n\t" \
  "v_mfma_f32_16x16x32_bf16 a[0:3], v[204:207], v[92:95], a[0:3]\n\t" \
  "buffer_load_dwordx4 v[156:159], v244, s[56:59], s81 offen offset:2048 sc1\n\t" \
  "v_mfma_f32_16x16x32_bf16 a[36:39], v[204:207], v[96:99], a[36:39]\n\t" \
  "v_mfma_f32_16x16x32_bf16 a[72:75], v[204:207], v[100:103], a[72:75]\n\t" \
  "buffer_load_dwordx4 v[160:163], v244, s[56:59], s82 offen offset:2048 sc1\n\t" \
  "v_mfma_f32_16x16x32_bf16 a[108:111], v[204:207], v[104:107], a[108:111]\n\t" \
  "v_mfma_f32_16x16x32_bf16 a[144:147], v[204:207], v[108:111], a[144:147]\n\t" \
  "ds_read_b64_tr_b16 v[204:205], v208 offset:27776\n\t" \
  "ds_read_b64_tr_b16 v[206:207], v208 offset:32384\n\t" \
  "s_waitcnt lgkmcnt(6)\n\t" \
  "v_mfma_f32_16x16x32_bf16 a[4:7], v[192:195], v[92:95], a[4:7]\n\t" \
  "buffer_load_dwordx4 v[164:167], v244, s[56:59], s87 offen offset:2048 sc1\n\t" \
  "v_mfma_f32_16x16x32_bf16 a[40:43], v[192:195], v[96:99], a[40:43]\n\t" \
  "v_mfma_f32_16x16x32_bf16 a[76:79], v[192:195], v[100:103], a[76:79]\n\t" \
  "buffer_load_dwordx4 v[168:171], v244, s[56:59], s95 offen offset:2048 sc1\n\t" \
  "v_mfma_f32_16x16x32_bf16 a[112:115], v[192:195], v[104:107], a[112:115]\n\t" \
  "v_mfma_f32_16x16x32_bf16 a[148:151], v[192:195], v[108:111], a[148:151]\n\t" \
  "ds_read_b64_tr_b16 v[192:193], v209 offset:27808\n\t" \
  "ds_read_b64_tr_b16 v[194:195], v209 offset:32416\n\t" \
  "s_waitcnt lgkmcnt(6)\n\t" \
  "v_mfma_f32_16x16x32_bf16 a[8:11], v[196:199], v[92:95], a[8:11]\n\t" \
  "buffer_load_dwordx4 v[172:175], v244, s[56:59], s80 offen offset:3072 sc1\n\t" \
  "v_mfma_f32_16x16x32_bf16 a[44:47], v[196:199], v[96:99], a[44:47]\n\t" \
  "v_mfma_f32_16x16x32_bf16 a[80:83], v[196:199], v[100:103], a[80:83]\n\t" \
  "buffer_load_dwordx4 v[176:179], v244, s[56:59], s81 offen offset:3072 sc1\n\t" \
  "v_mfma_f32_16x16x32_bf16 a[116:119], v[196:199], v[104:107], a[116:119]\n\t" \
  "v_mfma_f32_16x16x32_bf16 a[152:155], v[196:199], v[108:111], a[152:155]\n\t" \
  "ds_read_b64_tr_b16 v[196:197], v209 offset:27840\n\t" \
  "ds_read_b64_tr_b16 v[198:199], v209 offset:32448\n\t" \
  "s_waitcnt lgkmcnt(6)\n\t" \
  "v_mfma_f32_16x16x32_bf16 a[12:15], v[200:203], v[92:95], a[12:15]\n\t" \
  "buffer_load_dwordx4 v[180:183], v244, s[56:59], s82 offen offset:3072 sc1\n\t" \
  "v_mfma_f32_16x16x32_bf16 a[48:51], v[200:203], v[96:99], a[48:51]\n\t" \
  "v_mfma_f32_16x16x32_bf16 a[84:87], v[200:203], v[100:103], a[84:87]\n\t" \
  "buffer_load_dwordx4 v[184:187], v244, s[56:59], s87 offen offset:3072 sc1\n\t" \
  "v_mfma_f32_16x16x32_bf16 a[120:123], v[200:203], v[104:107], a[120:123]\n\t" \
  "v_mfma_f32_16x16x32_bf16 a[156:159], v[200:203], v[108:111], a[156:159]\n\t" \
  "ds_read_b64_tr_b16 v[200:201], v209 offset:27872\n\t" \
  "ds_read_b64_tr_b16 v[202:203], v209 offset:32480\n\t" \
  "s_waitcnt lgkmcnt(4)\n\t" \
  "v_mfma_f32_16x16x32_bf16 a[16:19], v[204:207], v[92:95], a[16:19]\n\t" \
  "buffer_load_dwordx4 v[188:191], v244, s[56:59], s95 offen offset:3072 sc1\n\t" \
  "v_mfma_f32_16x16x32_bf16 a[52:55], v[204:207], v[96:99], a[52:55]\n\t" \
  "v_mfma_f32_16x16x32_bf16 a[88:91], v[204:207], v[100:103], a[88:91]\n\t" \
  "v_mfma_f32_16x16x32_bf16 a[124:127], v[204:207], v[104:107], a[124:127]\n\t" \
  "v_mfma_f32_16x16x32_bf16 a[160:163], v[204:207], v[108:111], a[160:163]\n\t" \
  "ds_read_b64_tr_b16 v[204:205], v209 offset:27904\n\t" \
  "ds_read_b64_tr_b16 v[206:207], v209 offset:32512\n\t" \
  "v_mfma_f32_16x16x32_bf16 a[20:23], v[192:195], v[92:95], a[20:23]\n\t" \
  "v_mfma_f32_16x16x32_bf16 a[56:59], v[192:195], v[96:99], a[56:59]\n\t" \
  "v_mfma_f32_16x16x32_bf16 a[92:95], v[192:195], v[100:103], a[92:95]\n\t" \
  "v_mfma_f32_16x16x32_bf16 a[128:131], v[192:195], v[104:107], a[128:131]\n\t" \
  "s_waitcnt lgkmcnt(4)\n\t" \
  "v_mfma_f32_16x16x32_bf16 a[24:27], v[196:199], v[92:95], a[24:27]\n\t" \
  "v_mfma_f32_16x16x32_bf16 a[60:63], v[196:199], v[96:99], a[60:63]\n\t" \
  "v_mfma_f32_16x16x32_bf16 a[96:99], v[196:199], v[100:103], a[96:99]\n\t" \
  "v_mfma_f32_16x16x32_bf16 a[132:135], v[196:199], v[104:107], a[132:135]\n\t" \
  "s_waitcnt lgkmcnt(2)\n\t" \
  "v_mfma_f32_16x16x32_bf16 a[28:31], v[200:203], v[92:95], a[28:31]\n\t" \
  "v_mfma_f32_16x16x32_bf16 a[64:67], v[200:203], v[96:99], a[64:67]\n\t" \
  "v_mfma_f32_16x16x32_bf16 a[100:103], v[200:203], v[100:103], a[100:103]\n\t" \
  "v_mfma_f32_16x16x32_bf16 a[136:139], v[200:203], v[104:107], a[136:139]\n\t" \
  "s_waitcnt lgkmcnt(0)\n\t" \
  "v_mfma_f32_16x16x32_bf16 a[32:35], v[204:207], v[92:95], a[32:35]\n\t" \
  "v_mfma_f32_16x16x32_bf16 a[68:71], v[204:207], v[96:99], a[68:71]\n\t" \
  "v_mfma_f32_16x16x32_bf16 a[104:107], v[204:207], v[100:103], a[104:107]\n\t" \
  "v_mfma_f32_16x16x32_bf16 a[140:143], v[204:207], v[104:107], a[140:143]\n\t" \
  "s_waitcnt vmcnt(20)\n\t" \
  "s_barrier\n\t" \
  "v_add_u32 v208, 73728, v210\n\t" \
  "v_add_u32 v209, 73728, v211\n\t" \
  "ds_read_b64_tr_b16 v[192:193], v208 offset:0\n\t" \
  "ds_read_b64_tr_b16 v[194:195], v208 offset:4608\n\t" \
  "ds_read_b64_tr_b16 v[196:197], v208 offset:32\n\t" \
  "ds_read_b64_tr_b16 v[198:199], v208 offset:4640\n\t" \
  "ds_read_b64_tr_b16 v[200:201], v208 offset:64\n\t" \
  "ds_read_b64_tr_b16 v[202:203], v208 offset:4672\n\t" \
  "ds_read_b64_tr_b16 v[204:205], v208 offset:96\n\t" \
  "ds_read_b64_tr_b16 v[206:207], v208 offset:4704\n\t" \
  "s_waitcnt vmcnt(15)\n\t" \
  "s_waitcnt lgkmcnt(6)\n\t" \
  "v_mfma_f32_16x16x32_bf16 a[0:3], v[192:195], v[112:115], a[0:3]\n\t" \
  "v_mfma_f32_16x16x32_bf16 a[36:39], v[192:195], v[116:119], a[36:39]\n\t" \
  "v_mfma_f32_16x16x32_bf16 a[72:75], v[192:195], v[120:123], a[72:75]\n\t" \
  "v_mfma_f32_16x16x32_bf16 a[108:111], v[192:195], v[124:127], a[108:111]\n\t" \
  "v_mfma_f32_16x16x32_bf16 a[144:147], v[192:195], v[128:131], a[144:147]\n\t" \
  "ds_read_b64_tr_b16 v[192:193], v208 offset:128\n\t" \
  "ds_read_b64_tr_b16 v[194:195], v208 offset:4736\n\t" \
  "s_waitcnt lgkmcnt(6)\n\t" \
  "v_mfma_f32_16x16x32_bf16 a[4:7], v[196:199], v[112:115], a[4:7]\n\t" \
  "v_mfma_f32_16x16x32_bf16 a[40:43], v[196:199], v[116:119], a[40:43]\n\t" \
  "v_mfma_f32_16x16x32_bf16 a[76:79], v[196:199], v[120:123], a[76:79]\n\t" \
  "v_mfma_f32_16x16x32_bf16 a[112:115], v[196:199], v[124:127], a[112:115]\n\t" \
  "v_mfma_f32_16x16x32_bf16 a[148:151], v[196:199], v[128:131], a[148:151]\n\t" \
  "ds_read_b64_tr_b16 v[196:197], v209 offset:160\n\t" \
  "ds_read_b64_tr_b16 v[198:199], v209 offset:4768\n\t" \
  "s_waitcnt lgkmcnt(6)\n\t" \
  "v_mfma_f32_16x16x32_bf16 a[8:11], v[200:203], v[112:115], a[8:11]\n\t" \
  "v_mfma_f32_16x16x32_bf16 a[44:47], v[200:203], v[116:119], a[44:47]\n\t" \
  "v_mfma_f32_16x16x32_bf16 a[80:83], v[200:203], v[120:123], a[80:83]\n\t" \
  "v_mfma_f32_16x16x32_bf16 a[116:119], v[200:203], v[124:127], a[116:119]\n\t" \
  "v_mfma_f32_16x16x32_bf16 a[152:155], v[200:203], v[128:131], a[152:155]\n\t" \
  "ds_read_b64_tr_b16 v[200:201], v209 offset:192\n\t" \
  "ds_read_b64_tr_b16 v[202:203], v209 offset:4800\n\t" \
  "s_waitcnt lgkmcnt(6)\n\t" \
  "v_mfma_f32_16x16x32_bf16 a[12:15], v[204:207], v[112:115], a[12:15]\n\t" \
  "v_mfma_f32_16x16x32_bf16 a[48:51], v[204:207], v[116:119], a[48:51]\n\t" \
  "v_mfma_f32_16x16x32_bf16 a[84:87], v[204:207], v[120:123], a[84:87]\n\t" \
  "v_mfma_f32_16x16x32_bf16 a[120:123], v[204:207], v[124:127], a[120:123]\n\t" \
  "v_mfma_f32_16x16x32_bf16 a[156:159], v[204:207], v[128:131], a[156:159]\n\t" \
  "ds_read_b64_tr_b16 v[204:205], v209 offset:224\n\t" \
  "ds_read_b64_tr_b16 v[206:207], v209 offset:4832\n\t" \
  "s_waitcnt lgkmcnt(6)\n\t" \
  "v_mfma_f32_16x16x32_bf16 a[16:19], v[192:195], v[112:115], a[16:19]\n\t" \
  "v_mfma_f32_16x16x32_bf16 a[52:55], v[192:195], v[116:119], a[52:55]\n\t" \
  "v_mfma_f32_16x16x32_bf16 a[88:91], v[192:195], v[120:123], a[88:91]\n\t" \
  "v_mfma_f32_16x16x32_bf16 a[124:127], v[192:195], v[124:127], a[124:127]\n\t" \
  "v_mfma_f32_16x16x32_bf16 a[160:163], v[192:195], v[128:131], a[160:163]\n\t" \
  "ds_read_b64_tr_b16 v[192:193], v209 offset:256\n\t" \
  "ds_read_b64_tr_b16 v[194:195], v209 offset:4864\n\t" \
  "s_waitcnt lgkmcnt(6)\n\t" \
  "v_mfma_f32_16x16x32_bf16 a[20:23], v[196:199], v[112:115], a[20:23]\n\t" \
  "v_mfma_f32_16x16x32_bf16 a[56:59], v[196:199], v[116:119], a[56:59]\n\t" \
  "v_mfma_f32_16x16x32_bf16 a[92:95], v[196:199], v[120:123], a[92:95]\n\t" \
  "v_mfma_f32_16x16x32_bf16 a[128:131], v[196:199], v[124:127], a[128:131]\n\t" \
  "ds_read_b64_tr_b16 v[196:197], v208 offset:9216\n\t" \
  "ds_read_b64_tr_b16 v[198:199], v208 offset:13824\n\t" \
  "s_waitcnt lgkmcnt(6)\n\t" \
  "v_mfma_f32_16x16x32_bf16 a[24:27], v[200:203], v[112:115], a[24:27]\n\t" \
  "v_mfma_f32_16x16x32_bf16 a[60:63], v[200:203], v[116:119], a[60:63]\n\t" \
  "v_mfma_f32_16x16x32_bf16 a[96:99], v[200:203], v[120:123], a[96:99]\n\t" \
  "v_mfma_f32_16x16x32_bf16 a[132:135], v[200:203], v[124:127], a[132:135]\n\t" \
  "ds_read_b64_tr_b16 v[200:201], v208 offset:9248\n\t" \
  "ds_read_b64_tr_b16 v[202:203], v208 offset:13856\n\t" \
  "s_waitcnt lgkmcnt(6)\n\t" \
  "v_mfma_f32_16x16x32_bf16 a[28:31], v[204:207], v[112:115], a[28:31]\n\t" \
  "v_mfma_f32_16x16x32_bf16 a[64:67], v[204:207], v[116:119], a[64:67]\n\t" \
  "v_mfma_f32_16x16x32_bf16 a[100:103], v[204:207], v[120:123], a[100:103]\n\t" \
  "v_mfma_f32_16x16x32_bf16 a[136:139], v[204:207], v[124:127], a[136:139]\n\t" \
  "ds_read_b64_tr_b16 v[204:205], v208 offset:9280\n\t" \
  "ds_read_b64_tr_b16 v[206:207], v208 offset:13888\n\t" \
  "s_waitcnt lgkmcnt(6)\n\t" \
  "v_mfma_f32_16x16x32_bf16 a[32:35], v[192:195], v[112:115], a[32:35]\n\t" \
  "v_mfma_f32_16x16x32_bf16 a[68:71], v[192:195], v[116:119], a[68:71]\n\t" \
  "v_mfma_f32_16x16x32_bf16 a[104:107], v[192:195], v[120:123], a[104:107]\n\t" \
  "v_mfma_f32_16x16x32_bf16 a[140:143], v[192:195], v[124:127], a[140:143]\n\t" \
  "ds_read_b64_tr_b16 v[192:193], v208 offset:9312\n\t" \
  "ds_read_b64_tr_b16 v[194:195], v208 offset:13920\n\t" \
  "s_waitcnt vmcnt(10)\n\t" \
  "s_waitcnt lgkmcnt(6)\n\t" \
  "v_mfma_f32_16x16x32_bf16 a[0:3], v[196:199], v[132:135], a[0:3]\n\t" \
  "v_mfma_f32_16x16x32_bf16 a[36:39], v[196:199], v[136:139], a[36:39]\n\t" \
  "v_mfma_f32_16x16x32_bf16 a[72:75], v[196:199], v[140:143], a[72:75]\n\t" \
  "v_mfma_f32_16x16x32_bf16 a[108:111], v[196:199], v[144:147], a[108:111]\n\t" \
  "v_mfma_f32_16x16x32_bf16 a[144:147], v[196:199], v[148:151], a[144:147]\n\t" \
  "ds_read_b64_tr_b16 v[196:197], v208 offset:9344\n\t" \
  "ds_read_b64_tr_b16 v[198:199], v208 offset:13952\n\t" \
  "s_waitcnt lgkmcnt(6)\n\t" \
  "v_mfma_f32_16x16x32_bf16 a[4:7], v[200:203], v[132:135], a[4:7]\n\t" \
  "v_mfma_f32_16x16x32_bf16 a[40:43], v[200:203], v[136:139], a[40:43]\n\t" \
  "v_mfma_f32_16x16x32_bf16 a[76:79], v[200:203], v[140:143], a[76:79]\n\t" \
  "v_mfma_f32_16x16x32_bf16 a[112:115], v[200:203], v[144:147], a[112:115]\n\t" \
  "v_mfma_f32_16x16x32_bf16 a[148:151], v[200:203], v[148:151], a[148:151]\n\t" \
  "ds_read_b64_tr_b16 v[200:201], v209 offset:9376\n\t" \
  "ds_read_b64_tr_b16 v[202:203], v209 offset:13984\n\t" \
  "s_waitcnt lgkmcnt(6)\n\t" \
  "v_mfma_f32_16x16x32_bf16 a[8:11], v[204:207], v[132:135], a[8:11]\n\t" \
  "v_mfma_f32_16x16x32_bf16 a[44:47], v[204:207], v[136:139], a[44:47]\n\t" \
  "v_mfma_f32_16x16x32_bf16 a[80:83], v[204:207], v[140:143], a[80:83]\n\t" \
  "v_mfma_f32_16x16x32_bf16 a[116:119], v[204:207], v[144:147], a[116:119]\n\t" \
  "v_mfma_f32_16x16x32_bf16 a[152:155], v[204:207], v[148:151], a[152:155]\n\t" \
  "ds_read_b64_tr_b16 v[204:205], v209 offset:9408\n\t" \
  "ds_read_b64_tr_b16 v[206:207], v209 offset:14016\n\t" \
  "s_waitcnt lgkmcnt(6)\n\t" \
  "v_mfma_f32_16x16x32_bf16 a[12:15], v[192:195], v[132:135], a[12:15]\n\t" \
  "v_mfma_f32_16x16x32_bf16 a[48:51], v[192:195], v[136:139], a[48:51]\n\t" \
  "v_mfma_f32_16x16x32_bf16 a[84:87], v[192:195], v[140:143], a[84:87]\n\t" \
  "v_mfma_f32_16x16x32_bf16 a[120:123], v[192:195], v[144:147], a[120:123]\n\t" \
  "v_mfma_f32_16x16x32_bf16 a[156:159], v[192:195], v[148:151], a[156:159]\n\t" \
  "ds_read_b64_tr_b16 v[192:193], v209 offset:9440\n\t" \
  "ds_read_b64_tr_b16 v[194:195], v209 offset:14048\n\t" \
  "s_waitcnt lgkmcnt(6)\n\t" \
  "v_mfma_f32_16x16x32_bf16 a[16:19], v[196:199], v[132:135], a[16:19]\n\t" \
  "v_mfma_f32_16x16x32_bf16 a[52:55], v[196:199], v[136:139], a[52:55]\n\t" \
  "v_mfma_f32_16x16x32_bf16 a[88:91], v[196:199], v[140:143], a[88:91]\n\t" \
  "v_mfma_f32_16x16x32_bf16 a[124:127], v[196:199], v[144:147], a[124:127]\n\t" \
  "v_mfma_f32_16x16x32_bf16 a[160:163], v[196:199], v[148:151], a[160:163]\n\t" \
  "ds_read_b64_tr_b16 v[196:197], v209 offset:9472\n\t" \
  "ds_read_b64_tr_b16 v[198:199], v209 offset:14080\n\t" \
  "s_waitcnt lgkmcnt(6)\n\t" \
  "v_mfma_f32_16x16x32_bf16 a[20:23], v[200:203], v[132:135], a[20:23]\n\t" \
  "v_mfma_f32_16x16x32_bf16 a[56:59], v[200:203], v[136:139], a[56:59]\n\t" \
  "v_mfma_f32_16x16x32_bf16 a[92:95], v[200:203], v[140:143], a[92:95]\n\t" \
  "v_mfma_f32_16x16x32_bf16 a[128:131], v[200:203], v[144:147], a[128:131]\n\t" \
  "ds_read_b64_tr_b16 v[200:201], v208 offset:18432\n\t" \
  "ds_read_b64_tr_b16 v[202:203], v208 offset:23040\n\t" \
  "s_waitcnt lgkmcnt(6)\n\t" \
  "v_mfma_f32_16x16x32_bf16 a[24:27], v[204:207], v[132:135], a[24:27]\n\t" \
  "v_mfma_f32_16x16x32_bf16 a[60:63], v[204:207], v[136:139], a[60:63]\n\t" \
  "v_mfma_f32_16x16x32_bf16 a[96:99], v[204:207], v[140:143], a[96:99]\n\t" \
  "v_mfma_f32_16x16x32_bf16 a[132:135], v[204:207], v[144:147], a[132:135]\n\t" \
  "ds_read_b64_tr_b16 v[204:205], v208 offset:18464\n\t" \
  "ds_read_b64_tr_b16 v[206:207], v208 offset:23072\n\t" \
  "s_waitcnt lgkmcnt(6)\n\t" \
  "v_mfma_f32_16x16x32_bf16 a[28:31], v[192:195], v[132:135], a[28:31]\n\t" \
  "v_mfma_f32_16x16x32_bf16 a[64:67], v[192:195], v[136:139], a[64:67]\n\t" \
  "v_mfma_f32_16x16x32_bf16 a[100:103], v[192:195], v[140:143], a[100:103]\n\t" \
  "v_mfma_f32_16x16x32_bf16 a[136:139], v[192:195], v[144:147], a[136:139]\n\t" \
  "ds_read_b64_tr_b16 v[192:193], v208 offset:18496\n\t" \
  "ds_read_b64_tr_b16 v[194:195], v208 offset:23104\n\t" \
  "s_waitcnt lgkmcnt(6)\n\t" \
  "v_mfma_f32_16x16x32_bf16 a[32:35], v[196:199], v[132:135], a[32:35]\n\t" \
  "v_mfma_f32_16x16x32_bf16 a[68:71], v[196:199], v[136:139], a[68:71]\n\t" \
  "v_mfma_f32_16x16x32_bf16 a[104:107], v[196:199], v[140:143], a[104:107]\n\t" \
  "v_mfma_f32_16x16x32_bf16 a[140:143], v[196:199], v[144:147], a[140:143]\n\t" \
  "ds_read_b64_tr_b16 v[196:197], v208 offset:18528\n\t" \
  "ds_read_b64_tr_b16 v[198:199], v208 offset:23136\n\t" \
  "s_waitcnt vmcnt(5)\n\t" \
  "s_waitcnt lgkmcnt(6)\n\t" \
  "v_mfma_f32_16x16x32_bf16 a[0:3], v[200:203], v[152:155], a[0:3]\n\t" \
  "v_mfma_f32_16x16x32_bf16 a[36:39], v[200:203], v[156:159], a[36:39]\n\t" \
  "v_mfma_f32_16x16x32_bf16 a[72:75], v[200:203], v[160:163], a[72:75]\n\t" \
  "v_mfma_f32_16x16x32_bf16 a[108:111], v[200:203], v[164:167], a[108:111]\n\t" \
  "v_mfma_f32_16x16x32_bf16 a[144:147], v[200:203], v[168:171], a[144:147]\n\t" \
  "ds_read_b64_tr_b16 v[200:201], v208 offset:18560\n\t" \
  "ds_read_b64_tr_b16 v[202:203], v208 offset:23168\n\t" \
  "s_waitcnt lgkmcnt(6)\n\t" \
  "v_mfma_f32_16x16x32_bf16 a[4:7], v[204:207], v[152:155], a[4:7]\n\t" \
  "v_mfma_f32_16x16x32_bf16 a[40:43], v[204:207], v[156:159], a[40:43]\n\t" \
  "v_mfma_f32_16x16x32_bf16 a[76:79], v[204:207], v[160:163], a[76:79]\n\t" \
  "v_mfma_f32_16x16x32_bf16 a[112:115], v[204:207], v[164:167], a[112:115]\n\t" \
  "v_mfma_f32_16x16x32_bf16 a[148:151], v[204:207], v[168:171], a[148:151]\n\t" \
  "ds_read_b64_tr_b16 v[204:205], v209 offset:18592\n\t" \
  "ds_read_b64_tr_b16 v[206:207], v209 offset:23200\n\t" \
  "s_waitcnt lgkmcnt(6)\n\t" \
  "v_mfma_f32_16x16x32_bf16 a[8:11], v[192:195], v[152:155], a[8:11]\n\t" \
  "v_mfma_f32_16x16x32_bf16 a[44:47], v[192:195], v[156:159], a[44:47]\n\t" \
  "v_mfma_f32_16x16x32_bf16 a[80:83], v[192:195], v[160:163], a[80:83]\n\t" \
  "v_mfma_f32_16x16x32_bf16 a[116:119], v[192:195], v[164:167], a[116:119]\n\t" \
  "v_mfma_f32_16x16x32_bf16 a[152:155], v[192:195], v[168:171], a[152:155]\n\t" \
  "ds_read_b64_tr_b16 v[192:193], v209 offset:18624\n\t" \
  "ds_read_b64_tr_b16 v[194:195], v209 offset:23232\n\t" \
  "s_waitcnt lgkmcnt(6)\n\t" \
  "v_mfma_f32_16x16x32_bf16 a[12:15], v[196:199], v[152:155], a[12:15]\n\t" \
  "v_mfma_f32_16x16x32_bf16 a[48:51], v[196:199], v[156:159], a[48:51]\n\t" \
  "v_mfma_f32_16x16x32_bf16 a[84:87], v[196:199], v[160:163], a[84:87]\n\t" \
  "v_mfma_f32_16x16x32_bf16 a[120:123], v[196:199], v[164:167], a[120:123]\n\t" \
  "v_mfma_f32_16x16x32_bf16 a[156:159], v[196:199], v[168:171], a[156:159]\n\t" \
  "ds_read_b64_tr_b16 v[196:197], v209 offset:18656\n\t" \
  "ds_read_b64_tr_b16 v[198:199], v209 offset:23264\n\t" \
  "s_waitcnt lgkmcnt(6)\n\t" \
  "v_mfma_f32_16x16x32_bf16 a[16:19], v[200:203], v[152:155], a[16:19]\n\t" \
  "v_mfma_f32_16x16x32_bf16 a[52:55], v[200:203], v[156:159], a[52:55]\n\t" \
  "v_mfma_f32_16x16x32_bf16 a[88:91], v[200:203], v[160:163], a[88:91]\n\t" \
  "v_mfma_f32_16x16x32_bf16 a[124:127], v[200:203], v[164:167], a[124:127]\n\t" \
  "v_mfma_f32_16x16x32_bf16 a[160:163], v[200:203], v[168:171], a[160:163]\n\t" \
  "ds_read_b64_tr_b16 v[200:201], v209 offset:18688\n\t" \
  "ds_read_b64_tr_b16 v[202:203], v209 offset:23296\n\t" \
  "s_waitcnt lgkmcnt(6)\n\t" \
  "v_mfma_f32_16x16x32_bf16 a[20:23], v[204:207], v[152:155], a[20:23]\n\t" \
  "v_mfma_f32_16x16x32_bf16 a[56:59], v[204:207], v[156:159], a[56:59]\n\t" \
  "v_mfma_f32_16x16x32_bf16 a[92:95], v[204:207], v[160:163], a[92:95]\n\t" \
  "v_mfma_f32_16x16x32_bf16 a[128:131], v[204:207], v[164:167], a[128:131]\n\t" \
  "ds_read_b64_tr_b16 v[204:205], v208 offset:27648\n\t" \
  "ds_read_b64_tr_b16 v[206:207], v208 offset:32256\n\t" \
  "s_waitcnt lgkmcnt(6)\n\t" \
  "v_mfma_f32_16x16x32_bf16 a[24:27], v[192:195], v[152:155], a[24:27]\n\t" \
  "v_mfma_f32_16x16x32_bf16 a[60:63], v[192:195], v[156:159], a[60:63]\n\t" \
  "v_mfma_f32_16x16x32_bf16 a[96:99], v[192:195], v[160:163], a[96:99]\n\t" \
  "v_mfma_f32_16x16x32_bf16 a[132:135], v[192:195], v[164:167], a[132:135]\n\t" \
  "ds_read_b64_tr_b16 v[192:193], v208 offset:27680\n\t" \
  "ds_read_b64_tr_b16 v[194:195], v208 offset:32288\n\t" \
  "s_waitcnt lgkmcnt(6)\n\t" \
  "v_mfma_f32_16x16x32_bf16 a[28:31], v[196:199], v[152:155], a[28:31]\n\t" \
  "v_mfma_f32_16x16x32_bf16 a[64:67], v[196:199], v[156:159], a[64:67]\n\t" \
  "v_mfma_f32_16x16x32_bf16 a[100:103], v[196:199], v[160:163], a[100:103]\n\t" \
  "v_mfma_f32_16x16x32_bf16 a[136:139], v[196:199], v[164:167], a[136:139]\n\t" \
  "ds_read_b64_tr_b16 v[196:197], v208 offset:27712\n\t" \
  "ds_read_b64_tr_b16 v[198:199], v208 offset:32320\n\t" \
  "s_waitcnt lgkmcnt(6)\n\t" \
  "v_mfma_f32_16x16x32_bf16 a[32:35], v[200:203], v[152:155], a[32:35]\n\t" \
  "v_mfma_f32_16x16x32_bf16 a[68:71], v[200:203], v[156:159], a[68:71]\n\t" \
  "v_mfma_f32_16x16x32_bf16 a[104:107], v[200:203], v[160:163], a[104:107]\n\t" \
  "v_mfma_f32_16x16x32_bf16 a[140:143], v[200:203], v[164:167], a[140:143]\n\t" \
  "ds_read_b64_tr_b16 v[200:201], v208 offset:27744\n\t" \
  "ds_read_b64_tr_b16 v[202:203], v208 offset:32352\n\t" \
  "s_waitcnt vmcnt(0)\n\t" \
  "s_waitcnt lgkmcnt(6)\n\t" \
  "v_mfma_f32_16x16x32_bf16 a[0:3], v[204:207], v[172:175], a[0:3]\n\t" \
  "v_mfma_f32_16x16x32_bf16 a[36:39], v[204:207], v[176:179], a[36:39]\n\t" \
  "v_mfma_f32_16x16x32_bf16 a[72:75], v[204:207], v[180:183], a[72:75]\n\t" \
  "v_mfma_f32_16x16x32_bf16 a[108:111], v[204:207], v[184:187], a[108:111]\n\t" \
  "v_mfma_f32_16x16x32_bf16 a[144:147], v[204:207], v[188:191], a[144:147]\n\t" \
  "ds_read_b64_tr_b16 v[204:205], v208 offset:27776\n\t" \
  "ds_read_b64_tr_b16 v[206:207], v208 offset:32384\n\t" \
  "s_waitcnt lgkmcnt(6)\n\t" \
  "v_mfma_f32_16x16x32_bf16 a[4:7], v[192:195], v[172:175], a[4:7]\n\t" \
  "v_mfma_f32_16x16x32_bf16 a[40:43], v[192:195], v[176:179], a[40:43]\n\t" \
  "v_mfma_f32_16x16x32_bf16 a[76:79], v[192:195], v[180:183], a[76:79]\n\t" \
  "v_mfma_f32_16x16x32_bf16 a[112:115], v[192:195], v[184:187], a[112:115]\n\t" \
  "v_mfma_f32_16x16x32_bf16 a[148:151], v[192:195], v[188:191], a[148:151]\n\t" \
  "ds_read_b64_tr_b16 v[192:193], v209 offset:27808\n\t" \
  "ds_read_b64_tr_b16 v[194:195], v209 offset:32416\n\t" \
  "s_waitcnt lgkmcnt(6)\n\t" \
  "v_mfma_f32_16x16x32_bf16 a[8:11], v[196:199], v[172:175], a[8:11]\n\t" \
  "v_mfma_f32_16x16x32_bf16 a[44:47], v[196:199], v[176:179], a[44:47]\n\t" \
  "v_mfma_f32_16x16x32_bf16 a[80:83], v[196:199], v[180:183], a[80:83]\n\t" \
  "v_mfma_f32_16x16x32_bf16 a[116:119], v[196:199], v[184:187], a[116:119]\n\t" \
  "v_mfma_f32_16x16x32_bf16 a[152:155], v[196:199], v[188:191], a[152:155]\n\t" \
  "ds_read_b64_tr_b16 v[196:197], v209 offset:27840\n\t" \
  "ds_read_b64_tr_b16 v[198:199], v209 offset:32448\n\t" \
  "s_waitcnt lgkmcnt(6)\n\t" \
  "v_mfma_f32_16x16x32_bf16 a[12:15], v[200:203], v[172:175], a[12:15]\n\t" \
  "v_mfma_f32_16x16x32_bf16 a[48:51], v[200:203], v[176:179], a[48:51]\n\t" \
  "v_mfma_f32_16x16x32_bf16 a[84:87], v[200:203], v[180:183], a[84:87]\n\t" \
  "v_mfma_f32_16x16x32_bf16 a[120:123], v[200:203], v[184:187], a[120:123]\n\t" \
  "v_mfma_f32_16x16x32_bf16 a[156:159], v[200:203], v[188:191], a[156:159]\n\t" \
  "ds_read_b64_tr_b16 v[200:201], v209 offset:27872\n\t" \
  "ds_read_b64_tr_b16 v[202:203], v209 offset:32480\n\t" \
  "s_waitcnt lgkmcnt(6)\n\t" \
  "v_mfma_f32_16x16x32_bf16 a[16:19], v[204:207], v[172:175], a[16:19]\n\t" \
  "v_mfma_f32_16x16x32_bf16 a[52:55], v[204:207], v[176:179], a[52:55]\n\t" \
  "v_mfma_f32_16x16x32_bf16 a[88:91], v[204:207], v[180:183], a[88:91]\n\t" \
  "v_mfma_f32_16x16x32_bf16 a[124:127], v[204:207], v[184:187], a[124:127]\n\t" \
  "v_mfma_f32_16x16x32_bf16 a[160:163], v[204:207], v[188:191], a[160:163]\n\t" \
  "ds_read_b64_tr_b16 v[204:205], v209 offset:27904\n\t" \
  "ds_read_b64_tr_b16 v[206:207], v209 offset:32512\n\t" \
  "s_waitcnt lgkmcnt(6)\n\t" \
  "v_mfma_f32_16x16x32_bf16 a[20:23], v[192:195], v[172:175], a[20:23]\n\t" \
  "v_mfma_f32_16x16x32_bf16 a[56:59], v[192:195], v[176:179], a[56:59]\n\t" \
  "v_mfma_f32_16x16x32_bf16 a[92:95], v[192:195], v[180:183], a[92:95]\n\t" \
  "v_mfma_f32_16x16x32_bf16 a[128:131], v[192:195], v[184:187], a[128:131]\n\t" \
  "s_waitcnt lgkmcnt(4)\n\t" \
  "v_mfma_f32_16x16x32_bf16 a[24:27], v[196:199], v[172:175], a[24:27]\n\t" \
  "v_mfma_f32_16x16x32_bf16 a[60:63], v[196:199], v[176:179], a[60:63]\n\t" \
  "v_mfma_f32_16x16x32_bf16 a[96:99], v[196:199], v[180:183], a[96:99]\n\t" \
  "v_mfma_f32_16x16x32_bf16 a[132:135], v[196:199], v[184:187], a[132:135]\n\t" \
  "s_waitcnt lgkmcnt(2)\n\t" \
  "v_mfma_f32_16x16x32_bf16 a[28:31], v[200:203], v[172:175], a[28:31]\n\t" \
  "v_mfma_f32_16x16x32_bf16 a[64:67], v[200:203], v[176:179], a[64:67]\n\t" \
  "v_mfma_f32_16x16x32_bf16 a[100:103], v[200:203], v[180:183], a[100:103]\n\t" \
  "v_mfma_f32_16x16x32_bf16 a[136:139], v[200:203], v[184:187], a[136:139]\n\t" \
  "s_waitcnt lgkmcnt(0)\n\t" \
  "v_mfma_f32_16x16x32_bf16 a[32:35], v[204:207], v[172:175], a[32:35]\n\t" \
  "v_mfma_f32_16x16x32_bf16 a[68:71], v[204:207], v[176:179], a[68:71]\n\t" \
  "v_mfma_f32_16x16x32_bf16 a[104:107], v[204:207], v[180:183], a[104:107]\n\t" \
  "v_mfma_f32_16x16x32_bf16 a[140:143], v[204:207], v[184:187], a[140:143]\n\t" \
  "s_mul_i32 s61, s73, 18432\n\t" \
  "v_add_u32 v208, s61, v244\n\t" \
  "s_xor_b32 s62, s73, 2\n\t" \
  "s_mul_i32 s61, s62, 18432\n\t" \
  "v_add_u32 v209, s61, v244\n\t" \
  "s_mul_i32 s61, s73, 3072\n\t" \
  "s_add_u32 s61, s61, 147456\n\t" \
  "v_add_u32 v210, s61, v244\n\t" \
  "s_mul_i32 s61, s62, 3072\n\t" \
  "s_add_u32 s61, s61, 147456\n\t" \
  "v_add_u32 v211, s61, v244\n\t" \
  "s_nop 7\n\t" \
  "s_cmp_lg_u32 s66, 0\n\t" \
  "s_cbranch_scc1 RH1_%=\n\t" \
  "ds_write_b128 v208, a[72:75] offset:0\n\t" \
  "v_accvgpr_read_b32 v32, a0\n\t" \
  "v_accvgpr_read_b32 v33, a1\n\t" \
  "v_accvgpr_read_b32 v34, a2\n\t" \
  "v_accvgpr_read_b32 v35, a3\n\t" \
  "ds_write_b128 v208, a[76:79] offset:1024\n\t" \
  "v_accvgpr_read_b32 v36, a4\n\t" \
  "v_accvgpr_read_b32 v37, a5\n\t" \
  "v_accvgpr_read_b32 v38, a6\n\t" \
  "v_accvgpr_read_b32 v39, a7\n\t" \
  "ds_write_b128 v208, a[80:83] offset:2048\n\t" \
  "v_accvgpr_read_b32 v40, a8\n\t" \
  "v_accvgpr_read_b32 v41, a9\n\t" \
  "v_accvgpr_read_b32 v42, a10\n\t" \
  "v_accvgpr_read_b32 v43, a11\n\t" \
  "ds_write_b128 v208, a[84:87] offset:3072\n\t" \
  "v_accvgpr_read_b32 v44, a12\n\t" \
  "v_accvgpr_read_b32 v45, a13\n\t" \
  "v_accvgpr_read_b32 v46, a14\n\t" \
  "v_accvgpr_read_b32 v47, a15\n\t" \
  "ds_write_b128 v208, a[88:91] offset:4096\n\t" \
  "v_accvgpr_read_b32 v48, a16\n\t" \
  "v_accvgpr_read_b32 v49, a17\n\t" \
  "v_accvgpr_read_b32 v50, a18\n\t" \
  "v_accvgpr_read_b32 v51, a19\n\t" \
  "ds_write_b128 v208, a[92:95] offset:5120\n\t" \
  "v_accvgpr_read_b32 v52, a20\n\t" \
  "v_accvgpr_read_b32 v53, a21\n\t" \
  "v_accvgpr_read_b32 v54, a22\n\t" \
  "v_accvgpr_read_b32 v55, a23\n\t" \
  "ds_write_b128 v208, a[96:99] offset:6144\n\t" \
  "v_accvgpr_read_b32 v56, a24\n\t" \
  "v_accvgpr_read_b32 v57, a25\n\t" \
  "v_accvgpr_read_b32 v58, a26\n\t" \
  "v_accvgpr_read_b32 v59, a27\n\t" \
  "ds_write_b128 v208, a[100:103] offset:7168\n\t" \
  "v_accvgpr_read_b32 v60, a28\n\t" \
  "v_accvgpr_read_b32 v61, a29\n\t" \
  "v_accvgpr_read_b32 v62, a30\n\t" \
  "v_accvgpr_read_b32 v63, a31\n\t" \
  "ds_write_b128 v208, a[104:107] offset:8192\n\t" \
  "v_accvgpr_read_b32 v64, a32\n\t" \
  "v_accvgpr_read_b32 v65, a33\n\t" \
  "v_accvgpr_read_b32 v66, a34\n\t" \
  "v_accvgpr_read_b32 v67, a35\n\t" \
  "ds_write_b128 v208, a[108:111] offset:9216\n\t" \
  "v_accvgpr_read_b32 v68, a36\n\t" \
  "v_accvgpr_read_b32 v69, a37\n\t" \
  "v_accvgpr_read_b32 v70, a38\n\t" \
  "v_accvgpr_read_b32 v71, a39\n\t" \
  "ds_write_b128 v208, a[112:115] offset:10240\n\t" \
  "v_accvgpr_read_b32 v72, a40\n\t" \
  "v_accvgpr_read_b32 v73, a41\n\t" \
  "v_accvgpr_read_b32 v74, a42\n\t" \
  "v_accvgpr_read_b32 v75, a43\n\t" \
  "ds_write_b128 v208, a[116:119] offset:11264\n\t" \
  "v_accvgpr_read_b32 v76, a44\n\t" \
  "v_accvgpr_read_b32 v77, a45\n\t" \
  "v_accvgpr_read_b32 v78, a46\n\t" \
  "v_accvgpr_read_b32 v79, a47\n\t" \
  "ds_write_b128 v208, a[120:123] offset:12288\n\t" \
  "v_accvgpr_read_b32 v80, a48\n\t" \
  "v_accvgpr_read_b32 v81, a49\n\t" \
  "v_accvgpr_read_b32 v82, a50\n\t" \
  "v_accvgpr_read_b32 v83, a51\n\t" \
  "ds_write_b128 v208, a[124:127] offset:13312\n\t" \
  "v_accvgpr_read_b32 v84, a52\n\t" \
  "v_accvgpr_read_b32 v85, a53\n\t" \
  "v_accvgpr_read_b32 v86, a54\n\t" \
  "v_accvgpr_read_b32 v87, a55\n\t" \
  "ds_write_b128 v208, a[128:131] offset:14336\n\t" \
  "v_accvgpr_read_b32 v88, a56\n\t" \
  "v_accvgpr_read_b32 v89, a57\n\t" \
  "v_accvgpr_read_b32 v90, a58\n\t" \
  "v_accvgpr_read_b32 v91, a59\n\t" \
  "s_waitcnt lgkmcnt(8)\n\t" \
  "ds_write_b128 v208, a[132:135] offset:15360\n\t" \
  "v_accvgpr_read_b32 v92, a60\n\t" \
  "v_accvgpr_read_b32 v93, a61\n\t" \
  "v_accvgpr_read_b32 v94, a62\n\t" \
  "v_accvgpr_read_b32 v95, a63\n\t" \
  "ds_write_b128 v208, a[136:139] offset:16384\n\t" \
  "v_accvgpr_read_b32 v96, a64\n\t" \
  "v_accvgpr_read_b32 v97, a65\n\t" \
  "v_accvgpr_read_b32 v98, a66\n\t" \
  "v_accvgpr_read_b32 v99, a67\n\t" \
  "ds_write_b128 v208, a[140:143] offset:17408\n\t" \
  "v_accvgpr_read_b32 v100, a68\n\t" \
  "v_accvgpr_read_b32 v101, a69\n\t" \
  "v_accvgpr_read_b32 v102, a70\n\t" \
  "v_accvgpr_read_b32 v103, a71\n\t" \
  "ds_write_b128 v210, a[156:159] offset:0\n\t" \
  "v_accvgpr_read_b32 v104, a144\n\t" \
  "v_accvgpr_read_b32 v105, a145\n\t" \
  "v_accvgpr_read_b32 v106, a146\n\t" \
  "v_accvgpr_read_b32 v107, a147\n\t" \
  "ds_write_b128 v210, a[160:163] offset:1024\n\t" \
  "v_accvgpr_read_b32 v108, a148\n\t" \
  "v_accvgpr_read_b32 v109, a149\n\t" \
  "v_accvgpr_read_b32 v110, a150\n\t" \
  "v_accvgpr_read_b32 v111, a151\n\t" \
  "v_accvgpr_read_b32 v112, a152\n\t" \
  "v_accvgpr_read_b32 v113, a153\n\t" \
  "v_accvgpr_read_b32 v114, a154\n\t" \
  "v_accvgpr_read_b32 v115, a155\n\t" \
  "s_waitcnt lgkmcnt(0)\n\t" \
  "s_barrier\n\t" \
  "ds_read_b128 v[116:119], v209 offset:0\n\t" \
  "ds_read_b128 v[120:123], v209 offset:1024\n\t" \
  "ds_read_b128 v[124:127], v209 offset:2048\n\t" \
  "ds_read_b128 v[128:131], v209 offset:3072\n\t" \
  "ds_read_b128 v[132:135], v209 offset:4096\n\t" \
  "ds_read_b128 v[136:139], v209 offset:5120\n\t" \
  "ds_read_b128 v[140:143], v209 offset:6144\n\t" \
  "s_waitcnt lgkmcnt(6)\n\t" \
  "v_add_f32 v32, v32, v116\n\t" \
  "v_add_f32 v33, v33, v117\n\t" \
  "v_add_f32 v34, v34, v118\n\t" \
  "v_add_f32 v35, v35, v119\n\t" \
  "v_cvt_pk_bf16_f32 v160, v32, v33\n\t" \
  "v_cvt_pk_bf16_f32 v161, v34, v35\n\t" \
  "ds_read_b128 v[144:147], v209 offset:7168\n\t" \
  "s_waitcnt lgkmcnt(6)\n\t" \
  "v_add_f32 v36, v36, v120\n\t" \
  "v_add_f32 v37, v37, v121\n\t" \
  "v_add_f32 v38, v38, v122\n\t" \
  "v_add_f32 v39, v39, v123\n\t" \
  "v_cvt_pk_bf16_f32 v162, v36, v37\n\t" \
  "v_cvt_pk_bf16_f32 v163, v38, v39\n\t" \
  "ds_read_b128 v[116:119], v209 offset:8192\n\t" \
  "s_waitcnt lgkmcnt(5)\n\t" \
  "v_add_f32 v40, v40, v124\n\t" \
  "v_add_f32 v41, v41, v125\n\t" \
  "v_add_f32 v42, v42, v126\n\t" \
  "v_add_f32 v43, v43, v127\n\t" \
  "v_cvt_pk_bf16_f32 v164, v40, v41\n\t" \
  "v_cvt_pk_bf16_f32 v165, v42, v43\n\t" \
  "ds_read_b128 v[120:123], v209 offset:9216\n\t" \
  "v_add_f32 v44, v44, v128\n\t" \
  "v_add_f32 v45, v45, v129\n\t" \
  "v_add_f32 v46, v46, v130\n\t" \
  "v_add_f32 v47, v47, v131\n\t" \
  "v_cvt_pk_bf16_f32 v166, v44, v45\n\t" \
  "v_cvt_pk_bf16_f32 v167, v46, v47\n\t" \
  "ds_read_b128 v[124:127], v209 offset:10240\n\t" \
  "s_waitcnt lgkmcnt(5)\n\t" \
  "v_add_f32 v48, v48, v132\n\t" \
  "v_add_f32 v49, v49, v133\n\t" \
  "v_add_f32 v50, v50, v134\n\t" \
  "v_add_f32 v51, v51, v135\n\t" \
  "v_cvt_pk_bf16_f32 v168, v48, v49\n\t" \
  "v_cvt_pk_bf16_f32 v169, v50, v51\n\t" \
  "ds_read_b128 v[128:131], v209 offset:11264\n\t" \
  "v_add_f32 v52, v52, v136\n\t" \
  "v_add_f32 v53, v53, v137\n\t" \
  "v_add_f32 v54, v54, v138\n\t" \
  "v_add_f32 v55, v55, v139\n\t" \
  "v_cvt_pk_bf16_f32 v170, v52, v53\n\t" \
  "v_cvt_pk_bf16_f32 v171, v54, v55\n\t" \
  "ds_read_b128 v[132:135], v209 offset:12288\n\t" \
  "s_waitcnt lgkmcnt(5)\n\t" \
  "v_add_f32 v56, v56, v140\n\t" \
  "v_add_f32 v57, v57, v141\n\t" \
  "v_add_f32 v58, v58, v142\n\t" \
  "v_add_f32 v59, v59, v143\n\t" \
  "v_cvt_pk_bf16_f32 v172, v56, v57\n\t" \
  "v_cvt_pk_bf16_f32 v173, v58, v59\n\t" \
  "ds_read_b128 v[136:139], v209 offset:13312\n\t" \
  "v_add_f32 v60, v60, v144\n\t" \
  "v_add_f32 v61, v61, v145\n\t" \
  "v_add_f32 v62, v62, v146\n\t" \
  "v_add_f32 v63, v63, v147\n\t" \
  "v_cvt_pk_bf16_f32 v174, v60, v61\n\t" \
  "v_cvt_pk_bf16_f32 v175, v62, v63\n\t" \
  "ds_read_b128 v[140:143], v209 offset:14336\n\t" \
  "s_waitcnt lgkmcnt(5)\n\t" \
  "v_add_f32 v64, v64, v116\n\t" \
  "v_add_f32 v65, v65, v117\n\t" \
  "v_add_f32 v66, v66, v118\n\t" \
  "v_add_f32 v67, v67, v119\n\t" \
  "v_cvt_pk_bf16_f32 v176, v64, v65\n\t" \
  "v_cvt_pk_bf16_f32 v177, v66, v67\n\t" \
  "ds_read_b128 v[144:147], v209 offset:15360\n\t" \
  "v_add_f32 v68, v68, v120\n\t" \
  "v_add_f32 v69, v69, v121\n\t" \
  "v_add_f32 v70, v70, v122\n\t" \
  "v_add_f32 v71, v71, v123\n\t" \
  "v_cvt_pk_bf16_f32 v178, v68, v69\n\t" \
  "v_cvt_pk_bf16_f32 v179, v70, v71\n\t" \
  "ds_read_b128 v[116:119], v209 offset:16384\n\t" \
  "s_waitcnt lgkmcnt(5)\n\t" \
  "v_add_f32 v72, v72, v124\n\t" \
  "v_add_f32 v73, v73, v125\n\t" \
  "v_add_f32 v74, v74, v126\n\t" \
  "v_add_f32 v75, v75, v127\n\t" \
  "v_cvt_pk_bf16_f32 v180, v72, v73\n\t" \
  "v_cvt_pk_bf16_f32 v181, v74, v75\n\t" \
  "ds_read_b128 v[120:123], v209 offset:17408\n\t" \
  "v_add_f32 v76, v76, v128\n\t" \
  "v_add_f32 v77, v77, v129\n\t" \
  "v_add_f32 v78, v78, v130\n\t" \
  "v_add_f32 v79, v79, v131\n\t" \
  "v_cvt_pk_bf16_f32 v182, v76, v77\n\t" \
  "v_cvt_pk_bf16_f32 v183, v78, v79\n\t" \
  "ds_read_b128 v[124:127], v211 offset:0\n\t" \
  "s_waitcnt lgkmcnt(5)\n\t" \
  "v_add_f32 v80, v80, v132\n\t" \
  "v_add_f32 v81, v81, v133\n\t" \
  "v_add_f32 v82, v82, v134\n\t" \
  "v_add_f32 v83, v83, v135\n\t" \
  "v_cvt_pk_bf16_f32 v184, v80, v81\n\t" \
  "v_cvt_pk_bf16_f32 v185, v82, v83\n\t" \
  "ds_read_b128 v[128:131], v211 offset:1024\n\t" \
  "v_add_f32 v84, v84, v136\n\t" \
  "v_add_f32 v85, v85, v137\n\t" \
  "v_add_f32 v86, v86, v138\n\t" \
  "v_add_f32 v87, v87, v139\n\t" \
  "v_cvt_pk_bf16_f32 v186, v84, v85\n\t" \
  "v_cvt_pk_bf16_f32 v187, v86, v87\n\t" \
  "ds_read_b128 v[132:135], v211 offset:2048\n\t" \
  "s_waitcnt lgkmcnt(5)\n\t" \
  "v_add_f32 v88, v88, v140\n\t" \
  "v_add_f32 v89, v89, v141\n\t" \
  "v_add_f32 v90, v90, v142\n\t" \
  "v_add_f32 v91, v91, v143\n\t" \
  "v_cvt_pk_bf16_f32 v188, v88, v89\n\t" \
  "v_cvt_pk_bf16_f32 v189, v90, v91\n\t" \
  "v_add_f32 v92, v92, v144\n\t" \
  "v_add_f32 v93, v93, v145\n\t" \
  "v_add_f32 v94, v94, v146\n\t" \
  "v_add_f32 v95, v95, v147\n\t" \
  "v_cvt_pk_bf16_f32 v190, v92, v93\n\t" \
  "v_cvt_pk_bf16_f32 v191, v94, v95\n\t" \
  "s_waitcnt lgkmcnt(3)\n\t" \
  "v_add_f32 v96, v96, v116\n\t" \
  "v_add_f32 v97, v97, v117\n\t" \
  "v_add_f32 v98, v98, v118\n\t" \
  "v_add_f32 v99, v99, v119\n\t" \
  "v_cvt_pk_bf16_f32 v192, v96, v97\n\t" \
  "v_cvt_pk_bf16_f32 v193, v98, v99\n\t" \
  "v_add_f32 v100, v100, v120\n\t" \
  "v_add_f32 v101, v101, v121\n\t" \
  "v_add_f32 v102, v102, v122\n\t" \
  "v_add_f32 v103, v103, v123\n\t" \
  "v_cvt_pk_bf16_f32 v194, v100, v101\n\t" \
  "v_cvt_pk_bf16_f32 v195, v102, v103\n\t" \
  "s_waitcnt lgkmcnt(1)\n\t" \
  "v_add_f32 v104, v104, v124\n\t" \
  "v_add_f32 v105, v105, v125\n\t" \
  "v_add_f32 v106, v106, v126\n\t" \
  "v_add_f32 v107, v107, v127\n\t" \
  "v_cvt_pk_bf16_f32 v196, v104, v105\n\t" \
  "v_cvt_pk_bf16_f32 v197, v106, v107\n\t" \
  "v_add_f32 v108, v108, v128\n\t" \
  "v_add_f32 v109, v109, v129\n\t" \
  "v_add_f32 v110, v110, v130\n\t" \
  "v_add_f32 v111, v111, v131\n\t" \
  "v_cvt_pk_bf16_f32 v198, v108, v109\n\t" \
  "v_cvt_pk_bf16_f32 v199, v110, v111\n\t" \
  "s_waitcnt lgkmcnt(0)\n\t" \
  "v_add_f32 v112, v112, v132\n\t" \
  "v_add_f32 v113, v113, v133\n\t" \
  "v_add_f32 v114, v114, v134\n\t" \
  "v_add_f32 v115, v115, v135\n\t" \
  "v_cvt_pk_bf16_f32 v200, v112, v113\n\t" \
  "v_cvt_pk_bf16_f32 v201, v114, v115\n\t" \
  "v_mov_b32 v202, 0\n\t" \
  "v_mov_b32 v203, 0\n\t" \
  "s_branch RD_%=\n\t" \
  "RH1_%=:\n\t" \
  "ds_write_b128 v208, a[0:3] offset:0\n\t" \
  "v_accvgpr_read_b32 v32, a72\n\t" \
  "v_accvgpr_read_b32 v33, a73\n\t" \
  "v_accvgpr_read_b32 v34, a74\n\t" \
  "v_accvgpr_read_b32 v35, a75\n\t" \
  "ds_write_b128 v208, a[4:7] offset:1024\n\t" \
  "v_accvgpr_read_b32 v36, a76\n\t" \
  "v_accvgpr_read_b32 v37, a77\n\t" \
  "v_accvgpr_read_b32 v38, a78\n\t" \
  "v_accvgpr_read_b32 v39, a79\n\t" \
  "ds_write_b128 v208, a[8:11] offset:2048\n\t" \
  "v_accvgpr_read_b32 v40, a80\n\t" \
  "v_accvgpr_read_b32 v41, a81\n\t" \
  "v_accvgpr_read_b32 v42, a82\n\t" \
  "v_accvgpr_read_b32 v43, a83\n\t" \
  "ds_write_b128 v208, a[12:15] offset:3072\n\t" \
  "v_accvgpr_read_b32 v44, a84\n\t" \
  "v_accvgpr_read_b32 v45, a85\n\t" \
  "v_accvgpr_read_b32 v46, a86\n\t" \
  "v_accvgpr_read_b32 v47, a87\n\t" \
  "ds_write_b128 v208, a[16:19] offset:4096\n\t" \
  "v_accvgpr_read_b32 v48, a88\n\t" \
  "v_accvgpr_read_b32 v49, a89\n\t" \
  "v_accvgpr_read_b32 v50, a90\n\t" \
  "v_accvgpr_read_b32 v51, a91\n\t" \
  "ds_write_b128 v208, a[20:23] offset:5120\n\t" \
  "v_accvgpr_read_b32 v52, a92\n\t" \
  "v_accvgpr_read_b32 v53, a93\n\t" \
  "v_accvgpr_read_b32 v54, a94\n\t" \
  "v_accvgpr_read_b32 v55, a95\n\t" \
  "ds_write_b128 v208, a[24:27] offset:6144\n\t" \
  "v_accvgpr_read_b32 v56, a96\n\t" \
  "v_accvgpr_read_b32 v57, a97\n\t" \
  "v_accvgpr_read_b32 v58, a98\n\t" \
  "v_accvgpr_read_b32 v59, a99\n\t" \
  "ds_write_b128 v208, a[28:31] offset:7168\n\t" \
  "v_accvgpr_read_b32 v60, a100\n\t" \
  "v_accvgpr_read_b32 v61, a101\n\t" \
  "v_accvgpr_read_b32 v62, a102\n\t" \
  "v_accvgpr_read_b32 v63, a103\n\t" \
  "ds_write_b128 v208, a[32:35] offset:8192\n\t" \
  "v_accvgpr_read_b32 v64, a104\n\t" \
  "v_accvgpr_read_b32 v65, a105\n\t" \
  "v_accvgpr_read_b32 v66, a106\n\t" \
  "v_accvgpr_read_b32 v67, a107\n\t" \
  "ds_write_b128 v208, a[36:39] offset:9216\n\t" \
  "v_accvgpr_read_b32 v68, a108\n\t" \
  "v_accvgpr_read_b32 v69, a109\n\t" \
  "v_accvgpr_read_b32 v70, a110\n\t" \
  "v_accvgpr_read_b32 v71, a111\n\t" \
  "ds_write_b128 v208, a[40:43] offset:10240\n\t" \
  "v_accvgpr_read_b32 v72, a112\n\t" \
  "v_accvgpr_read_b32 v73, a113\n\t" \
  "v_accvgpr_read_b32 v74, a114\n\t" \
  "v_accvgpr_read_b32 v75, a115\n\t" \
  "ds_write_b128 v208, a[44:47] offset:11264\n\t" \
  "v_accvgpr_read_b32 v76, a116\n\t" \
  "v_accvgpr_read_b32 v77, a117\n\t" \
  "v_accvgpr_read_b32 v78, a118\n\t" \
  "v_accvgpr_read_b32 v79, a119\n\t" \
  "ds_write_b128 v208, a[48:51] offset:12288\n\t" \
  "v_accvgpr_read_b32 v80, a120\n\t" \
  "v_accvgpr_read_b32 v81, a121\n\t" \
  "v_accvgpr_read_b32 v82, a122\n\t" \
  "v_accvgpr_read_b32 v83, a123\n\t" \
  "ds_write_b128 v208, a[52:55] offset:13312\n\t" \
  "v_accvgpr_read_b32 v84, a124\n\t" \
  "v_accvgpr_read_b32 v85, a125\n\t" \
  "v_accvgpr_read_b32 v86, a126\n\t" \
  "v_accvgpr_read_b32 v87, a127\n\t" \
  "ds_write_b128 v208, a[56:59] offset:14336\n\t" \
  "v_accvgpr_read_b32 v88, a128\n\t" \
  "v_accvgpr_read_b32 v89, a129\n\t" \
  "v_accvgpr_read_b32 v90, a130\n\t" \
  "v_accvgpr_read_b32 v91, a131\n\t" \
  "s_waitcnt lgkmcnt(8)\n\t" \
  "ds_write_b128 v208, a[60:63] offset:15360\n\t" \
  "v_accvgpr_read_b32 v92, a132\n\t" \
  "v_accvgpr_read_b32 v93, a133\n\t" \
  "v_accvgpr_read_b32 v94, a134\n\t" \
  "v_accvgpr_read_b32 v95, a135\n\t" \
  "ds_write_b128 v208, a[64:67] offset:16384\n\t" \
  "v_accvgpr_read_b32 v96, a136\n\t" \
  "v_accvgpr_read_b32 v97, a137\n\t" \
  "v_accvgpr_read_b32 v98, a138\n\t" \
  "v_accvgpr_read_b32 v99, a139\n\t" \
  "ds_write_b128 v208, a[68:71] offset:17408\n\t" \
  "v_accvgpr_read_b32 v100, a140\n\t" \
  "v_accvgpr_read_b32 v101, a141\n\t" \
  "v_accvgpr_read_b32 v102, a142\n\t" \
  "v_accvgpr_read_b32 v103, a143\n\t" \
  "ds_write_b128 v210, a[144:147] offset:0\n\t" \
  "v_accvgpr_read_b32 v104, a156\n\t" \
  "v_accvgpr_read_b32 v105, a157\n\t" \
  "v_accvgpr_read_b32 v106, a158\n\t" \
  "v_accvgpr_read_b32 v107, a159\n\t" \
  "ds_write_b128 v210, a[148:151] offset:1024\n\t" \
  "v_accvgpr_read_b32 v108, a160\n\t" \
  "v_accvgpr_read_b32 v109, a161\n\t" \
  "v_accvgpr_read_b32 v110, a162\n\t" \
  "v_accvgpr_read_b32 v111, a163\n\t" \
  "ds_write_b128 v210, a[152:155] offset:2048\n\t" \
  "s_waitcnt lgkmcnt(0)\n\t" \
  "s_barrier\n\t" \
  "ds_read_b128 v[116:119], v209 offset:0\n\t" \
  "ds_read_b128 v[120:123], v209 offset:1024\n\t" \
  "ds_read_b128 v[124:127], v209 offset:2048\n\t" \
  "ds_read_b128 v[128:131], v209 offset:3072\n\t" \
  "ds_read_b128 v[132:135], v209 offset:4096\n\t" \
  "ds_read_b128 v[136:139], v209 offset:5120\n\t" \
  "ds_read_b128 v[140:143], v209 offset:6144\n\t" \
  "s_waitcnt lgkmcnt(6)\n\t" \
  "v_add_f32 v32, v32, v116\n\t" \
  "v_add_f32 v33, v33, v117\n\t" \
  "v_add_f32 v34, v34, v118\n\t" \
  "v_add_f32 v35, v35, v119\n\t" \
  "v_cvt_pk_bf16_f32 v160, v32, v33\n\t" \
  "v_cvt_pk_bf16_f32 v161, v34, v35\n\t" \
  "ds_read_b128 v[144:147], v209 offset:7168\n\t" \
  "s_waitcnt lgkmcnt(6)\n\t" \
  "v_add_f32 v36, v36, v120\n\t" \
  "v_add_f32 v37, v37, v121\n\t" \
  "v_add_f32 v38, v38, v122\n\t" \
  "v_add_f32 v39, v39, v123\n\t" \
  "v_cvt_pk_bf16_f32 v162, v36, v37\n\t" \
  "v_cvt_pk_bf16_f32 v163, v38, v39\n\t" \
  "ds_read_b128 v[116:119], v209 offset:8192\n\t" \
  "s_waitcnt lgkmcnt(5)\n\t" \
  "v_add_f32 v40, v40, v124\n\t" \
  "v_add_f32 v41, v41, v125\n\t" \
  "v_add_f32 v42, v42, v126\n\t" \
  "v_add_f32 v43, v43, v127\n\t" \
  "v_cvt_pk_bf16_f32 v164, v40, v41\n\t" \
  "v_cvt_pk_bf16_f32 v165, v42, v43\n\t" \
  "ds_read_b128 v[120:123], v209 offset:9216\n\t" \
  "v_add_f32 v44, v44, v128\n\t" \
  "v_add_f32 v45, v45, v129\n\t" \
  "v_add_f32 v46, v46, v130\n\t" \
  "v_add_f32 v47, v47, v131\n\t" \
  "v_cvt_pk_bf16_f32 v166, v44, v45\n\t" \
  "v_cvt_pk_bf16_f32 v167, v46, v47\n\t" \
  "ds_read_b128 v[124:127], v209 offset:10240\n\t" \
  "s_waitcnt lgkmcnt(5)\n\t" \
  "v_add_f32 v48, v48, v132\n\t" \
  "v_add_f32 v49, v49, v133\n\t" \
  "v_add_f32 v50, v50, v134\n\t" \
  "v_add_f32 v51, v51, v135\n\t" \
  "v_cvt_pk_bf16_f32 v168, v48, v49\n\t" \
  "v_cvt_pk_bf16_f32 v169, v50, v51\n\t" \
  "ds_read_b128 v[128:131], v209 offset:11264\n\t" \
  "v_add_f32 v52, v52, v136\n\t" \
  "v_add_f32 v53, v53, v137\n\t" \
  "v_add_f32 v54, v54, v138\n\t" \
  "v_add_f32 v55, v55, v139\n\t" \
  "v_cvt_pk_bf16_f32 v170, v52, v53\n\t" \
  "v_cvt_pk_bf16_f32 v171, v54, v55\n\t" \
  "ds_read_b128 v[132:135], v209 offset:12288\n\t" \
  "s_waitcnt lgkmcnt(5)\n\t" \
  "v_add_f32 v56, v56, v140\n\t" \
  "v_add_f32 v57, v57, v141\n\t" \
  "v_add_f32 v58, v58, v142\n\t" \
  "v_add_f32 v59, v59, v143\n\t" \
  "v_cvt_pk_bf16_f32 v172, v56, v57\n\t" \
  "v_cvt_pk_bf16_f32 v173, v58, v59\n\t" \
  "ds_read_b128 v[136:139], v209 offset:13312\n\t" \
  "v_add_f32 v60, v60, v144\n\t" \
  "v_add_f32 v61, v61, v145\n\t" \
  "v_add_f32 v62, v62, v146\n\t" \
  "v_add_f32 v63, v63, v147\n\t" \
  "v_cvt_pk_bf16_f32 v174, v60, v61\n\t" \
  "v_cvt_pk_bf16_f32 v175, v62, v63\n\t" \
  "ds_read_b128 v[140:143], v209 offset:14336\n\t" \
  "s_waitcnt lgkmcnt(5)\n\t" \
  "v_add_f32 v64, v64, v116\n\t" \
  "v_add_f32 v65, v65, v117\n\t" \
  "v_add_f32 v66, v66, v118\n\t" \
  "v_add_f32 v67, v67, v119\n\t" \
  "v_cvt_pk_bf16_f32 v176, v64, v65\n\t" \
  "v_cvt_pk_bf16_f32 v177, v66, v67\n\t" \
  "ds_read_b128 v[144:147], v209 offset:15360\n\t" \
  "v_add_f32 v68, v68, v120\n\t" \
  "v_add_f32 v69, v69, v121\n\t" \
  "v_add_f32 v70, v70, v122\n\t" \
  "v_add_f32 v71, v71, v123\n\t" \
  "v_cvt_pk_bf16_f32 v178, v68, v69\n\t" \
  "v_cvt_pk_bf16_f32 v179, v70, v71\n\t" \
  "ds_read_b128 v[116:119], v209 offset:16384\n\t" \
  "s_waitcnt lgkmcnt(5)\n\t" \
  "v_add_f32 v72, v72, v124\n\t" \
  "v_add_f32 v73, v73, v125\n\t" \
  "v_add_f32 v74, v74, v126\n\t" \
  "v_add_f32 v75, v75, v127\n\t" \
  "v_cvt_pk_bf16_f32 v180, v72, v73\n\t" \
  "v_cvt_pk_bf16_f32 v181, v74, v75\n\t" \
  "ds_read_b128 v[120:123], v209 offset:17408\n\t" \
  "v_add_f32 v76, v76, v128\n\t" \
  "v_add_f32 v77, v77, v129\n\t" \
  "v_add_f32 v78, v78, v130\n\t" \
  "v_add_f32 v79, v79, v131\n\t" \
  "v_cvt_pk_bf16_f32 v182, v76, v77\n\t" \
  "v_cvt_pk_bf16_f32 v183, v78, v79\n\t" \
  "ds_read_b128 v[124:127], v211 offset:0\n\t" \
  "s_waitcnt lgkmcnt(5)\n\t" \
  "v_add_f32 v80, v80, v132\n\t" \
  "v_add_f32 v81, v81, v133\n\t" \
  "v_add_f32 v82, v82, v134\n\t" \
  "v_add_f32 v83, v83, v135\n\t" \
  "v_cvt_pk_bf16_f32 v184, v80, v81\n\t" \
  "v_cvt_pk_bf16_f32 v185, v82, v83\n\t" \
  "ds_read_b128 v[128:131], v211 offset:1024\n\t" \
  "v_add_f32 v84, v84, v136\n\t" \
  "v_add_f32 v85, v85, v137\n\t" \
  "v_add_f32 v86, v86, v138\n\t" \
  "v_add_f32 v87, v87, v139\n\t" \
  "v_cvt_pk_bf16_f32 v186, v84, v85\n\t" \
  "v_cvt_pk_bf16_f32 v187, v86, v87\n\t" \
  "s_waitcnt lgkmcnt(4)\n\t" \
  "v_add_f32 v88, v88, v140\n\t" \
  "v_add_f32 v89, v89, v141\n\t" \
  "v_add_f32 v90, v90, v142\n\t" \
  "v_add_f32 v91, v91, v143\n\t" \
  "v_cvt_pk_bf16_f32 v188, v88, v89\n\t" \
  "v_cvt_pk_bf16_f32 v189, v90, v91\n\t" \
  "v_add_f32 v92, v92, v144\n\t" \
  "v_add_f32 v93, v93, v145\n\t" \
  "v_add_f32 v94, v94, v146\n\t" \
  "v_add_f32 v95, v95, v147\n\t" \
  "v_cvt_pk_bf16_f32 v190, v92, v93\n\t" \
  "v_cvt_pk_bf16_f32 v191, v94, v95\n\t" \
  "s_waitcnt lgkmcnt(2)\n\t" \
  "v_add_f32 v96, v96, v116\n\t" \
  "v_add_f32 v97, v97, v117\n\t" \
  "v_add_f32 v98, v98, v118\n\t" \
  "v_add_f32 v99, v99, v119\n\t" \
  "v_cvt_pk_bf16_f32 v192, v96, v97\n\t" \
  "v_cvt_pk_bf16_f32 v193, v98, v99\n\t" \
  "v_add_f32 v100, v100, v120\n\t" \
  "v_add_f32 v101, v101, v121\n\t" \
  "v_add_f32 v102, v102, v122\n\t" \
  "v_add_f32 v103, v103, v123\n\t" \
  "v_cvt_pk_bf16_f32 v194, v100, v101\n\t" \
  "v_cvt_pk_bf16_f32 v195, v102, v103\n\t" \
  "s_waitcnt lgkmcnt(0)\n\t" \
  "v_add_f32 v104, v104, v124\n\t" \
  "v_add_f32 v105, v105, v125\n\t" \
  "v_add_f32 v106, v106, v126\n\t" \
  "v_add_f32 v107, v107, v127\n\t" \
  "v_cvt_pk_bf16_f32 v196, v104, v105\n\t" \
  "v_cvt_pk_bf16_f32 v197, v106, v107\n\t" \
  "v_add_f32 v108, v108, v128\n\t" \
  "v_add_f32 v109, v109, v129\n\t" \
  "v_add_f32 v110, v110, v130\n\t" \
  "v_add_f32 v111, v111, v131\n\t" \
  "v_cvt_pk_bf16_f32 v198, v108, v109\n\t" \
  "v_cvt_pk_bf16_f32 v199, v110, v111\n\t" \
  "v_mov_b32 v200, 0\n\t" \
  "v_mov_b32 v201, 0\n\t" \
  "v_mov_b32 v202, 0\n\t" \
  "v_mov_b32 v203, 0\n\t" \
  "RD_%=:\n\t" \
  "s_mul_i32 s75, s73, 49152\n\t" \
  "s_mul_i32 s61, s64, 12288\n\t" \
  "s_add_u32 s62, s75, s61\n\t" \
  "buffer_store_dwordx4 v[160:163], v244, s[44:47], s62 offen sc1\n\t" \
  "buffer_store_dwordx4 v[164:167], v244, s[44:47], s62 offen offset:1024 sc1\n\t" \
  "buffer_store_dwordx4 v[168:171], v244, s[44:47], s62 offen offset:2048 sc1\n\t" \
  "buffer_store_dwordx4 v[172:175], v244, s[44:47], s62 offen offset:3072 sc1\n\t" \
  "s_add_u32 s62, s62, 4096\n\t" \
  "buffer_store_dwordx4 v[176:179], v244, s[44:47], s62 offen sc1\n\t" \
  "buffer_store_dwordx4 v[180:183], v244, s[44:47], s62 offen offset:1024 sc1\n\t" \
  "buffer_store_dwordx4 v[184:187], v244, s[44:47], s62 offen offset:2048 sc1\n\t" \
  "buffer_store_dwordx4 v[188:191], v244, s[44:47], s62 offen offset:3072 sc1\n\t" \
  "s_add_u32 s62, s62, 4096\n\t" \
  "buffer_store_dwordx4 v[192:195], v244, s[44:47], s62 offen sc1\n\t" \
  "buffer_store_dwordx4 v[196:199], v244, s[44:47], s62 offen offset:1024 sc1\n\t" \
  "buffer_store_dwordx4 v[200:203], v244, s[44:47], s62 offen offset:2048 sc1\n\t" \
  "v_lshrrev_b32 v248, 4, v244\n\t" \
  "v_and_b32 v249, 15, v248\n\t" \
  "v_lshrrev_b32 v248, 4, v248\n\t" \
  "v_mul_u32_u24 v249, 0x900, v249\n\t" \
  "v_lshl_add_u32 v247, v248, 3, v249\n\t" \
  "s_lshl_b32 s61, s64, 1\n\t" \
  "s_add_u32 s61, s61, 0\n\t" \
  "s_cmp_ge_u32 s61, 9\n\t" \
  "s_cselect_b32 s76, 1, 0\n\t" \
  "s_mul_i32 s63, s76, 9\n\t" \
  "s_sub_u32 s63, s61, s63\n\t" \
  "s_lshl_b32 s68, s66, 1\n\t" \
  "s_add_u32 s76, s76, s68\n\t" \
  "s_lshl_b32 s68, s85, 2\n\t" \
  "s_add_u32 s76, s76, s68\n\t" \
  "s_mul_i32 s68, s66, 3\n\t" \
  "s_add_u32 s68, s68, s61\n\t" \
  "s_sub_u32 s68, s68, 18\n\t" \
  "s_cmp_ge_u32 s61, 18\n\t" \
  "s_cselect_b32 s76, 8, s76\n\t" \
  "s_cselect_b32 s63, s68, s63\n\t" \
  "s_add_u32 s68, s61, s66\n\t" \
  "s_lshl_b32 s69, s85, 2\n\t" \
  "s_add_u32 s63, s63, s69\n\t" \
  "s_cmp_ge_u32 s63, 9\n\t" \
  "s_cselect_b32 s69, 9, 0\n\t" \
  "s_sub_u32 s63, s63, s69\n\t" \
  "s_mul_i32 s76, s76, 36864\n\t" \
  "s_lshl_b32 s63, s63, 5\n\t" \
  "s_add_u32 s76, s76, s63\n\t" \
  "s_cmp_gt_u32 s68, 20\n\t" \
  "s_cselect_b32 s76, 0x7f000000, s76\n\t" \
  "s_lshl_b32 s61, s64, 1\n\t" \
  "s_add_u32 s61, s61, 1\n\t" \
  "s_cmp_ge_u32 s61, 9\n\t" \
  "s_cselect_b32 s77, 1, 0\n\t" \
  "s_mul_i32 s63, s77, 9\n\t" \
  "s_sub_u32 s63, s61, s63\n\t" \
  "s_lshl_b32 s68, s66, 1\n\t" \
  "s_add_u32 s77, s77, s68\n\t" \
  "s_lshl_b32 s68, s85, 2\n\t" \
  "s_add_u32 s77, s77, s68\n\t" \
  "s_mul_i32 s68, s66, 3\n\t" \
  "s_add_u32 s68, s68, s61\n\t" \
  "s_sub_u32 s68, s68, 18\n\t" \
  "s_cmp_ge_u32 s61, 18\n\t" \
  "s_cselect_b32 s77, 8, s77\n\t" \
  "s_cselect_b32 s63, s68, s63\n\t" \
  "s_add_u32 s68, s61, s66\n\t" \
  "s_lshl_b32 s69, s85, 2\n\t" \
  "s_add_u32 s63, s63, s69\n\t" \
  "s_cmp_ge_u32 s63, 9\n\t" \
  "s_cselect_b32 s69, 9, 0\n\t" \
  "s_sub_u32 s63, s63, s69\n\t" \
  "s_mul_i32 s77, s77, 36864\n\t" \
  "s_lshl_b32 s63, s63, 5\n\t" \
  "s_add_u32 s77, s77, s63\n\t" \
  "s_cmp_gt_u32 s68, 20\n\t" \
  "s_cselect_b32 s77, 0x7f000000, s77\n\t" \
  "s_lshl_b32 s61, s64, 1\n\t" \
  "s_add_u32 s61, s61, 8\n\t" \
  "s_cmp_ge_u32 s61, 9\n\t" \
  "s_cselect_b32 s78, 1, 0\n\t" \
  "s_mul_i32 s63, s78, 9\n\t" \
  "s_sub_u32 s63, s61, s63\n\t" \
  "s_lshl_b32 s68, s66, 1\n\t" \
  "s_add_u32 s78, s78, s68\n\t" \
  "s_lshl_b32 s68, s85, 2\n\t" \
  "s_add_u32 s78, s78, s68\n\t" \
  "s_mul_i32 s68, s66, 3\n\t" \
  "s_add_u32 s68, s68, s61\n\t" \
  "s_sub_u32 s68, s68, 18\n\t" \
  "s_cmp_ge_u32 s61, 18\n\t" \
  "s_cselect_b32 s78, 8, s78\n\t" \
  "s_cselect_b32 s63, s68, s63\n\t" \
  "s_add_u32 s68, s61, s66\n\t" \
  "s_lshl_b32 s69, s85, 2\n\t" \
  "s_add_u32 s63, s63, s69\n\t" \
  "s_cmp_ge_u32 s63, 9\n\t" \
  "s_cselect_b32 s69, 9, 0\n\t" \
  "s_sub_u32 s63, s63, s69\n\t" \
  "s_mul_i32 s78, s78, 36864\n\t" \
  "s_lshl_b32 s63, s63, 5\n\t" \
  "s_add_u32 s78, s78, s63\n\t" \
  "s_cmp_gt_u32 s68, 20\n\t" \
  "s_cselect_b32 s78, 0x7f000000, s78\n\t" \
  "s_lshl_b32 s61, s64, 1\n\t" \
  "s_add_u32 s61, s61, 9\n\t" \
  "s_cmp_ge_u32 s61, 9\n\t" \
  "s_cselect_b32 s80, 1, 0\n\t" \
  "s_mul_i32 s63, s80, 9\n\t" \
  "s_sub_u32 s63, s61, s63\n\t" \
  "s_lshl_b32 s68, s66, 1\n\t" \
  "s_add_u32 s80, s80, s68\n\t" \
  "s_lshl_b32 s68, s85, 2\n\t" \
  "s_add_u32 s80, s80, s68\n\t" \
  "s_mul_i32 s68, s66, 3\n\t" \
  "s_add_u32 s68, s68, s61\n\t" \
  "s_sub_u32 s68, s68, 18\n\t" \
  "s_cmp_ge_u32 s61, 18\n\t" \
  "s_cselect_b32 s80, 8, s80\n\t" \
  "s_cselect_b32 s63, s68, s63\n\t" \
  "s_add_u32 s68, s61, s66\n\t" \
  "s_lshl_b32 s69, s85, 2\n\t" \
  "s_add_u32 s63, s63, s69\n\t" \
  "s_cmp_ge_u32 s63, 9\n\t" \
  "s_cselect_b32 s69, 9, 0\n\t" \
  "s_sub_u32 s63, s63, s69\n\t" \
  "s_mul_i32 s80, s80, 36864\n\t" \
  "s_lshl_b32 s63, s63, 5\n\t" \
  "s_add_u32 s80, s80, s63\n\t" \
  "s_cmp_gt_u32 s68, 20\n\t" \
  "s_cselect_b32 s80, 0x7f000000, s80\n\t" \
  "s_lshl_b32 s61, s64, 1\n\t" \
  "s_add_u32 s61, s61, 16\n\t" \
  "s_cmp_ge_u32 s61, 9\n\t" \
  "s_cselect_b32 s81, 1, 0\n\t" \
  "s_mul_i32 s63, s81, 9\n\t" \
  "s_sub_u32 s63, s61, s63\n\t" \
  "s_lshl_b32 s68, s66, 1\n\t" \
  "s_add_u32 s81, s81, s68\n\t" \
  "s_lshl_b32 s68, s85, 2\n\t" \
  "s_add_u32 s81, s81, s68\n\t" \
  "s_mul_i32 s68, s66, 3\n\t" \
  "s_add_u32 s68, s68, s61\n\t" \
  "s_sub_u32 s68, s68, 18\n\t" \
  "s_cmp_ge_u32 s61, 18\n\t" \
  "s_cselect_b32 s81, 8, s81\n\t" \
  "s_cselect_b32 s63, s68, s63\n\t" \
  "s_add_u32 s68, s61, s66\n\t" \
  "s_lshl_b32 s69, s85, 2\n\t" \
  "s_add_u32 s63, s63, s69\n\t" \
  "s_cmp_ge_u32 s63, 9\n\t" \
  "s_cselect_b32 s69, 9, 0\n\t" \
  "s_sub_u32 s63, s63, s69\n\t" \
  "s_mul_i32 s81, s81, 36864\n\t" \
  "s_lshl_b32 s63, s63, 5\n\t" \
  "s_add_u32 s81, s81, s63\n\t" \
  "s_cmp_gt_u32 s68, 20\n\t" \
  "s_cselect_b32 s81, 0x7f000000, s81\n\t" \
  "s_lshl_b32 s61, s64, 1\n\t" \
  "s_add_u32 s61, s61, 17\n\t" \
  "s_cmp_ge_u32 s61, 9\n\t" \
  "s_cselect_b32 s82, 1, 0\n\t" \
  "s_mul_i32 s63, s82, 9\n\t" \
  "s_sub_u32 s63, s61, s63\n\t" \
  "s_lshl_b32 s68, s66, 1\n\t" \
  "s_add_u32 s82, s82, s68\n\t" \
  "s_lshl_b32 s68, s85, 2\n\t" \
  "s_add_u32 s82, s82, s68\n\t" \
  "s_mul_i32 s68, s66, 3\n\t" \
  "s_add_u32 s68, s68, s61\n\t" \
  "s_sub_u32 s68, s68, 18\n\t" \
  "s_cmp_ge_u32 s61, 18\n\t" \
  "s_cselect_b32 s82, 8, s82\n\t" \
  "s_cselect_b32 s63, s68, s63\n\t" \
  "s_add_u32 s68, s61, s66\n\t" \
  "s_lshl_b32 s69, s85, 2\n\t" \
  "s_add_u32 s63, s63, s69\n\t" \
  "s_cmp_ge_u32 s63, 9\n\t" \
  "s_cselect_b32 s69, 9, 0\n\t" \
  "s_sub_u32 s63, s63, s69\n\t" \
  "s_mul_i32 s82, s82, 36864\n\t" \
  "s_lshl_b32 s63, s63, 5\n\t" \
  "s_add_u32 s82, s82, s63\n\t" \
  "s_cmp_gt_u32 s68, 20\n\t" \
  "s_cselect_b32 s82, 0x7f000000, s82\n\t" \
  "s_waitcnt vmcnt(0)\n\t" \
  "s_barrier\n\t" \
  "s_cmp_lg_u32 s73, 0\n\t" \
  "s_cbranch_scc1 T4W_%=\n\t" \
  "s_add_u32 s61, s79, 4096\n\t" \
  "v_mov_b32 v250, s52\n\t" \
  "v_mov_b32 v251, s53\n\t" \
  "v_mov_b32 v254, 0\n\t" \
  "s_mov_b64 exec, 1\n\t" \
  "s_nop 1\n\t" \
  "buffer_store_dwordx2 v[250:251], v254, s[96:99], s61 offen sc1\n\t" \
  "s_mov_b64 exec, -1\n\t" \
  "s_nop 4\n\t" \
  "P4L_%=:\n\t" \
  "buffer_load_dwordx2 v[250:251], v253, s[96:99], 0 offen sc1\n\t" \
  "s_waitcnt vmcnt(0)\n\t" \
  "v_cmp_eq_u64 vcc, s[52:53], v[250:251]\n\t" \
  "s_nop 4\n\t" \
  "s_cmp_eq_u64 vcc, exec\n\t" \
  "s_cbranch_scc1 P4D_%=\n\t" \
  "s_sleep 2\n\t" \
  "s_add_u32 s74, s74, 1\n\t" \
  "s_cmp_lt_u32 s74, 0x40000\n\t" \
  "s_cbranch_scc1 P4L_%=\n\t" \
  "P4D_%=:\n\t" \
  "s_or_b32 s61, s64, s65\n\t" \
  "s_cmp_lg_u32 s61, 0\n\t" \
  "s_cbranch_scc1 T4W_%=\n\t" \
  "v_mov_b32 v250, s52\n\t" \
  "v_mov_b32 v251, s53\n\t" \
  "v_mov_b32 v254, 0\n\t" \
  "s_mov_b64 exec, 1\n\t" \
  "s_nop 1\n\t" \
  "buffer_store_dwordx2 v[250:251], v254, s[96:99], 0 offen sc1\n\t" \
  "s_mov_b64 exec, -1\n\t" \
  "s_nop 4\n\t" \
  "T4W_%=:\n\t" \
  "s_barrier\n\t" \
  "s_lshl_b32 s61, s64, 10\n\t" \
  "s_add_u32 s62, s75, s61\n\t" \
  "s_add_u32 s63, s62, 0\n\t" \
  "buffer_load_dwordx4 v[32:35], v244, s[44:47], s63 offen sc1\n\t" \
  "s_add_u32 s63, s62, 12288\n\t" \
  "buffer_load_dwordx4 v[36:39], v244, s[44:47], s63 offen sc1\n\t" \
  "s_add_u32 s63, s62, 24576\n\t" \
  "buffer_load_dwordx4 v[40:43], v244, s[44:47], s63 offen sc1\n\t" \
  "s_add_u32 s63, s62, 36864\n\t" \
  "buffer_load_dwordx4 v[44:47], v244, s[44:47], s63 offen sc1\n\t" \
  "s_add_u32 s63, s62, 4096\n\t" \
  "buffer_load_dwordx4 v[48:51], v244, s[44:47], s63 offen sc1\n\t" \
  "s_add_u32 s63, s62, 16384\n\t" \
  "buffer_load_dwordx4 v[52:55], v244, s[44:47], s63 offen sc1\n\t" \
  "s_add_u32 s63, s62, 28672\n\t" \
  "buffer_load_dwordx4 v[56:59], v244, s[44:47], s63 offen sc1\n\t" \
  "s_add_u32 s63, s62, 40960\n\t" \
  "buffer_load_dwordx4 v[60:63], v244, s[44:47], s63 offen sc1\n\t" \
  "s_add_u32 s63, s62, 8192\n\t" \
  "buffer_load_dwordx4 v[64:67], v244, s[44:47], s63 offen sc1\n\t" \
  "s_add_u32 s63, s62, 20480\n\t" \
  "buffer_load_dwordx4 v[68:71], v244, s[44:47], s63 offen sc1\n\t" \
  "s_add_u32 s63, s62, 32768\n\t" \
  "buffer_load_dwordx4 v[72:75], v244, s[44:47], s63 offen sc1\n\t" \
  "s_add_u32 s63, s62, 45056\n\t" \
  "buffer_load_dwordx4 v[76:79], v244, s[44:47], s63 offen sc1\n\t" \
  "s_waitcnt vmcnt(11)\n\t" \
  "v_lshlrev_b32 v100, 16, v32\n\t" \
  "v_and_b32 v101, 0xffff0000, v32\n\t" \
  "v_lshlrev_b32 v102, 16, v33\n\t" \
  "v_and_b32 v103, 0xffff0000, v33\n\t" \
  "s_waitcnt vmcnt(10)\n\t" \
  "v_lshlrev_b32 v104, 16, v36\n\t" \
  "v_and_b32 v105, 0xffff0000, v36\n\t" \
  "v_lshlrev_b32 v106, 16, v37\n\t" \
  "v_and_b32 v107, 0xffff0000, v37\n\t" \
  "v_add_f32 v100, v100, v104\n\t" \
  "v_add_f32 v101, v101, v105\n\t" \
  "v_add_f32 v102, v102, v106\n\t" \
  "v_add_f32 v103, v103, v107\n\t" \
  "s_waitcnt vmcnt(9)\n\t" \
  "v_lshlrev_b32 v104, 16, v40\n\t" \
  "v_and_b32 v105, 0xffff0000, v40\n\t" \
  "v_lshlrev_b32 v106, 16, v41\n\t" \
  "v_and_b32 v107, 0xffff0000, v41\n\t" \
  "v_add_f32 v100, v100, v104\n\t" \
  "v_add_f32 v101, v101, v105\n\t" \
  "v_add_f32 v102, v102, v106\n\t" \
  "v_add_f32 v103, v103, v107\n\t" \
  "s_waitcnt vmcnt(8)\n\t" \
  "v_lshlrev_b32 v104, 16, v44\n\t" \
  "v_and_b32 v105, 0xffff0000, v44\n\t" \
  "v_lshlrev_b32 v106, 16, v45\n\t" \
  "v_and_b32 v107, 0xffff0000, v45\n\t" \
  "v_add_f32 v100, v100, v104\n\t" \
  "v_add_f32 v101, v101, v105\n\t" \
  "v_add_f32 v102, v102, v106\n\t" \
  "v_add_f32 v103, v103, v107\n\t" \
  "v_cvt_pk_bf16_f32 v120, v100, v101\n\t" \
  "v_cvt_pk_bf16_f32 v121, v102, v103\n\t" \
  "buffer_store_dwordx2 v[120:121], v247, s[48:51], s76 offen\n\t" \
  "v_lshlrev_b32 v108, 16, v34\n\t" \
  "v_and_b32 v109, 0xffff0000, v34\n\t" \
  "v_lshlrev_b32 v110, 16, v35\n\t" \
  "v_and_b32 v111, 0xffff0000, v35\n\t" \
  "v_lshlrev_b32 v112, 16, v38\n\t" \
  "v_and_b32 v113, 0xffff0000, v38\n\t" \
  "v_lshlrev_b32 v114, 16, v39\n\t" \
  "v_and_b32 v115, 0xffff0000, v39\n\t" \
  "v_add_f32 v108, v108, v112\n\t" \
  "v_add_f32 v109, v109, v113\n\t" \
  "v_add_f32 v110, v110, v114\n\t" \
  "v_add_f32 v111, v111, v115\n\t" \
  "v_lshlrev_b32 v112, 16, v42\n\t" \
  "v_and_b32 v113, 0xffff0000, v42\n\t" \
  "v_lshlrev_b32 v114, 16, v43\n\t" \
  "v_and_b32 v115, 0xffff0000, v43\n\t" \
  "v_add_f32 v108, v108, v112\n\t" \
  "v_add_f32 v109, v109, v113\n\t" \
  "v_add_f32 v110, v110, v114\n\t" \
  "v_add_f32 v111, v111, v115\n\t" \
  "v_lshlrev_b32 v112, 16, v46\n\t" \
  "v_and_b32 v113, 0xffff0000, v46\n\t" \
  "v_lshlrev_b32 v114, 16, v47\n\t" \
  "v_and_b32 v115, 0xffff0000, v47\n\t" \
  "v_add_f32 v108, v108, v112\n\t" \
  "v_add_f32 v109, v109, v113\n\t" \
  "v_add_f32 v110, v110, v114\n\t" \
  "v_add_f32 v111, v111, v115\n\t" \
  "v_cvt_pk_bf16_f32 v122, v108, v109\n\t" \
  "v_cvt_pk_bf16_f32 v123, v110, v111\n\t" \
  "buffer_store_dwordx2 v[122:123], v247, s[48:51], s77 offen\n\t" \
  "s_waitcnt vmcnt(9)\n\t" \
  "v_lshlrev_b32 v100, 16, v48\n\t" \
  "v_and_b32 v101, 0xffff0000, v48\n\t" \
  "v_lshlrev_b32 v102, 16, v49\n\t" \
  "v_and_b32 v103, 0xffff0000, v49\n\t" \
  "s_waitcnt vmcnt(8)\n\t" \
  "v_lshlrev_b32 v104, 16, v52\n\t" \
  "v_and_b32 v105, 0xffff0000, v52\n\t" \
  "v_lshlrev_b32 v106, 16, v53\n\t" \
  "v_and_b32 v107, 0xffff0000, v53\n\t" \
  "v_add_f32 v100, v100, v104\n\t" \
  "v_add_f32 v101, v101, v105\n\t" \
  "v_add_f32 v102, v102, v106\n\t" \
  "v_add_f32 v103, v103, v107\n\t" \
  "s_waitcnt vmcnt(7)\n\t" \
  "v_lshlrev_b32 v104, 16, v56\n\t" \
  "v_and_b32 v105, 0xffff0000, v56\n\t" \
  "v_lshlrev_b32 v106, 16, v57\n\t" \
  "v_and_b32 v107, 0xffff0000, v57\n\t" \
  "v_add_f32 v100, v100, v104\n\t" \
  "v_add_f32 v101, v101, v105\n\t" \
  "v_add_f32 v102, v102, v106\n\t" \
  "v_add_f32 v103, v103, v107\n\t" \
  "s_waitcnt vmcnt(6)\n\t" \
  "v_lshlrev_b32 v104, 16, v60\n\t" \
  "v_and_b32 v105, 0xffff0000, v60\n\t" \
  "v_lshlrev_b32 v106, 16, v61\n\t" \
  "v_and_b32 v107, 0xffff0000, v61\n\t" \
  "v_add_f32 v100, v100, v104\n\t" \
  "v_add_f32 v101, v101, v105\n\t" \
  "v_add_f32 v102, v102, v106\n\t" \
  "v_add_f32 v103, v103, v107\n\t" \
  "v_cvt_pk_bf16_f32 v124, v100, v101\n\t" \
  "v_cvt_pk_bf16_f32 v125, v102, v103\n\t" \
  "buffer_store_dwordx2 v[124:125], v247, s[48:51], s78 offen\n\t" \
  "v_lshlrev_b32 v108, 16, v50\n\t" \
  "v_and_b32 v109, 0xffff0000, v50\n\t" \
  "v_lshlrev_b32 v110, 16, v51\n\t" \
  "v_and_b32 v111, 0xffff0000, v51\n\t" \
  "v_lshlrev_b32 v112, 16, v54\n\t" \
  "v_and_b32 v113, 0xffff0000, v54\n\t" \
  "v_lshlrev_b32 v114, 16, v55\n\t" \
  "v_and_b32 v115, 0xffff0000, v55\n\t" \
  "v_add_f32 v108, v108, v112\n\t" \
  "v_add_f32 v109, v109, v113\n\t" \
  "v_add_f32 v110, v110, v114\n\t" \
  "v_add_f32 v111, v111, v115\n\t" \
  "v_lshlrev_b32 v112, 16, v58\n\t" \
  "v_and_b32 v113, 0xffff0000, v58\n\t" \
  "v_lshlrev_b32 v114, 16, v59\n\t" \
  "v_and_b32 v115, 0xffff0000, v59\n\t" \
  "v_add_f32 v108, v108, v112\n\t" \
  "v_add_f32 v109, v109, v113\n\t" \
  "v_add_f32 v110, v110, v114\n\t" \
  "v_add_f32 v111, v111, v115\n\t" \
  "v_lshlrev_b32 v112, 16, v62\n\t" \
  "v_and_b32 v113, 0xffff0000, v62\n\t" \
  "v_lshlrev_b32 v114, 16, v63\n\t" \
  "v_and_b32 v115, 0xffff0000, v63\n\t" \
  "v_add_f32 v108, v108, v112\n\t" \
  "v_add_f32 v109, v109, v113\n\t" \
  "v_add_f32 v110, v110, v114\n\t" \
  "v_add_f32 v111, v111, v115\n\t" \
  "v_cvt_pk_bf16_f32 v126, v108, v109\n\t" \
  "v_cvt_pk_bf16_f32 v127, v110, v111\n\t" \
  "buffer_store_dwordx2 v[126:127], v247, s[48:51], s80 offen\n\t" \
  "s_waitcnt vmcnt(7)\n\t" \
  "v_lshlrev_b32 v100, 16, v64\n\t" \
  "v_and_b32 v101, 0xffff0000, v64\n\t" \
  "v_lshlrev_b32 v102, 16, v65\n\t" \
  "v_and_b32 v103, 0xffff0000, v65\n\t" \
  "s_waitcnt vmcnt(6)\n\t" \
  "v_lshlrev_b32 v104, 16, v68\n\t" \
  "v_and_b32 v105, 0xffff0000, v68\n\t" \
  "v_lshlrev_b32 v106, 16, v69\n\t" \
  "v_and_b32 v107, 0xffff0000, v69\n\t" \
  "v_add_f32 v100, v100, v104\n\t" \
  "v_add_f32 v101, v101, v105\n\t" \
  "v_add_f32 v102, v102, v106\n\t" \
  "v_add_f32 v103, v103, v107\n\t" \
  "s_waitcnt vmcnt(5)\n\t" \
  "v_lshlrev_b32 v104, 16, v72\n\t" \
  "v_and_b32 v105, 0xffff0000, v72\n\t" \
  "v_lshlrev_b32 v106, 16, v73\n\t" \
  "v_and_b32 v107, 0xffff0000, v73\n\t" \
  "v_add_f32 v100, v100, v104\n\t" \
  "v_add_f32 v101, v101, v105\n\t" \
  "v_add_f32 v102, v102, v106\n\t" \
  "v_add_f32 v103, v103, v107\n\t" \
  "s_waitcnt vmcnt(4)\n\t" \
  "v_lshlrev_b32 v104, 16, v76\n\t" \
  "v_and_b32 v105, 0xffff0000, v76\n\t" \
  "v_lshlrev_b32 v106, 16, v77\n\t" \
  "v_and_b32 v107, 0xffff0000, v77\n\t" \
  "v_add_f32 v100, v100, v104\n\t" \
  "v_add_f32 v101, v101, v105\n\t" \
  "v_add_f32 v102, v102, v106\n\t" \
  "v_add_f32 v103, v103, v107\n\t" \
  "v_cvt_pk_bf16_f32 v128, v100, v101\n\t" \
  "v_cvt_pk_bf16_f32 v129, v102, v103\n\t" \
  "buffer_store_dwordx2 v[128:129], v247, s[48:51], s81 offen\n\t" \
  "v_lshlrev_b32 v108, 16, v66\n\t" \
  "v_and_b32 v109, 0xffff0000, v66\n\t" \
  "v_lshlrev_b32 v110, 16, v67\n\t" \
  "v_and_b32 v111, 0xffff0000, v67\n\t" \
  "v_lshlrev_b32 v112, 16, v70\n\t" \
  "v_and_b32 v113, 0xffff0000, v70\n\t" \
  "v_lshlrev_b32 v114, 16, v71\n\t" \
  "v_and_b32 v115, 0xffff0000, v71\n\t" \
  "v_add_f32 v108, v108, v112\n\t" \
  "v_add_f32 v109, v109, v113\n\t" \
  "v_add_f32 v110, v110, v114\n\t" \
  "v_add_f32 v111, v111, v115\n\t" \
  "v_lshlrev_b32 v112, 16, v74\n\t" \
  "v_and_b32 v113, 0xffff0000, v74\n\t" \
  "v_lshlrev_b32 v114, 16, v75\n\t" \
  "v_and_b32 v115, 0xffff0000, v75\n\t" \
  "v_add_f32 v108, v108, v112\n\t" \
  "v_add_f32 v109, v109, v113\n\t" \
  "v_add_f32 v110, v110, v114\n\t" \
  "v_add_f32 v111, v111, v115\n\t" \
  "v_lshlrev_b32 v112, 16, v78\n\t" \
  "v_and_b32 v113, 0xffff0000, v78\n\t" \
  "v_lshlrev_b32 v114, 16, v79\n\t" \
  "v_and_b32 v115, 0xffff0000, v79\n\t" \
  "v_add_f32 v108, v108, v112\n\t" \
  "v_add_f32 v109, v109, v113\n\t" \
  "v_add_f32 v110, v110, v114\n\t" \
  "v_add_f32 v111, v111, v115\n\t" \
  "v_cvt_pk_bf16_f32 v130, v108, v109\n\t" \
  "v_cvt_pk_bf16_f32 v131, v110, v111\n\t" \
  "buffer_store_dwordx2 v[130:131], v247, s[48:51], s82 offen\n\t" \
  "s_mov_b32 %0, 0\n\t" \
  "s_branch END_%=\n\t" \
  "FALLBACK_%=:\n\t" \
  "s_mov_b32 %0, 1\n\t" \
  "END_%=:\n\t"
#define KZ_FB_ASM \
  "v_mov_b64 v[244:245], %5\n\t" \
  "s_mov_b64 s[56:57], %15\n\t" \
  "s_mov_b32 s58, 9437184\n\t" \
  "s_mov_b32 s59, 0x20000\n\t" \
  "s_mov_b64 s[82:83], %18\n\t" \
  "s_mov_b64 s[84:85], %19\n\t" \
  "s_mov_b64 s[90:91], %17\n\t" \
  "s_mov_b64 s[96:97], %24\n\t" \
  "s_mov_b32 s98, 16384\n\t" \
  "s_mov_b32 s99, 0x20000\n\t" \
  "v_mov_b32 v247, 0\n\t" \
  "s_nop 4\n\t" \
  "buffer_load_dwordx2 v[214:215], v247, s[96:99], 0 offen sc1\n\t" \
  "s_mov_b64 s[40:41], %21\n\t" \
  "s_mov_b64 s[64:65], %25\n\t" \
  "s_mul_i32 s61, s65, 288\n\t" \
  "s_sub_u32 s42, 4718592, s61\n\t" \
  "s_mov_b32 s43, 0x20000\n\t" \
  "s_lshr_b32 s73, s90, 10\n\t" \
  "s_lshr_b32 s66, s73, 1\n\t" \
  "s_lshl_b32 s61, s66, 2\n\t" \
  "s_lshl_b32 s62, s85, 1\n\t" \
  "s_add_u32 s61, s61, s62\n\t" \
  "s_mul_i32 s67, s61, 9216\n\t" \
  "s_mov_b32 s72, s65\n\t" \
  "s_lshl_b32 s61, s85, 11\n\t" \
  "s_sub_u32 s75, s84, s61\n\t" \
  "s_mov_b32 s74, 0\n\t" \
  "s_add_u32 s72, s72, 0\n\t" \
  "s_and_b32 s72, s72, 3\n\t" \
  "s_lshl_b32 s61, s64, 2\n\t" \
  "s_add_u32 s61, s61, s72\n\t" \
  "s_lshl_b32 s61, s61, 1\n\t" \
  "s_add_u32 s61, s61, s66\n\t" \
  "s_mul_i32 s68, s61, 147456\n\t" \
  "s_mul_i32 s61, s85, 73728\n\t" \
  "s_add_u32 s68, s68, s61\n\t" \
  "s_sub_i32 s69, s72, s65\n\t" \
  "s_lshl_b32 s69, s69, 13\n\t" \
  "v_lshrrev_b32 v232, 4, v244\n\t" \
  "v_add_u32 v232, 0, v232\n\t" \
  "v_mul_u32_u24 v248, 0xe39, v232\n\t" \
  "v_lshrrev_b32 v248, 16, v248\n\t" \
  "v_mul_u32_u24 v248, 0x360, v248\n\t" \
  "v_lshl_add_u32 v232, v232, 4, v248\n\t" \
  "v_lshrrev_b32 v233, 4, v244\n\t" \
  "v_add_u32 v233, 64, v233\n\t" \
  "v_mul_u32_u24 v248, 0xe39, v233\n\t" \
  "v_lshrrev_b32 v248, 16, v248\n\t" \
  "v_mul_u32_u24 v248, 0x360, v248\n\t" \
  "v_lshl_add_u32 v233, v233, 4, v248\n\t" \
  "v_lshrrev_b32 v234, 4, v244\n\t" \
  "v_add_u32 v234, 128, v234\n\t" \
  "v_mul_u32_u24 v248, 0xe39, v234\n\t" \
  "v_lshrrev_b32 v248, 16, v248\n\t" \
  "v_mul_u32_u24 v248, 0x360, v248\n\t" \
  "v_lshl_add_u32 v234, v234, 4, v248\n\t" \
  "v_lshrrev_b32 v235, 4, v244\n\t" \
  "v_add_u32 v235, 192, v235\n\t" \
  "v_mul_u32_u24 v248, 0xe39, v235\n\t" \
  "v_lshrrev_b32 v248, 16, v248\n\t" \
  "v_mul_u32_u24 v248, 0x360, v248\n\t" \
  "v_lshl_add_u32 v235, v235, 4, v248\n\t" \
  "v_lshrrev_b32 v236, 4, v244\n\t" \
  "v_add_u32 v236, 256, v236\n\t" \
  "v_mul_u32_u24 v248, 0xe39, v236\n\t" \
  "v_lshrrev_b32 v248, 16, v248\n\t" \
  "v_mul_u32_u24 v248, 0x360, v248\n\t" \
  "v_lshl_add_u32 v236, v236, 4, v248\n\t" \
  "v_lshrrev_b32 v237, 4, v244\n\t" \
  "v_add_u32 v237, 320, v237\n\t" \
  "v_mul_u32_u24 v248, 0xe39, v237\n\t" \
  "v_lshrrev_b32 v248, 16, v248\n\t" \
  "v_mul_u32_u24 v248, 0x360, v248\n\t" \
  "v_lshl_add_u32 v237, v237, 4, v248\n\t" \
  "v_lshrrev_b32 v238, 4, v244\n\t" \
  "v_add_u32 v238, 384, v238\n\t" \
  "v_mul_u32_u24 v248, 0xe39, v238\n\t" \
  "v_lshrrev_b32 v248, 16, v248\n\t" \
  "v_mul_u32_u24 v248, 0x360, v248\n\t" \
  "v_lshl_add_u32 v238, v238, 4, v248\n\t" \
  "v_lshrrev_b32 v239, 4, v244\n\t" \
  "v_add_u32 v239, 448, v239\n\t" \
  "v_mul_u32_u24 v248, 0xe39, v239\n\t" \
  "v_lshrrev_b32 v248, 16, v248\n\t" \
  "v_mul_u32_u24 v248, 0x360, v248\n\t" \
  "v_lshl_add_u32 v239, v239, 4, v248\n\t" \
  "v_lshrrev_b32 v240, 4, v244\n\t" \
  "v_add_u32 v240, 512, v240\n\t" \
  "v_mul_u32_u24 v248, 0xe39, v240\n\t" \
  "v_lshrrev_b32 v248, 16, v248\n\t" \
  "v_mul_u32_u24 v248, 0x360, v248\n\t" \
  "v_lshl_add_u32 v240, v240, 4, v248\n\t" \
  "s_add_u32 s62, s67, 0\n\t" \
  "s_add_u32 m0, s62, 0\n\t" \
  "s_nop 0\n\t" \
  "buffer_load_dwordx4 v232, s[40:43], s68 offen lds\n\t" \
  "s_add_u32 m0, s62, 1024\n\t" \
  "s_nop 0\n\t" \
  "buffer_load_dwordx4 v233, s[40:43], s68 offen lds\n\t" \
  "s_add_u32 m0, s62, 2048\n\t" \
  "s_nop 0\n\t" \
  "buffer_load_dwordx4 v234, s[40:43], s68 offen lds\n\t" \
  "s_add_u32 m0, s62, 3072\n\t" \
  "s_nop 0\n\t" \
  "buffer_load_dwordx4 v235, s[40:43], s68 offen lds\n\t" \
  "s_add_u32 m0, s62, 4096\n\t" \
  "s_nop 0\n\t" \
  "buffer_load_dwordx4 v236, s[40:43], s68 offen lds\n\t" \
  "s_add_u32 m0, s62, 5120\n\t" \
  "s_nop 0\n\t" \
  "buffer_load_dwordx4 v237, s[40:43], s68 offen lds\n\t" \
  "s_add_u32 m0, s62, 6144\n\t" \
  "s_nop 0\n\t" \
  "buffer_load_dwordx4 v238, s[40:43], s68 offen lds\n\t" \
  "s_add_u32 m0, s62, 7168\n\t" \
  "s_nop 0\n\t" \
  "buffer_load_dwordx4 v239, s[40:43], s68 offen lds\n\t" \
  "s_add_u32 m0, s62, 8192\n\t" \
  "s_nop 0\n\t" \
  "buffer_load_dwordx4 v240, s[40:43], s68 offen lds\n\t" \
  "s_add_u32 s61, s68, 36864\n\t" \
  "s_add_u32 m0, s62, 9216\n\t" \
  "s_nop 0\n\t" \
  "buffer_load_dwordx4 v232, s[40:43], s61 offen lds\n\t" \
  "s_add_u32 m0, s62, 10240\n\t" \
  "s_nop 0\n\t" \
  "buffer_load_dwordx4 v233, s[40:43], s61 offen lds\n\t" \
  "s_add_u32 m0, s62, 11264\n\t" \
  "s_nop 0\n\t" \
  "buffer_load_dwordx4 v234, s[40:43], s61 offen lds\n\t" \
  "s_add_u32 m0, s62, 12288\n\t" \
  "s_nop 0\n\t" \
  "buffer_load_dwordx4 v235, s[40:43], s61 offen lds\n\t" \
  "s_add_u32 m0, s62, 13312\n\t" \
  "s_nop 0\n\t" \
  "buffer_load_dwordx4 v236, s[40:43], s61 offen lds\n\t" \
  "s_add_u32 m0, s62, 14336\n\t" \
  "s_nop 0\n\t" \
  "buffer_load_dwordx4 v237, s[40:43], s61 offen lds\n\t" \
  "s_add_u32 m0, s62, 15360\n\t" \
  "s_nop 0\n\t" \
  "buffer_load_dwordx4 v238, s[40:43], s61 offen lds\n\t" \
  "s_add_u32 m0, s62, 16384\n\t" \
  "s_nop 0\n\t" \
  "buffer_load_dwordx4 v239, s[40:43], s61 offen lds\n\t" \
  "s_add_u32 m0, s62, 17408\n\t" \
  "s_nop 0\n\t" \
  "buffer_load_dwordx4 v240, s[40:43], s61 offen lds\n\t" \
  "s_add_u32 s80, s83, s69\n\t" \
  "s_add_u32 s81, s83, s69\n\t" \
  "s_add_u32 s81, s81, 131072\n\t" \
  "s_add_u32 s82, s83, s69\n\t" \
  "s_add_u32 s82, s82, 262144\n\t" \
  "s_add_u32 s87, s83, s69\n\t" \
  "s_add_u32 s87, s87, 393216\n\t" \
  "s_add_u32 s95, s75, s69\n\t" \
  "buffer_load_dwordx4 v[32:35], v244, s[56:59], s80 offen sc1\n\t" \
  "buffer_load_dwordx4 v[36:39], v244, s[56:59], s81 offen sc1\n\t" \
  "buffer_load_dwordx4 v[40:43], v244, s[56:59], s82 offen sc1\n\t" \
  "buffer_load_dwordx4 v[44:47], v244, s[56:59], s87 offen sc1\n\t" \
  "buffer_load_dwordx4 v[48:51], v244, s[56:59], s95 offen sc1\n\t" \
  "buffer_load_dwordx4 v[52:55], v244, s[56:59], s80 offen offset:1024 sc1\n\t" \
  "buffer_load_dwordx4 v[56:59], v244, s[56:59], s81 offen offset:1024 sc1\n\t" \
  "buffer_load_dwordx4 v[60:63], v244, s[56:59], s82 offen offset:1024 sc1\n\t" \
  "buffer_load_dwordx4 v[64:67], v244, s[56:59], s87 offen offset:1024 sc1\n\t" \
  "buffer_load_dwordx4 v[68:71], v244, s[56:59], s95 offen offset:1024 sc1\n\t" \
  "buffer_load_dwordx4 v[72:75], v244, s[56:59], s80 offen offset:2048 sc1\n\t" \
  "buffer_load_dwordx4 v[76:79], v244, s[56:59], s81 offen offset:2048 sc1\n\t" \
  "buffer_load_dwordx4 v[80:83], v244, s[56:59], s82 offen offset:2048 sc1\n\t" \
  "buffer_load_dwordx4 v[84:87], v244, s[56:59], s87 offen offset:2048 sc1\n\t" \
  "buffer_load_dwordx4 v[88:91], v244, s[56:59], s95 offen offset:2048 sc1\n\t" \
  "buffer_load_dwordx4 v[92:95], v244, s[56:59], s80 offen offset:3072 sc1\n\t" \
  "buffer_load_dwordx4 v[96:99], v244, s[56:59], s81 offen offset:3072 sc1\n\t" \
  "buffer_load_dwordx4 v[100:103], v244, s[56:59], s82 offen offset:3072 sc1\n\t" \
  "buffer_load_dwordx4 v[104:107], v244, s[56:59], s87 offen offset:3072 sc1\n\t" \
  "buffer_load_dwordx4 v[108:111], v244, s[56:59], s95 offen offset:3072 sc1\n\t" \
  "s_waitcnt vmcnt(0)\n\t" \
  "v_mov_b64 v[210:211], %26\n\t" \
  "v_readfirstlane_b32 s52, v214\n\t" \
  "v_readfirstlane_b32 s53, v215\n\t" \
  "s_nop 3\n\t" \
  "s_add_u32 s52, s52, 1\n\t" \
  "s_addc_u32 s53, s53, 0\n\t" \
  "s_mov_b32 s74, 0\n\t" \
  "s_mov_b64 s[44:45], %22\n\t" \
  "s_mov_b32 s46, 0x2000000\n\t" \
  "s_mov_b32 s47, 0x20000\n\t" \
  "s_mov_b64 s[48:49], %23\n\t" \
  "s_mov_b32 s50, 2654208\n\t" \
  "s_mov_b32 s51, 0x20000\n\t" \
  "v_lshrrev_b32 v252, 4, v244\n\t" \
  "v_and_b32 v252, 3, v252\n\t" \
  "v_lshlrev_b32 v253, 10, v252\n\t" \
  "v_lshlrev_b32 v252, 8, v252\n\t" \
  "s_lshl_b32 s61, s64, 10\n\t" \
  "s_add_u32 s61, s61, 256\n\t" \
  "v_add_u32 v252, s61, v252\n\t" \
  "s_lshl_b32 s61, s65, 8\n\t" \
  "s_add_u32 s61, s61, 4352\n\t" \
  "v_add_u32 v253, s61, v253\n\t" \
  "s_lshl_b32 s79, s64, 2\n\t" \
  "s_add_u32 s79, s79, s65\n\t" \
  "s_lshl_b32 s79, s79, 8\n\t" \
  "s_add_u32 s79, s79, 256\n\t" \
  "s_barrier\n\t" \
  "v_add_u32 v208, 0, v210\n\t" \
  "v_add_u32 v209, 0, v211\n\t" \
  "ds_read_b64_tr_b16 v[192:193], v208 offset:0\n\t" \
  "ds_read_b64_tr_b16 v[194:195], v208 offset:4608\n\t" \
  "ds_read_b64_tr_b16 v[196:197], v208 offset:32\n\t" \
  "ds_read_b64_tr_b16 v[198:199], v208 offset:4640\n\t" \
  "ds_read_b64_tr_b16 v[200:201], v208 offset:64\n\t" \
  "ds_read_b64_tr_b16 v[202:203], v208 offset:4672\n\t" \
  "ds_read_b64_tr_b16 v[204:205], v208 offset:96\n\t" \
  "ds_read_b64_tr_b16 v[206:207], v208 offset:4704\n\t" \
  "s_waitcnt lgkmcnt(6)\n\t" \
  "v_mfma_f32_16x16x32_bf16 a[0:3], v[192:195], v[32:35], 0\n\t" \
  "s_add_u32 s72, s72, 1\n\t" \
  "v_mfma_f32_16x16x32_bf16 a[36:39], v[192:195], v[36:39], 0\n\t" \
  "s_and_b32 s72, s72, 3\n\t" \
  "v_mfma_f32_16x16x32_bf16 a[72:75], v[192:195], v[40:43], 0\n\t" \
  "s_lshl_b32 s61, s64, 2\n\t" \
  "v_mfma_f32_16x16x32_bf16 a[108:111], v[192:195], v[44:47], 0\n\t" \
  "v_mfma_f32_16x16x32_bf16 a[144:147], v[192:195], v[48:51], 0\n\t" \
  "ds_read_b64_tr_b16 v[192:193], v208 offset:128\n\t" \
  "ds_read_b64_tr_b16 v[194:195], v208 offset:4736\n\t" \
  "s_waitcnt lgkmcnt(6)\n\t" \
  "v_mfma_f32_16x16x32_bf16 a[4:7], v[196:199], v[32:35], 0\n\t" \
  "s_add_u32 s61, s61, s72\n\t" \
  "s_lshl_b32 s61, s61, 1\n\t" \
  "v_mfma_f32_16x16x32_bf16 a[40:43], v[196:199], v[36:39], 0\n\t" \
  "v_mfma_f32_16x16x32_bf16 a[76:79], v[196:199], v[40:43], 0\n\t" \
  "s_add_u32 s61, s61, s66\n\t" \
  "v_mfma_f32_16x16x32_bf16 a[112:115], v[196:199], v[44:47], 0\n\t" \
  "s_mul_i32 s68, s61, 147456\n\t" \
  "v_mfma_f32_16x16x32_bf16 a[148:151], v[196:199], v[48:51], 0\n\t" \
  "ds_read_b64_tr_b16 v[196:197], v209 offset:160\n\t" \
  "ds_read_b64_tr_b16 v[198:199], v209 offset:4768\n\t" \
  "s_waitcnt lgkmcnt(6)\n\t" \
  "v_mfma_f32_16x16x32_bf16 a[8:11], v[200:203], v[32:35], 0\n\t" \
  "s_mul_i32 s61, s85, 73728\n\t" \
  "v_mfma_f32_16x16x32_bf16 a[44:47], v[200:203], v[36:39], 0\n\t" \
  "s_add_u32 s68, s68, s61\n\t" \
  "v_mfma_f32_16x16x32_bf16 a[80:83], v[200:203], v[40:43], 0\n\t" \
  "v_mfma_f32_16x16x32_bf16 a[116:119], v[200:203], v[44:47], 0\n\t" \
  "s_sub_i32 s69, s72, s65\n\t" \
  "v_mfma_f32_16x16x32_bf16 a[152:155], v[200:203], v[48:51], 0\n\t" \
  "ds_read_b64_tr_b16 v[200:201], v209 offset:192\n\t" \
  "ds_read_b64_tr_b16 v[202:203], v209 offset:4800\n\t" \
  "s_waitcnt lgkmcnt(6)\n\t" \
  "v_mfma_f32_16x16x32_bf16 a[12:15], v[204:207], v[32:35], 0\n\t" \
  "s_lshl_b32 s69, s69, 13\n\t" \
  "s_add_u32 s62, s67, 73728\n\t" \
  "v_mfma_f32_16x16x32_bf16 a[48:51], v[204:207], v[36:39], 0\n\t" \
  "v_mfma_f32_16x16x32_bf16 a[84:87], v[204:207], v[40:43], 0\n\t" \
  "s_add_u32 m0, s62, 0\n\t" \
  "v_mfma_f32_16x16x32_bf16 a[120:123], v[204:207], v[44:47], 0\n\t" \
  "buffer_load_dwordx4 v232, s[40:43], s68 offen lds\n\t" \
  "v_mfma_f32_16x16x32_bf16 a[156:159], v[204:207], v[48:51], 0\n\t" \
  "ds_read_b64_tr_b16 v[204:205], v209 offset:224\n\t" \
  "ds_read_b64_tr_b16 v[206:207], v209 offset:4832\n\t" \
  "s_waitcnt lgkmcnt(6)\n\t" \
  "v_mfma_f32_16x16x32_bf16 a[16:19], v[192:195], v[32:35], 0\n\t" \
  "s_add_u32 m0, s62, 1024\n\t" \
  "v_mfma_f32_16x16x32_bf16 a[52:55], v[192:195], v[36:39], 0\n\t" \
  "buffer_load_dwordx4 v233, s[40:43], s68 offen lds\n\t" \
  "v_mfma_f32_16x16x32_bf16 a[88:91], v[192:195], v[40:43], 0\n\t" \
  "v_mfma_f32_16x16x32_bf16 a[124:127], v[192:195], v[44:47], 0\n\t" \
  "s_add_u32 m0, s62, 2048\n\t" \
  "v_mfma_f32_16x16x32_bf16 a[160:163], v[192:195], v[48:51], 0\n\t" \
  "ds_read_b64_tr_b16 v[192:193], v209 offset:256\n\t" \
  "ds_read_b64_tr_b16 v[194:195], v209 offset:4864\n\t" \
  "s_waitcnt lgkmcnt(6)\n\t" \
  "v_mfma_f32_16x16x32_bf16 a[20:23], v[196:199], v[32:35], 0\n\t" \
  "buffer_load_dwordx4 v234, s[40:43], s68 offen lds\n\t" \
  "v_mfma_f32_16x16x32_bf16 a[56:59], v[196:199], v[36:39], 0\n\t" \
  "s_add_u32 m0, s62, 3072\n\t" \
  "v_mfma_f32_16x16x32_bf16 a[92:95], v[196:199], v[40:43], 0\n\t" \
  "buffer_load_dwordx4 v235, s[40:43], s68 offen lds\n\t" \
  "v_mfma_f32_16x16x32_bf16 a[128:131], v[196:199], v[44:47], 0\n\t" \
  "ds_read_b64_tr_b16 v[196:197], v208 offset:9216\n\t" \
  "ds_read_b64_tr_b16 v[198:199], v208 offset:13824\n\t" \
  "s_waitcnt lgkmcnt(6)\n\t" \
  "v_mfma_f32_16x16x32_bf16 a[24:27], v[200:203], v[32:35], 0\n\t" \
  "s_add_u32 m0, s62, 4096\n\t" \
  "v_mfma_f32_16x16x32_bf16 a[60:63], v[200:203], v[36:39], 0\n\t" \
  "buffer_load_dwordx4 v236, s[40:43], s68 offen lds\n\t" \
  "v_mfma_f32_16x16x32_bf16 a[96:99], v[200:203], v[40:43], 0\n\t" \
  "s_add_u32 m0, s62, 5120\n\t" \
  "v_mfma_f32_16x16x32_bf16 a[132:135], v[200:203], v[44:47], 0\n\t" \
  "ds_read_b64_tr_b16 v[200:201], v208 offset:9248\n\t" \
  "ds_read_b64_tr_b16 v[202:203], v208 offset:13856\n\t" \
  "s_waitcnt lgkmcnt(4)\n\t" \
  "v_mfma_f32_16x16x32_bf16 a[28:31], v[204:207], v[32:35], 0\n\t" \
  "buffer_load_dwordx4 v237, s[40:43], s68 offen lds\n\t" \
  "v_mfma_f32_16x16x32_bf16 a[64:67], v[204:207], v[36:39], 0\n\t" \
  "s_add_u32 m0, s62, 6144\n\t" \
  "v_mfma_f32_16x16x32_bf16 a[100:103], v[204:207], v[40:43], 0\n\t" \
  "v_mfma_f32_16x16x32_bf16 a[136:139], v[204:207], v[44:47], 0\n\t" \
  "ds_read_b64_tr_b16 v[204:205], v208 offset:9280\n\t" \
  "ds_read_b64_tr_b16 v[206:207], v208 offset:13888\n\t" \
  "v_mfma_f32_16x16x32_bf16 a[32:35], v[192:195], v[32:35], 0\n\t" \
  "buffer_load_dwordx4 v238, s[40:43], s68 offen lds\n\t" \
  "s_add_u32 m0, s62, 7168\n\t" \
  "v_mfma_f32_16x16x32_bf16 a[68:71], v[192:195], v[36:39], 0\n\t" \
  "v_mfma_f32_16x16x32_bf16 a[104:107], v[192:195], v[40:43], 0\n\t" \
  "buffer_load_dwordx4 v239, s[40:43], s68 offen lds\n\t" \
  "v_mfma_f32_16x16x32_bf16 a[140:143], v[192:195], v[44:47], 0\n\t" \
  "ds_read_b64_tr_b16 v[192:193], v208 offset:9312\n\t" \
  "ds_read_b64_tr_b16 v[194:195], v208 offset:13920\n\t" \
  "s_waitcnt lgkmcnt(6)\n\t" \
  "v_mfma_f32_16x16x32_bf16 a[0:3], v[196:199], v[52:55], a[0:3]\n\t" \
  "s_add_u32 m0, s62, 8192\n\t" \
  "v_mfma_f32_16x16x32_bf16 a[36:39], v[196:199], v[56:59], a[36:39]\n\t" \
  "buffer_load_dwordx4 v240, s[40:43], s68 offen lds\n\t" \
  "v_mfma_f32_16x16x32_bf16 a[72:75], v[196:199], v[60:63], a[72:75]\n\t" \
  "s_add_u32 s61, s68, 36864\n\t" \
  "v_mfma_f32_16x16x32_bf16 a[108:111], v[196:199], v[64:67], a[108:111]\n\t" \
  "s_add_u32 m0, s62, 9216\n\t" \
  "v_mfma_f32_16x16x32_bf16 a[144:147], v[196:199], v[68:71], a[144:147]\n\t" \
  "ds_read_b64_tr_b16 v[196:197], v208 offset:9344\n\t" \
  "ds_read_b64_tr_b16 v[198:199], v208 offset:13952\n\t" \
  "s_waitcnt lgkmcnt(6)\n\t" \
  "v_mfma_f32_16x16x32_bf16 a[4:7], v[200:203], v[52:55], a[4:7]\n\t" \
  "buffer_load_dwordx4 v232, s[40:43], s61 offen lds\n\t" \
  "v_mfma_f32_16x16x32_bf16 a[40:43], v[200:203], v[56:59], a[40:43]\n\t" \
  "s_add_u32 m0, s62, 10240\n\t" \
  "v_mfma_f32_16x16x32_bf16 a[76:79], v[200:203], v[60:63], a[76:79]\n\t" \
  "v_mfma_f32_16x16x32_bf16 a[112:115], v[200:203], v[64:67], a[112:115]\n\t" \
  "buffer_load_dwordx4 v233, s[40:43], s61 offen lds\n\t" \
  "v_mfma_f32_16x16x32_bf16 a[148:151], v[200:203], v[68:71], a[148:151]\n\t" \
  "ds_read_b64_tr_b16 v[200:201], v209 offset:9376\n\t" \
  "ds_read_b64_tr_b16 v[202:203], v209 offset:13984\n\t" \
  "s_waitcnt lgkmcnt(6)\n\t" \
  "v_mfma_f32_16x16x32_bf16 a[8:11], v[204:207], v[52:55], a[8:11]\n\t" \
  "s_add_u32 m0, s62, 11264\n\t" \
  "v_mfma_f32_16x16x32_bf16 a[44:47], v[204:207], v[56:59], a[44:47]\n\t" \
  "buffer_load_dwordx4 v234, s[40:43], s61 offen lds\n\t" \
  "v_mfma_f32_16x16x32_bf16 a[80:83], v[204:207], v[60:63], a[80:83]\n\t" \
  "s_add_u32 m0, s62, 12288\n\t" \
  "v_mfma_f32_16x16x32_bf16 a[116:119], v[204:207], v[64:67], a[116:119]\n\t" \
  "v_mfma_f32_16x16x32_bf16 a[152:155], v[204:207], v[68:71], a[152:155]\n\t" \
  "ds_read_b64_tr_b16 v[204:205], v209 offset:9408\n\t" \
  "ds_read_b64_tr_b16 v[206:207], v209 offset:14016\n\t" \
  "s_waitcnt lgkmcnt(6)\n\t" \
  "v_mfma_f32_16x16x32_bf16 a[12:15], v[192:195], v[52:55], a[12:15]\n\t" \
  "buffer_load_dwordx4 v235, s[40:43], s61 offen lds\n\t" \
  "s_add_u32 m0, s62, 13312\n\t" \
  "v_mfma_f32_16x16x32_bf16 a[48:51], v[192:195], v[56:59], a[48:51]\n\t" \
  "v_mfma_f32_16x16x32_bf16 a[84:87], v[192:195], v[60:63], a[84:87]\n\t" \
  "buffer_load_dwordx4 v236, s[40:43], s61 offen lds\n\t" \
  "v_mfma_f32_16x16x32_bf16 a[120:123], v[192:195], v[64:67], a[120:123]\n\t" \
  "s_add_u32 m0, s62, 14336\n\t" \
  "v_mfma_f32_16x16x32_bf16 a[156:159], v[192:195], v[68:71], a[156:159]\n\t" \
  "ds_read_b64_tr_b16 v[192:193], v209 offset:9440\n\t" \
  "ds_read_b64_tr_b16 v[194:195], v209 offset:14048\n\t" \
  "s_waitcnt lgkmcnt(6)\n\t" \
  "v_mfma_f32_16x16x32_bf16 a[16:19], v[196:199], v[52:55], a[16:19]\n\t" \
  "buffer_load_dwordx4 v237, s[40:43], s61 offen lds\n\t" \
  "v_mfma_f32_16x16x32_bf16 a[52:55], v[196:199], v[56:59], a[52:55]\n\t" \
  "s_add_u32 m0, s62, 15360\n\t" \
  "v_mfma_f32_16x16x32_bf16 a[88:91], v[196:199], v[60:63], a[88:91]\n\t" \
  "buffer_load_dwordx4 v238, s[40:43], s61 offen lds\n\t" \
  "v_mfma_f32_16x16x32_bf16 a[124:127], v[196:199], v[64:67], a[124:127]\n\t" \
  "v_mfma_f32_16x16x32_bf16 a[160:163], v[196:199], v[68:71], a[160:163]\n\t" \
  "ds_read_b64_tr_b16 v[196:197], v209 offset:9472\n\t" \
  "ds_read_b64_tr_b16 v[198:199], v209 offset:14080\n\t" \
  "s_waitcnt lgkmcnt(6)\n\t" \
  "v_mfma_f32_16x16x32_bf16 a[20:23], v[200:203], v[52:55], a[20:23]\n\t" \
  "s_add_u32 m0, s62, 16384\n\t" \
  "s_nop 0\n\t" \
  "buffer_load_dwordx4 v239, s[40:43], s61 offen lds\n\t" \
  "v_mfma_f32_16x16x32_bf16 a[56:59], v[200:203], v[56:59], a[56:59]\n\t" \
  "v_mfma_f32_16x16x32_bf16 a[92:95], v[200:203], v[60:63], a[92:95]\n\t" \
  "s_add_u32 m0, s62, 17408\n\t" \
  "v_mfma_f32_16x16x32_bf16 a[128:131], v[200:203], v[64:67], a[128:131]\n\t" \
  "ds_read_b64_tr_b16 v[200:201], v208 offset:18432\n\t" \
  "ds_read_b64_tr_b16 v[202:203], v208 offset:23040\n\t" \
  "s_waitcnt lgkmcnt(4)\n\t" \
  "v_mfma_f32_16x16x32_bf16 a[24:27], v[204:207], v[52:55], a[24:27]\n\t" \
  "buffer_load_dwordx4 v240, s[40:43], s61 offen lds\n\t" \
  "v_mfma_f32_16x16x32_bf16 a[60:63], v[204:207], v[56:59], a[60:63]\n\t" \
  "v_mfma_f32_16x16x32_bf16 a[96:99], v[204:207], v[60:63], a[96:99]\n\t" \
  "v_mfma_f32_16x16x32_bf16 a[132:135], v[204:207], v[64:67], a[132:135]\n\t" \
  "ds_read_b64_tr_b16 v[204:205], v208 offset:18464\n\t" \
  "ds_read_b64_tr_b16 v[206:207], v208 offset:23072\n\t" \
  "v_mfma_f32_16x16x32_bf16 a[28:31], v[192:195], v[52:55], a[28:31]\n\t" \
  "v_mfma_f32_16x16x32_bf16 a[64:67], v[192:195], v[56:59], a[64:67]\n\t" \
  "v_mfma_f32_16x16x32_bf16 a[100:103], v[192:195], v[60:63], a[100:103]\n\t" \
  "v_mfma_f32_16x16x32_bf16 a[136:139], v[192:195], v[64:67], a[136:139]\n\t" \
  "ds_read_b64_tr_b16 v[192:193], v208 offset:18496\n\t" \
  "ds_read_b64_tr_b16 v[194:195], v208 offset:23104\n\t" \
  "s_waitcnt lgkmcnt(6)\n\t" \
  "v_mfma_f32_16x16x32_bf16 a[32:35], v[196:199], v[52:55], a[32:35]\n\t" \
  "v_mfma_f32_16x16x32_bf16 a[68:71], v[196:199], v[56:59], a[68:71]\n\t" \
  "v_mfma_f32_16x16x32_bf16 a[104:107], v[196:199], v[60:63], a[104:107]\n\t" \
  "v_mfma_f32_16x16x32_bf16 a[140:143], v[196:199], v[64:67], a[140:143]\n\t" \
  "ds_read_b64_tr_b16 v[196:197], v208 offset:18528\n\t" \
  "ds_read_b64_tr_b16 v[198:199], v208 offset:23136\n\t" \
  "s_waitcnt vmcnt(0)\n\t" \
  "s_barrier\n\t" \
  "s_cmp_lg_u32 s73, 0\n\t" \
  "s_cbranch_scc1 FPB0_%=\n\t" \
  "v_mov_b32 v250, s52\n\t" \
  "v_mov_b32 v251, s53\n\t" \
  "v_mov_b32 v254, 0\n\t" \
  "s_mov_b64 exec, 1\n\t" \
  "s_nop 1\n\t" \
  "buffer_store_dwordx2 v[250:251], v254, s[96:99], s79 offen sc1\n\t" \
  "s_mov_b64 exec, -1\n\t" \
  "s_nop 4\n\t" \
  "FPB0_%=:\n\t" \
  "s_waitcnt lgkmcnt(4)\n\t" \
  "v_mfma_f32_16x16x32_bf16 a[0:3], v[200:203], v[72:75], a[0:3]\n\t" \
  "v_mfma_f32_16x16x32_bf16 a[36:39], v[200:203], v[76:79], a[36:39]\n\t" \
  "v_mfma_f32_16x16x32_bf16 a[72:75], v[200:203], v[80:83], a[72:75]\n\t" \
  "v_mfma_f32_16x16x32_bf16 a[108:111], v[200:203], v[84:87], a[108:111]\n\t" \
  "v_mfma_f32_16x16x32_bf16 a[144:147], v[200:203], v[88:91], a[144:147]\n\t" \
  "ds_read_b64_tr_b16 v[200:201], v208 offset:18560\n\t" \
  "ds_read_b64_tr_b16 v[202:203], v208 offset:23168\n\t" \
  "v_mfma_f32_16x16x32_bf16 a[4:7], v[204:207], v[72:75], a[4:7]\n\t" \
  "v_mfma_f32_16x16x32_bf16 a[40:43], v[204:207], v[76:79], a[40:43]\n\t" \
  "v_mfma_f32_16x16x32_bf16 a[76:79], v[204:207], v[80:83], a[76:79]\n\t" \
  "v_mfma_f32_16x16x32_bf16 a[112:115], v[204:207], v[84:87], a[112:115]\n\t" \
  "v_mfma_f32_16x16x32_bf16 a[148:151], v[204:207], v[88:91], a[148:151]\n\t" \
  "ds_read_b64_tr_b16 v[204:205], v209 offset:18592\n\t" \
  "ds_read_b64_tr_b16 v[206:207], v209 offset:23200\n\t" \
  "s_waitcnt lgkmcnt(4)\n\t" \
  "v_mfma_f32_16x16x32_bf16 a[8:11], v[192:195], v[72:75], a[8:11]\n\t" \
  "v_mfma_f32_16x16x32_bf16 a[44:47], v[192:195], v[76:79], a[44:47]\n\t" \
  "v_mfma_f32_16x16x32_bf16 a[80:83], v[192:195], v[80:83], a[80:83]\n\t" \
  "v_mfma_f32_16x16x32_bf16 a[116:119], v[192:195], v[84:87], a[116:119]\n\t" \
  "v_mfma_f32_16x16x32_bf16 a[152:155], v[192:195], v[88:91], a[152:155]\n\t" \
  "ds_read_b64_tr_b16 v[192:193], v209 offset:18624\n\t" \
  "ds_read_b64_tr_b16 v[194:195], v209 offset:23232\n\t" \
  "v_mfma_f32_16x16x32_bf16 a[12:15], v[196:199], v[72:75], a[12:15]\n\t" \
  "v_mfma_f32_16x16x32_bf16 a[48:51], v[196:199], v[76:79], a[48:51]\n\t" \
  "v_mfma_f32_16x16x32_bf16 a[84:87], v[196:199], v[80:83], a[84:87]\n\t" \
  "v_mfma_f32_16x16x32_bf16 a[120:123], v[196:199], v[84:87], a[120:123]\n\t" \
  "v_mfma_f32_16x16x32_bf16 a[156:159], v[196:199], v[88:91], a[156:159]\n\t" \
  "ds_read_b64_tr_b16 v[196:197], v209 offset:18656\n\t" \
  "ds_read_b64_tr_b16 v[198:199], v209 offset:23264\n\t" \
  "s_waitcnt lgkmcnt(6)\n\t" \
  "v_mfma_f32_16x16x32_bf16 a[16:19], v[200:203], v[72:75], a[16:19]\n\t" \
  "v_mfma_f32_16x16x32_bf16 a[52:55], v[200:203], v[76:79], a[52:55]\n\t" \
  "v_mfma_f32_16x16x32_bf16 a[88:91], v[200:203], v[80:83], a[88:91]\n\t" \
  "v_mfma_f32_16x16x32_bf16 a[124:127], v[200:203], v[84:87], a[124:127]\n\t" \
  "v_mfma_f32_16x16x32_bf16 a[160:163], v[200:203], v[88:91], a[160:163]\n\t" \
  "ds_read_b64_tr_b16 v[200:201], v209 offset:18688\n\t" \
  "ds_read_b64_tr_b16 v[202:203], v209 offset:23296\n\t" \
  "s_waitcnt lgkmcnt(6)\n\t" \
  "v_mfma_f32_16x16x32_bf16 a[20:23], v[204:207], v[72:75], a[20:23]\n\t" \
  "v_mfma_f32_16x16x32_bf16 a[56:59], v[204:207], v[76:79], a[56:59]\n\t" \
  "v_mfma_f32_16x16x32_bf16 a[92:95], v[204:207], v[80:83], a[92:95]\n\t" \
  "v_mfma_f32_16x16x32_bf16 a[128:131], v[204:207], v[84:87], a[128:131]\n\t" \
  "ds_read_b64_tr_b16 v[204:205], v208 offset:27648\n\t" \
  "ds_read_b64_tr_b16 v[206:207], v208 offset:32256\n\t" \
  "s_waitcnt lgkmcnt(6)\n\t" \
  "v_mfma_f32_16x16x32_bf16 a[24:27], v[192:195], v[72:75], a[24:27]\n\t" \
  "v_mfma_f32_16x16x32_bf16 a[60:63], v[192:195], v[76:79], a[60:63]\n\t" \
  "v_mfma_f32_16x16x32_bf16 a[96:99], v[192:195], v[80:83], a[96:99]\n\t" \
  "v_mfma_f32_16x16x32_bf16 a[132:135], v[192:195], v[84:87], a[132:135]\n\t" \
  "ds_read_b64_tr_b16 v[192:193], v208 offset:27680\n\t" \
  "ds_read_b64_tr_b16 v[194:195], v208 offset:32288\n\t" \
  "s_waitcnt lgkmcnt(6)\n\t" \
  "v_mfma_f32_16x16x32_bf16 a[28:31], v[196:199], v[72:75], a[28:31]\n\t" \
  "v_mfma_f32_16x16x32_bf16 a[64:67], v[196:199], v[76:79], a[64:67]\n\t" \
  "v_mfma_f32_16x16x32_bf16 a[100:103], v[196:199], v[80:83], a[100:103]\n\t" \
  "v_mfma_f32_16x16x32_bf16 a[136:139], v[196:199], v[84:87], a[136:139]\n\t" \
  "ds_read_b64_tr_b16 v[196:197], v208 offset:27712\n\t" \
  "ds_read_b64_tr_b16 v[198:199], v208 offset:32320\n\t" \
  "s_waitcnt lgkmcnt(6)\n\t" \
  "v_mfma_f32_16x16x32_bf16 a[32:35], v[200:203], v[72:75], a[32:35]\n\t" \
  "v_mfma_f32_16x16x32_bf16 a[68:71], v[200:203], v[76:79], a[68:71]\n\t" \
  "v_mfma_f32_16x16x32_bf16 a[104:107], v[200:203], v[80:83], a[104:107]\n\t" \
  "v_mfma_f32_16x16x32_bf16 a[140:143], v[200:203], v[84:87], a[140:143]\n\t" \
  "ds_read_b64_tr_b16 v[200:201], v208 offset:27744\n\t" \
  "ds_read_b64_tr_b16 v[202:203], v208 offset:32352\n\t" \
  "s_waitcnt lgkmcnt(6)\n\t" \
  "v_mfma_f32_16x16x32_bf16 a[0:3], v[204:207], v[92:95], a[0:3]\n\t" \
  "v_mfma_f32_16x16x32_bf16 a[36:39], v[204:207], v[96:99], a[36:39]\n\t" \
  "v_mfma_f32_16x16x32_bf16 a[72:75], v[204:207], v[100:103], a[72:75]\n\t" \
  "v_mfma_f32_16x16x32_bf16 a[108:111], v[204:207], v[104:107], a[108:111]\n\t" \
  "v_mfma_f32_16x16x32_bf16 a[144:147], v[204:207], v[108:111], a[144:147]\n\t" \
  "ds_read_b64_tr_b16 v[204:205], v208 offset:27776\n\t" \
  "ds_read_b64_tr_b16 v[206:207], v208 offset:32384\n\t" \
  "s_waitcnt lgkmcnt(6)\n\t" \
  "v_mfma_f32_16x16x32_bf16 a[4:7], v[192:195], v[92:95], a[4:7]\n\t" \
  "v_mfma_f32_16x16x32_bf16 a[40:43], v[192:195], v[96:99], a[40:43]\n\t" \
  "v_mfma_f32_16x16x32_bf16 a[76:79], v[192:195], v[100:103], a[76:79]\n\t" \
  "v_mfma_f32_16x16x32_bf16 a[112:115], v[192:195], v[104:107], a[112:115]\n\t" \
  "v_mfma_f32_16x16x32_bf16 a[148:151], v[192:195], v[108:111], a[148:151]\n\t" \
  "ds_read_b64_tr_b16 v[192:193], v209 offset:27808\n\t" \
  "ds_read_b64_tr_b16 v[194:195], v209 offset:32416\n\t" \
  "s_waitcnt lgkmcnt(6)\n\t" \
  "v_mfma_f32_16x16x32_bf16 a[8:11], v[196:199], v[92:95], a[8:11]\n\t" \
  "v_mfma_f32_16x16x32_bf16 a[44:47], v[196:199], v[96:99], a[44:47]\n\t" \
  "v_mfma_f32_16x16x32_bf16 a[80:83], v[196:199], v[100:103], a[80:83]\n\t" \
  "v_mfma_f32_16x16x32_bf16 a[116:119], v[196:199], v[104:107], a[116:119]\n\t" \
  "v_mfma_f32_16x16x32_bf16 a[152:155], v[196:199], v[108:111], a[152:155]\n\t" \
  "ds_read_b64_tr_b16 v[196:197], v209 offset:27840\n\t" \
  "ds_read_b64_tr_b16 v[198:199], v209 offset:32448\n\t" \
  "s_waitcnt lgkmcnt(6)\n\t" \
  "v_mfma_f32_16x16x32_bf16 a[12:15], v[200:203], v[92:95], a[12:15]\n\t" \
  "v_mfma_f32_16x16x32_bf16 a[48:51], v[200:203], v[96:99], a[48:51]\n\t" \
  "v_mfma_f32_16x16x32_bf16 a[84:87], v[200:203], v[100:103], a[84:87]\n\t" \
  "v_mfma_f32_16x16x32_bf16 a[120:123], v[200:203], v[104:107], a[120:123]\n\t" \
  "v_mfma_f32_16x16x32_bf16 a[156:159], v[200:203], v[108:111], a[156:159]\n\t" \
  "ds_read_b64_tr_b16 v[200:201], v209 offset:27872\n\t" \
  "ds_read_b64_tr_b16 v[202:203], v209 offset:32480\n\t" \
  "s_waitcnt lgkmcnt(6)\n\t" \
  "v_mfma_f32_16x16x32_bf16 a[16:19], v[204:207], v[92:95], a[16:19]\n\t" \
  "v_mfma_f32_16x16x32_bf16 a[52:55], v[204:207], v[96:99], a[52:55]\n\t" \
  "v_mfma_f32_16x16x32_bf16 a[88:91], v[204:207], v[100:103], a[88:91]\n\t" \
  "v_mfma_f32_16x16x32_bf16 a[124:127], v[204:207], v[104:107], a[124:127]\n\t" \
  "v_mfma_f32_16x16x32_bf16 a[160:163], v[204:207], v[108:111], a[160:163]\n\t" \
  "ds_read_b64_tr_b16 v[204:205], v209 offset:27904\n\t" \
  "ds_read_b64_tr_b16 v[206:207], v209 offset:32512\n\t" \
  "s_waitcnt lgkmcnt(6)\n\t" \
  "v_mfma_f32_16x16x32_bf16 a[20:23], v[192:195], v[92:95], a[20:23]\n\t" \
  "v_mfma_f32_16x16x32_bf16 a[56:59], v[192:195], v[96:99], a[56:59]\n\t" \
  "v_mfma_f32_16x16x32_bf16 a[92:95], v[192:195], v[100:103], a[92:95]\n\t" \
  "v_mfma_f32_16x16x32_bf16 a[128:131], v[192:195], v[104:107], a[128:131]\n\t" \
  "s_waitcnt lgkmcnt(4)\n\t" \
  "v_mfma_f32_16x16x32_bf16 a[24:27], v[196:199], v[92:95], a[24:27]\n\t" \
  "v_mfma_f32_16x16x32_bf16 a[60:63], v[196:199], v[96:99], a[60:63]\n\t" \
  "v_mfma_f32_16x16x32_bf16 a[96:99], v[196:199], v[100:103], a[96:99]\n\t" \
  "v_mfma_f32_16x16x32_bf16 a[132:135], v[196:199], v[104:107], a[132:135]\n\t" \
  "s_waitcnt lgkmcnt(2)\n\t" \
  "v_mfma_f32_16x16x32_bf16 a[28:31], v[200:203], v[92:95], a[28:31]\n\t" \
  "v_mfma_f32_16x16x32_bf16 a[64:67], v[200:203], v[96:99], a[64:67]\n\t" \
  "v_mfma_f32_16x16x32_bf16 a[100:103], v[200:203], v[100:103], a[100:103]\n\t" \
  "v_mfma_f32_16x16x32_bf16 a[136:139], v[200:203], v[104:107], a[136:139]\n\t" \
  "s_waitcnt lgkmcnt(0)\n\t" \
  "v_mfma_f32_16x16x32_bf16 a[32:35], v[204:207], v[92:95], a[32:35]\n\t" \
  "v_mfma_f32_16x16x32_bf16 a[68:71], v[204:207], v[96:99], a[68:71]\n\t" \
  "v_mfma_f32_16x16x32_bf16 a[104:107], v[204:207], v[100:103], a[104:107]\n\t" \
  "v_mfma_f32_16x16x32_bf16 a[140:143], v[204:207], v[104:107], a[140:143]\n\t" \
  "s_cmp_lg_u32 s73, 0\n\t" \
  "s_cbranch_scc1 FPL0_%=\n\t" \
  "FP2L_%=:\n\t" \
  "buffer_load_dwordx2 v[250:251], v252, s[96:99], 0 offen sc1\n\t" \
  "s_waitcnt vmcnt(0)\n\t" \
  "v_cmp_eq_u64 vcc, s[52:53], v[250:251]\n\t" \
  "s_nop 4\n\t" \
  "s_cmp_eq_u64 vcc, exec\n\t" \
  "s_cbranch_scc1 FP2D_%=\n\t" \
  "s_sleep 2\n\t" \
  "s_add_u32 s74, s74, 1\n\t" \
  "s_cmp_lt_u32 s74, 0x40000\n\t" \
  "s_cbranch_scc1 FP2L_%=\n\t" \
  "FP2D_%=:\n\t" \
  "FPL0_%=:\n\t" \
  "s_barrier\n\t" \
  "s_add_u32 s80, s83, s69\n\t" \
  "s_add_u32 s81, s83, s69\n\t" \
  "s_add_u32 s81, s81, 131072\n\t" \
  "s_add_u32 s82, s83, s69\n\t" \
  "s_add_u32 s82, s82, 262144\n\t" \
  "s_add_u32 s87, s83, s69\n\t" \
  "s_add_u32 s87, s87, 393216\n\t" \
  "s_add_u32 s95, s75, s69\n\t" \
  "buffer_load_dwordx4 v[112:115], v244, s[56:59], s80 offen sc1\n\t" \
  "buffer_load_dwordx4 v[116:119], v244, s[56:59], s81 offen sc1\n\t" \
  "buffer_load_dwordx4 v[120:123], v244, s[56:59], s82 offen sc1\n\t" \
  "buffer_load_dwordx4 v[124:127], v244, s[56:59], s87 offen sc1\n\t" \
  "buffer_load_dwordx4 v[128:131], v244, s[56:59], s95 offen sc1\n\t" \
  "buffer_load_dwordx4 v[132:135], v244, s[56:59], s80 offen offset:1024 sc1\n\t" \
  "buffer_load_dwordx4 v[136:139], v244, s[56:59], s81 offen offset:1024 sc1\n\t" \
  "buffer_load_dwordx4 v[140:143], v244, s[56:59], s82 offen offset:1024 sc1\n\t" \
  "buffer_load_dwordx4 v[144:147], v244, s[56:59], s87 offen offset:1024 sc1\n\t" \
  "buffer_load_dwordx4 v[148:151], v244, s[56:59], s95 offen offset:1024 sc1\n\t" \
  "buffer_load_dwordx4 v[152:155], v244, s[56:59], s80 offen offset:2048 sc1\n\t" \
  "buffer_load_dwordx4 v[156:159], v244, s[56:59], s81 offen offset:2048 sc1\n\t" \
  "buffer_load_dwordx4 v[160:163], v244, s[56:59], s82 offen offset:2048 sc1\n\t" \
  "buffer_load_dwordx4 v[164:167], v244, s[56:59], s87 offen offset:2048 sc1\n\t" \
  "buffer_load_dwordx4 v[168:171], v244, s[56:59], s95 offen offset:2048 sc1\n\t" \
  "buffer_load_dwordx4 v[172:175], v244, s[56:59], s80 offen offset:3072 sc1\n\t" \
  "buffer_load_dwordx4 v[176:179], v244, s[56:59], s81 offen offset:3072 sc1\n\t" \
  "buffer_load_dwordx4 v[180:183], v244, s[56:59], s82 offen offset:3072 sc1\n\t" \
  "buffer_load_dwordx4 v[184:187], v244, s[56:59], s87 offen offset:3072 sc1\n\t" \
  "buffer_load_dwordx4 v[188:191], v244, s[56:59], s95 offen offset:3072 sc1\n\t" \
  "s_waitcnt vmcnt(20)\n\t" \
  "s_barrier\n\t" \
  "v_add_u32 v208, 73728, v210\n\t" \
  "v_add_u32 v209, 73728, v211\n\t" \
  "ds_read_b64_tr_b16 v[192:193], v208 offset:0\n\t" \
  "ds_read_b64_tr_b16 v[194:195], v208 offset:4608\n\t" \
  "ds_read_b64_tr_b16 v[196:197], v208 offset:32\n\t" \
  "ds_read_b64_tr_b16 v[198:199], v208 offset:4640\n\t" \
  "ds_read_b64_tr_b16 v[200:201], v208 offset:64\n\t" \
  "ds_read_b64_tr_b16 v[202:203], v208 offset:4672\n\t" \
  "ds_read_b64_tr_b16 v[204:205], v208 offset:96\n\t" \
  "ds_read_b64_tr_b16 v[206:207], v208 offset:4704\n\t" \
  "s_waitcnt vmcnt(15)\n\t" \
  "s_waitcnt lgkmcnt(6)\n\t" \
  "v_mfma_f32_16x16x32_bf16 a[0:3], v[192:195], v[112:115], a[0:3]\n\t" \
  "s_add_u32 s72, s72, 1\n\t" \
  "v_mfma_f32_16x16x32_bf16 a[36:39], v[192:195], v[116:119], a[36:39]\n\t" \
  "s_and_b32 s72, s72, 3\n\t" \
  "v_mfma_f32_16x16x32_bf16 a[72:75], v[192:195], v[120:123], a[72:75]\n\t" \
  "v_mfma_f32_16x16x32_bf16 a[108:111], v[192:195], v[124:127], a[108:111]\n\t" \
  "s_lshl_b32 s61, s64, 2\n\t" \
  "v_mfma_f32_16x16x32_bf16 a[144:147], v[192:195], v[128:131], a[144:147]\n\t" \
  "ds_read_b64_tr_b16 v[192:193], v208 offset:128\n\t" \
  "ds_read_b64_tr_b16 v[194:195], v208 offset:4736\n\t" \
  "s_waitcnt lgkmcnt(6)\n\t" \
  "v_mfma_f32_16x16x32_bf16 a[4:7], v[196:199], v[112:115], a[4:7]\n\t" \
  "s_add_u32 s61, s61, s72\n\t" \
  "v_mfma_f32_16x16x32_bf16 a[40:43], v[196:199], v[116:119], a[40:43]\n\t" \
  "s_lshl_b32 s61, s61, 1\n\t" \
  "v_mfma_f32_16x16x32_bf16 a[76:79], v[196:199], v[120:123], a[76:79]\n\t" \
  "s_add_u32 s61, s61, s66\n\t" \
  "v_mfma_f32_16x16x32_bf16 a[112:115], v[196:199], v[124:127], a[112:115]\n\t" \
  "v_mfma_f32_16x16x32_bf16 a[148:151], v[196:199], v[128:131], a[148:151]\n\t" \
  "ds_read_b64_tr_b16 v[196:197], v209 offset:160\n\t" \
  "ds_read_b64_tr_b16 v[198:199], v209 offset:4768\n\t" \
  "s_waitcnt lgkmcnt(6)\n\t" \
  "v_mfma_f32_16x16x32_bf16 a[8:11], v[200:203], v[112:115], a[8:11]\n\t" \
  "s_mul_i32 s68, s61, 147456\n\t" \
  "s_mul_i32 s61, s85, 73728\n\t" \
  "v_mfma_f32_16x16x32_bf16 a[44:47], v[200:203], v[116:119], a[44:47]\n\t" \
  "v_mfma_f32_16x16x32_bf16 a[80:83], v[200:203], v[120:123], a[80:83]\n\t" \
  "s_add_u32 s68, s68, s61\n\t" \
  "v_mfma_f32_16x16x32_bf16 a[116:119], v[200:203], v[124:127], a[116:119]\n\t" \
  "s_sub_i32 s69, s72, s65\n\t" \
  "v_mfma_f32_16x16x32_bf16 a[152:155], v[200:203], v[128:131], a[152:155]\n\t" \
  "ds_read_b64_tr_b16 v[200:201], v209 offset:192\n\t" \
  "ds_read_b64_tr_b16 v[202:203], v209 offset:4800\n\t" \
  "s_waitcnt lgkmcnt(6)\n\t" \
  "v_mfma_f32_16x16x32_bf16 a[12:15], v[204:207], v[112:115], a[12:15]\n\t" \
  "s_lshl_b32 s69, s69, 13\n\t" \
  "v_mfma_f32_16x16x32_bf16 a[48:51], v[204:207], v[116:119], a[48:51]\n\t" \
  "s_add_u32 s62, s67, 0\n\t" \
  "v_mfma_f32_16x16x32_bf16 a[84:87], v[204:207], v[120:123], a[84:87]\n\t" \
  "v_mfma_f32_16x16x32_bf16 a[120:123], v[204:207], v[124:127], a[120:123]\n\t" \
  "s_add_u32 m0, s62, 0\n\t" \
  "v_mfma_f32_16x16x32_bf16 a[156:159], v[204:207], v[128:131], a[156:159]\n\t" \
  "ds_read_b64_tr_b16 v[204:205], v209 offset:224\n\t" \
  "ds_read_b64_tr_b16 v[206:207], v209 offset:4832\n\t" \
  "s_waitcnt lgkmcnt(6)\n\t" \
  "v_mfma_f32_16x16x32_bf16 a[16:19], v[192:195], v[112:115], a[16:19]\n\t" \
  "buffer_load_dwordx4 v232, s[40:43], s68 offen lds\n\t" \
  "v_mfma_f32_16x16x32_bf16 a[52:55], v[192:195], v[116:119], a[52:55]\n\t" \
  "s_add_u32 m0, s62, 1024\n\t" \
  "v_mfma_f32_16x16x32_bf16 a[88:91], v[192:195], v[120:123], a[88:91]\n\t" \
  "buffer_load_dwordx4 v233, s[40:43], s68 offen lds\n\t" \
  "v_mfma_f32_16x16x32_bf16 a[124:127], v[192:195], v[124:127], a[124:127]\n\t" \
  "v_mfma_f32_16x16x32_bf16 a[160:163], v[192:195], v[128:131], a[160:163]\n\t" \
  "ds_read_b64_tr_b16 v[192:193], v209 offset:256\n\t" \
  "ds_read_b64_tr_b16 v[194:195], v209 offset:4864\n\t" \
  "s_waitcnt lgkmcnt(6)\n\t" \
  "v_mfma_f32_16x16x32_bf16 a[20:23], v[196:199], v[112:115], a[20:23]\n\t" \
  "s_add_u32 m0, s62, 2048\n\t" \
  "s_nop 0\n\t" \
  "buffer_load_dwordx4 v234, s[40:43], s68 offen lds\n\t" \
  "v_mfma_f32_16x16x32_bf16 a[56:59], v[196:199], v[116:119], a[56:59]\n\t" \
  "v_mfma_f32_16x16x32_bf16 a[92:95], v[196:199], v[120:123], a[92:95]\n\t" \
  "s_add_u32 m0, s62, 3072\n\t" \
  "v_mfma_f32_16x16x32_bf16 a[128:131], v[196:199], v[124:127], a[128:131]\n\t" \
  "ds_read_b64_tr_b16 v[196:197], v208 offset:9216\n\t" \
  "ds_read_b64_tr_b16 v[198:199], v208 offset:13824\n\t" \
  "s_waitcnt lgkmcnt(6)\n\t" \
  "v_mfma_f32_16x16x32_bf16 a[24:27], v[200:203], v[112:115], a[24:27]\n\t" \
  "buffer_load_dwordx4 v235, s[40:43], s68 offen lds\n\t" \
  "v_mfma_f32_16x16x32_bf16 a[60:63], v[200:203], v[116:119], a[60:63]\n\t" \
  "s_add_u32 m0, s62, 4096\n\t" \
  "v_mfma_f32_16x16x32_bf16 a[96:99], v[200:203], v[120:123], a[96:99]\n\t" \
  "buffer_load_dwordx4 v236, s[40:43], s68 offen lds\n\t" \
  "v_mfma_f32_16x16x32_bf16 a[132:135], v[200:203], v[124:127], a[132:135]\n\t" \
  "ds_read_b64_tr_b16 v[200:201], v208 offset:9248\n\t" \
  "ds_read_b64_tr_b16 v[202:203], v208 offset:13856\n\t" \
  "s_waitcnt lgkmcnt(4)\n\t" \
  "v_mfma_f32_16x16x32_bf16 a[28:31], v[204:207], v[112:115], a[28:31]\n\t" \
  "s_add_u32 m0, s62, 5120\n\t" \
  "v_mfma_f32_16x16x32_bf16 a[64:67], v[204:207], v[116:119], a[64:67]\n\t" \
  "buffer_load_dwordx4 v237, s[40:43], s68 offen lds\n\t" \
  "v_mfma_f32_16x16x32_bf16 a[100:103], v[204:207], v[120:123], a[100:103]\n\t" \
  "v_mfma_f32_16x16x32_bf16 a[136:139], v[204:207], v[124:127], a[136:139]\n\t" \
  "ds_read_b64_tr_b16 v[204:205], v208 offset:9280\n\t" \
  "ds_read_b64_tr_b16 v[206:207], v208 offset:13888\n\t" \
  "v_mfma_f32_16x16x32_bf16 a[32:35], v[192:195], v[112:115], a[32:35]\n\t" \
  "s_add_u32 m0, s62, 6144\n\t" \
  "s_nop 0\n\t" \
  "buffer_load_dwordx4 v238, s[40:43], s68 offen lds\n\t" \
  "v_mfma_f32_16x16x32_bf16 a[68:71], v[192:195], v[116:119], a[68:71]\n\t" \
  "v_mfma_f32_16x16x32_bf16 a[104:107], v[192:195], v[120:123], a[104:107]\n\t" \
  "s_add_u32 m0, s62, 7168\n\t" \
  "v_mfma_f32_16x16x32_bf16 a[140:143], v[192:195], v[124:127], a[140:143]\n\t" \
  "ds_read_b64_tr_b16 v[192:193], v208 offset:9312\n\t" \
  "ds_read_b64_tr_b16 v[194:195], v208 offset:13920\n\t" \
  "s_waitcnt vmcnt(17)\n\t" \
  "s_waitcnt lgkmcnt(6)\n\t" \
  "v_mfma_f32_16x16x32_bf16 a[0:3], v[196:199], v[132:135], a[0:3]\n\t" \
  "buffer_load_dwordx4 v239, s[40:43], s68 offen lds\n\t" \
  "v_mfma_f32_16x16x32_bf16 a[36:39], v[196:199], v[136:139], a[36:39]\n\t" \
  "s_add_u32 m0, s62, 8192\n\t" \
  "v_mfma_f32_16x16x32_bf16 a[72:75], v[196:199], v[140:143], a[72:75]\n\t" \
  "buffer_load_dwordx4 v240, s[40:43], s68 offen lds\n\t" \
  "v_mfma_f32_16x16x32_bf16 a[108:111], v[196:199], v[144:147], a[108:111]\n\t" \
  "v_mfma_f32_16x16x32_bf16 a[144:147], v[196:199], v[148:151], a[144:147]\n\t" \
  "ds_read_b64_tr_b16 v[196:197], v208 offset:9344\n\t" \
  "ds_read_b64_tr_b16 v[198:199], v208 offset:13952\n\t" \
  "s_waitcnt lgkmcnt(6)\n\t" \
  "v_mfma_f32_16x16x32_bf16 a[4:7], v[200:203], v[132:135], a[4:7]\n\t" \
  "s_add_u32 s61, s68, 36864\n\t" \
  "s_add_u32 m0, s62, 9216\n\t" \
  "v_mfma_f32_16x16x32_bf16 a[40:43], v[200:203], v[136:139], a[40:43]\n\t" \
  "v_mfma_f32_16x16x32_bf16 a[76:79], v[200:203], v[140:143], a[76:79]\n\t" \
  "buffer_load_dwordx4 v232, s[40:43], s61 offen lds\n\t" \
  "v_mfma_f32_16x16x32_bf16 a[112:115], v[200:203], v[144:147], a[112:115]\n\t" \
  "s_add_u32 m0, s62, 10240\n\t" \
  "v_mfma_f32_16x16x32_bf16 a[148:151], v[200:203], v[148:151], a[148:151]\n\t" \
  "ds_read_b64_tr_b16 v[200:201], v209 offset:9376\n\t" \
  "ds_read_b64_tr_b16 v[202:203], v209 offset:13984\n\t" \
  "s_waitcnt lgkmcnt(6)\n\t" \
  "v_mfma_f32_16x16x32_bf16 a[8:11], v[204:207], v[132:135], a[8:11]\n\t" \
  "buffer_load_dwordx4 v233, s[40:43], s61 offen lds\n\t" \
  "v_mfma_f32_16x16x32_bf16 a[44:47], v[204:207], v[136:139], a[44:47]\n\t" \
  "s_add_u32 m0, s62, 11264\n\t" \
  "v_mfma_f32_16x16x32_bf16 a[80:83], v[204:207], v[140:143], a[80:83]\n\t" \
  "v_mfma_f32_16x16x32_bf16 a[116:119], v[204:207], v[144:147], a[116:119]\n\t" \
  "buffer_load_dwordx4 v234, s[40:43], s61 offen lds\n\t" \
  "v_mfma_f32_16x16x32_bf16 a[152:155], v[204:207], v[148:151], a[152:155]\n\t" \
  "ds_read_b64_tr_b16 v[204:205], v209 offset:9408\n\t" \
  "ds_read_b64_tr_b16 v[206:207], v209 offset:14016\n\t" \
  "s_waitcnt lgkmcnt(6)\n\t" \
  "v_mfma_f32_16x16x32_bf16 a[12:15], v[192:195], v[132:135], a[12:15]\n\t" \
  "s_add_u32 m0, s62, 12288\n\t" \
  "v_mfma_f32_16x16x32_bf16 a[48:51], v[192:195], v[136:139], a[48:51]\n\t" \
  "buffer_load_dwordx4 v235, s[40:43], s61 offen lds\n\t" \
  "v_mfma_f32_16x16x32_bf16 a[84:87], v[192:195], v[140:143], a[84:87]\n\t" \
  "s_add_u32 m0, s62, 13312\n\t" \
  "v_mfma_f32_16x16x32_bf16 a[120:123], v[192:195], v[144:147], a[120:123]\n\t" \
  "v_mfma_f32_16x16x32_bf16 a[156:159], v[192:195], v[148:151], a[156:159]\n\t" \
  "ds_read_b64_tr_b16 v[192:193], v209 offset:9440\n\t" \
  "ds_read_b64_tr_b16 v[194:195], v209 offset:14048\n\t" \
  "s_waitcnt lgkmcnt(6)\n\t" \
  "v_mfma_f32_16x16x32_bf16 a[16:19], v[196:199], v[132:135], a[16:19]\n\t" \
  "buffer_load_dwordx4 v236, s[40:43], s61 offen lds\n\t" \
  "s_add_u32 m0, s62, 14336\n\t" \
  "v_mfma_f32_16x16x32_bf16 a[52:55], v[196:199], v[136:139], a[52:55]\n\t" \
  "v_mfma_f32_16x16x32_bf16 a[88:91], v[196:199], v[140:143], a[88:91]\n\t" \
  "buffer_load_dwordx4 v237, s[40:43], s61 offen lds\n\t" \
  "v_mfma_f32_16x16x32_bf16 a[124:127], v[196:199], v[144:147], a[124:127]\n\t" \
  "s_add_u32 m0, s62, 15360\n\t" \
  "v_mfma_f32_16x16x32_bf16 a[160:163], v[196:199], v[148:151], a[160:163]\n\t" \
  "ds_read_b64_tr_b16 v[196:197], v209 offset:9472\n\t" \
  "ds_read_b64_tr_b16 v[198:199], v209 offset:14080\n\t" \
  "s_waitcnt lgkmcnt(4)\n\t" \
  "v_mfma_f32_16x16x32_bf16 a[20:23], v[200:203], v[132:135], a[20:23]\n\t" \
  "buffer_load_dwordx4 v238, s[40:43], s61 offen lds\n\t" \
  "v_mfma_f32_16x16x32_bf16 a[56:59], v[200:203], v[136:139], a[56:59]\n\t" \
  "s_add_u32 m0, s62, 16384\n\t" \
  "v_mfma_f32_16x16x32_bf16 a[92:95], v[200:203], v[140:143], a[92:95]\n\t" \
  "v_mfma_f32_16x16x32_bf16 a[128:131], v[200:203], v[144:147], a[128:131]\n\t" \
  "ds_read_b64_tr_b16 v[200:201], v208 offset:18432\n\t" \
  "ds_read_b64_tr_b16 v[202:203], v208 offset:23040\n\t" \
  "v_mfma_f32_16x16x32_bf16 a[24:27], v[204:207], v[132:135], a[24:27]\n\t" \
  "buffer_load_dwordx4 v239, s[40:43], s61 offen lds\n\t" \
  "s_add_u32 m0, s62, 17408\n\t" \
  "v_mfma_f32_16x16x32_bf16 a[60:63], v[204:207], v[136:139], a[60:63]\n\t" \
  "v_mfma_f32_16x16x32_bf16 a[96:99], v[204:207], v[140:143], a[96:99]\n\t" \
  "buffer_load_dwordx4 v240, s[40:43], s61 offen lds\n\t" \
  "v_mfma_f32_16x16x32_bf16 a[132:135], v[204:207], v[144:147], a[132:135]\n\t" \
  "ds_read_b64_tr_b16 v[204:205], v208 offset:18464\n\t" \
  "ds_read_b64_tr_b16 v[206:207], v208 offset:23072\n\t" \
  "s_waitcnt lgkmcnt(5)\n\t" \
  "v_mfma_f32_16x16x32_bf16 a[28:31], v[192:195], v[132:135], a[28:31]\n\t" \
  "v_mfma_f32_16x16x32_bf16 a[64:67], v[192:195], v[136:139], a[64:67]\n\t" \
  "s_add_u32 s80, s83, s69\n\t" \
  "v_mfma_f32_16x16x32_bf16 a[100:103], v[192:195], v[140:143], a[100:103]\n\t" \
  "v_mfma_f32_16x16x32_bf16 a[136:139], v[192:195], v[144:147], a[136:139]\n\t" \
  "ds_read_b64_tr_b16 v[192:193], v208 offset:18496\n\t" \
  "ds_read_b64_tr_b16 v[194:195], v208 offset:23104\n\t" \
  "s_waitcnt lgkmcnt(6)\n\t" \
  "v_mfma_f32_16x16x32_bf16 a[32:35], v[196:199], v[132:135], a[32:35]\n\t" \
  "s_add_u32 s81, s83, s69\n\t" \
  "v_mfma_f32_16x16x32_bf16 a[68:71], v[196:199], v[136:139], a[68:71]\n\t" \
  "v_mfma_f32_16x16x32_bf16 a[104:107], v[196:199], v[140:143], a[104:107]\n\t" \
  "s_add_u32 s81, s81, 131072\n\t" \
  "v_mfma_f32_16x16x32_bf16 a[140:143], v[196:199], v[144:147], a[140:143]\n\t" \
  "ds_read_b64_tr_b16 v[196:197], v208 offset:18528\n\t" \
  "ds_read_b64_tr_b16 v[198:199], v208 offset:23136\n\t" \
  "s_waitcnt vmcnt(23)\n\t" \
  "s_waitcnt lgkmcnt(6)\n\t" \
  "v_mfma_f32_16x16x32_bf16 a[0:3], v[200:203], v[152:155], a[0:3]\n\t" \
  "s_add_u32 s82, s83, s69\n\t" \
  "v_mfma_f32_16x16x32_bf16 a[36:39], v[200:203], v[156:159], a[36:39]\n\t" \
  "v_mfma_f32_16x16x32_bf16 a[72:75], v[200:203], v[160:163], a[72:75]\n\t" \
  "v_mfma_f32_16x16x32_bf16 a[108:111], v[200:203], v[164:167], a[108:111]\n\t" \
  "s_add_u32 s82, s82, 262144\n\t" \
  "v_mfma_f32_16x16x32_bf16 a[144:147], v[200:203], v[168:171], a[144:147]\n\t" \
  "ds_read_b64_tr_b16 v[200:201], v208 offset:18560\n\t" \
  "ds_read_b64_tr_b16 v[202:203], v208 offset:23168\n\t" \
  "s_waitcnt lgkmcnt(6)\n\t" \
  "v_mfma_f32_16x16x32_bf16 a[4:7], v[204:207], v[152:155], a[4:7]\n\t" \
  "s_add_u32 s87, s83, s69\n\t" \
  "v_mfma_f32_16x16x32_bf16 a[40:43], v[204:207], v[156:159], a[40:43]\n\t" \
  "v_mfma_f32_16x16x32_bf16 a[76:79], v[204:207], v[160:163], a[76:79]\n\t" \
  "v_mfma_f32_16x16x32_bf16 a[112:115], v[204:207], v[164:167], a[112:115]\n\t" \
  "s_add_u32 s87, s87, 393216\n\t" \
  "v_mfma_f32_16x16x32_bf16 a[148:151], v[204:207], v[168:171], a[148:151]\n\t" \
  "ds_read_b64_tr_b16 v[204:205], v209 offset:18592\n\t" \
  "ds_read_b64_tr_b16 v[206:207], v209 offset:23200\n\t" \
  "s_waitcnt lgkmcnt(6)\n\t" \
  "v_mfma_f32_16x16x32_bf16 a[8:11], v[192:195], v[152:155], a[8:11]\n\t" \
  "s_add_u32 s95, s75, s69\n\t" \
  "v_mfma_f32_16x16x32_bf16 a[44:47], v[192:195], v[156:159], a[44:47]\n\t" \
  "v_mfma_f32_16x16x32_bf16 a[80:83], v[192:195], v[160:163], a[80:83]\n\t" \
  "v_mfma_f32_16x16x32_bf16 a[116:119], v[192:195], v[164:167], a[116:119]\n\t" \
  "buffer_load_dwordx4 v[32:35], v244, s[56:59], s80 offen sc1\n\t" \
  "v_mfma_f32_16x16x32_bf16 a[152:155], v[192:195], v[168:171], a[152:155]\n\t" \
  "ds_read_b64_tr_b16 v[192:193], v209 offset:18624\n\t" \
  "ds_read_b64_tr_b16 v[194:195], v209 offset:23232\n\t" \
  "s_waitcnt lgkmcnt(6)\n\t" \
  "v_mfma_f32_16x16x32_bf16 a[12:15], v[196:199], v[152:155], a[12:15]\n\t" \
  "buffer_load_dwordx4 v[36:39], v244, s[56:59], s81 offen sc1\n\t" \
  "v_mfma_f32_16x16x32_bf16 a[48:51], v[196:199], v[156:159], a[48:51]\n\t" \
  "v_mfma_f32_16x16x32_bf16 a[84:87], v[196:199], v[160:163], a[84:87]\n\t" \
  "v_mfma_f32_16x16x32_bf16 a[120:123], v[196:199], v[164:167], a[120:123]\n\t" \
  "buffer_load_dwordx4 v[40:43], v244, s[56:59], s82 offen sc1\n\t" \
  "v_mfma_f32_16x16x32_bf16 a[156:159], v[196:199], v[168:171], a[156:159]\n\t" \
  "ds_read_b64_tr_b16 v[196:197], v209 offset:18656\n\t" \
  "ds_read_b64_tr_b16 v[198:199], v209 offset:23264\n\t" \
  "s_waitcnt lgkmcnt(6)\n\t" \
  "v_mfma_f32_16x16x32_bf16 a[16:19], v[200:203], v[152:155], a[16:19]\n\t" \
  "buffer_load_dwordx4 v[44:47], v244, s[56:59], s87 offen sc1\n\t" \
  "v_mfma_f32_16x16x32_bf16 a[52:55], v[200:203], v[156:159], a[52:55]\n\t" \
  "v_mfma_f32_16x16x32_bf16 a[88:91], v[200:203], v[160:163], a[88:91]\n\t" \
  "v_mfma_f32_16x16x32_bf16 a[124:127], v[200:203], v[164:167], a[124:127]\n\t" \
  "buffer_load_dwordx4 v[48:51], v244, s[56:59], s95 offen sc1\n\t" \
  "v_mfma_f32_16x16x32_bf16 a[160:163], v[200:203], v[168:171], a[160:163]\n\t" \
  "ds_read_b64_tr_b16 v[200:201], v209 offset:18688\n\t" \
  "ds_read_b64_tr_b16 v[202:203], v209 offset:23296\n\t" \
  "s_waitcnt lgkmcnt(4)\n\t" \
  "v_mfma_f32_16x16x32_bf16 a[20:23], v[204:207], v[152:155], a[20:23]\n\t" \
  "buffer_load_dwordx4 v[52:55], v244, s[56:59], s80 offen offset:1024 sc1\n\t" \
  "v_mfma_f32_16x16x32_bf16 a[56:59], v[204:207], v[156:159], a[56:59]\n\t" \
  "v_mfma_f32_16x16x32_bf16 a[92:95], v[204:207], v[160:163], a[92:95]\n\t" \
  "v_mfma_f32_16x16x32_bf16 a[128:131], v[204:207], v[164:167], a[128:131]\n\t" \
  "ds_read_b64_tr_b16 v[204:205], v208 offset:27648\n\t" \
  "ds_read_b64_tr_b16 v[206:207], v208 offset:32256\n\t" \
  "v_mfma_f32_16x16x32_bf16 a[24:27], v[192:195], v[152:155], a[24:27]\n\t" \
  "buffer_load_dwordx4 v[56:59], v244, s[56:59], s81 offen offset:1024 sc1\n\t" \
  "v_mfma_f32_16x16x32_bf16 a[60:63], v[192:195], v[156:159], a[60:63]\n\t" \
  "buffer_load_dwordx4 v[60:63], v244, s[56:59], s82 offen offset:1024 sc1\n\t" \
  "v_mfma_f32_16x16x32_bf16 a[96:99], v[192:195], v[160:163], a[96:99]\n\t" \
  "v_mfma_f32_16x16x32_bf16 a[132:135], v[192:195], v[164:167], a[132:135]\n\t" \
  "ds_read_b64_tr_b16 v[192:193], v208 offset:27680\n\t" \
  "ds_read_b64_tr_b16 v[194:195], v208 offset:32288\n\t" \
  "s_waitcnt lgkmcnt(6)\n\t" \
  "v_mfma_f32_16x16x32_bf16 a[28:31], v[196:199], v[152:155], a[28:31]\n\t" \
  "buffer_load_dwordx4 v[64:67], v244, s[56:59], s87 offen offset:1024 sc1\n\t" \
  "v_mfma_f32_16x16x32_bf16 a[64:67], v[196:199], v[156:159], a[64:67]\n\t" \
  "v_mfma_f32_16x16x32_bf16 a[100:103], v[196:199], v[160:163], a[100:103]\n\t" \
  "buffer_load_dwordx4 v[68:71], v244, s[56:59], s95 offen offset:1024 sc1\n\t" \
  "v_mfma_f32_16x16x32_bf16 a[136:139], v[196:199], v[164:167], a[136:139]\n\t" \
  "ds_read_b64_tr_b16 v[196:197], v208 offset:27712\n\t" \
  "ds_read_b64_tr_b16 v[198:199], v208 offset:32320\n\t" \
  "s_waitcnt lgkmcnt(6)\n\t" \
  "v_mfma_f32_16x16x32_bf16 a[32:35], v[200:203], v[152:155], a[32:35]\n\t" \
  "v_mfma_f32_16x16x32_bf16 a[68:71], v[200:203], v[156:159], a[68:71]\n\t" \
  "buffer_load_dwordx4 v[72:75], v244, s[56:59], s80 offen offset:2048 sc1\n\t" \
  "v_mfma_f32_16x16x32_bf16 a[104:107], v[200:203], v[160:163], a[104:107]\n\t" \
  "v_mfma_f32_16x16x32_bf16 a[140:143], v[200:203], v[164:167], a[140:143]\n\t" \
  "ds_read_b64_tr_b16 v[200:201], v208 offset:27744\n\t" \
  "ds_read_b64_tr_b16 v[202:203], v208 offset:32352\n\t" \
  "s_waitcnt vmcnt(29)\n\t" \
  "s_waitcnt lgkmcnt(6)\n\t" \
  "v_mfma_f32_16x16x32_bf16 a[0:3], v[204:207], v[172:175], a[0:3]\n\t" \
  "buffer_load_dwordx4 v[76:79], v244, s[56:59], s81 offen offset:2048 sc1\n\t" \
  "v_mfma_f32_16x16x32_bf16 a[36:39], v[204:207], v[176:179], a[36:39]\n\t" \
  "v_mfma_f32_16x16x32_bf16 a[72:75], v[204:207], v[180:183], a[72:75]\n\t" \
  "buffer_load_dwordx4 v[80:83], v244, s[56:59], s82 offen offset:2048 sc1\n\t" \
  "v_mfma_f32_16x16x32_bf16 a[108:111], v[204:207], v[184:187], a[108:111]\n\t" \
  "v_mfma_f32_16x16x32_bf16 a[144:147], v[204:207], v[188:191], a[144:147]\n\t" \
  "ds_read_b64_tr_b16 v[204:205], v208 offset:27776\n\t" \
  "ds_read_b64_tr_b16 v[206:207], v208 offset:32384\n\t" \
  "s_waitcnt lgkmcnt(6)\n\t" \
  "v_mfma_f32_16x16x32_bf16 a[4:7], v[192:195], v[172:175], a[4:7]\n\t" \
  "buffer_load_dwordx4 v[84:87], v244, s[56:59], s87 offen offset:2048 sc1\n\t" \
  "v_mfma_f32_16x16x32_bf16 a[40:43], v[192:195], v[176:179], a[40:43]\n\t" \
  "v_mfma_f32_16x16x32_bf16 a[76:79], v[192:195], v[180:183], a[76:79]\n\t" \
  "buffer_load_dwordx4 v[88:91], v244, s[56:59], s95 offen offset:2048 sc1\n\t" \
  "v_mfma_f32_16x16x32_bf16 a[112:115], v[192:195], v[184:187], a[112:115]\n\t" \
  "v_mfma_f32_16x16x32_bf16 a[148:151], v[192:195], v[188:191], a[148:151]\n\t" \
  "ds_read_b64_tr_b16 v[192:193], v209 offset:27808\n\t" \
  "ds_read_b64_tr_b16 v[194:195], v209 offset:32416\n\t" \
  "s_waitcnt lgkmcnt(6)\n\t" \
  "v_mfma_f32_16x16x32_bf16 a[8:11], v[196:199], v[172:175], a[8:11]\n\t" \
  "buffer_load_dwordx4 v[92:95], v244, s[56:59], s80 offen offset:3072 sc1\n\t" \
  "v_mfma_f32_16x16x32_bf16 a[44:47], v[196:199], v[176:179], a[44:47]\n\t" \
  "v_mfma_f32_16x16x32_bf16 a[80:83], v[196:199], v[180:183], a[80:83]\n\t" \
  "buffer_load_dwordx4 v[96:99], v244, s[56:59], s81 offen offset:3072 sc1\n\t" \
  "v_mfma_f32_16x16x32_bf16 a[116:119], v[196:199], v[184:187], a[116:119]\n\t" \
  "v_mfma_f32_16x16x32_bf16 a[152:155], v[196:199], v[188:191], a[152:155]\n\t" \
  "ds_read_b64_tr_b16 v[196:197], v209 offset:27840\n\t" \
  "ds_read_b64_tr_b16 v[198:199], v209 offset:32448\n\t" \
  "s_waitcnt lgkmcnt(6)\n\t" \
  "v_mfma_f32_16x16x32_bf16 a[12:15], v[200:203], v[172:175], a[12:15]\n\t" \
  "buffer_load_dwordx4 v[100:103], v244, s[56:59], s82 offen offset:3072 sc1\n\t" \
  "v_mfma_f32_16x16x32_bf16 a[48:51], v[200:203], v[176:179], a[48:51]\n\t" \
  "v_mfma_f32_16x16x32_bf16 a[84:87], v[200:203], v[180:183], a[84:87]\n\t" \
  "buffer_load_dwordx4 v[104:107], v244, s[56:59], s87 offen offset:3072 sc1\n\t" \
  "v_mfma_f32_16x16x32_bf16 a[120:123], v[200:203], v[184:187], a[120:123]\n\t" \
  "v_mfma_f32_16x16x32_bf16 a[156:159], v[200:203], v[188:191], a[156:159]\n\t" \
  "ds_read_b64_tr_b16 v[200:201], v209 offset:27872\n\t" \
  "ds_read_b64_tr_b16 v[202:203], v209 offset:32480\n\t" \
  "s_waitcnt lgkmcnt(4)\n\t" \
  "v_mfma_f32_16x16x32_bf16 a[16:19], v[204:207], v[172:175], a[16:19]\n\t" \
  "buffer_load_dwordx4 v[108:111], v244, s[56:59], s95 offen offset:3072 sc1\n\t" \
  "v_mfma_f32_16x16x32_bf16 a[52:55], v[204:207], v[176:179], a[52:55]\n\t" \
  "v_mfma_f32_16x16x32_bf16 a[88:91], v[204:207], v[180:183], a[88:91]\n\t" \
  "v_mfma_f32_16x16x32_bf16 a[124:127], v[204:207], v[184:187], a[124:127]\n\t" \
  "v_mfma_f32_16x16x32_bf16 a[160:163], v[204:207], v[188:191], a[160:163]\n\t" \
  "ds_read_b64_tr_b16 v[204:205], v209 offset:27904\n\t" \
  "ds_read_b64_tr_b16 v[206:207], v209 offset:32512\n\t" \
  "v_mfma_f32_16x16x32_bf16 a[20:23], v[192:195], v[172:175], a[20:23]\n\t" \
  "v_mfma_f32_16x16x32_bf16 a[56:59], v[192:195], v[176:179], a[56:59]\n\t" \
  "v_mfma_f32_16x16x32_bf16 a[92:95], v[192:195], v[180:183], a[92:95]\n\t" \
  "v_mfma_f32_16x16x32_bf16 a[128:131], v[192:195], v[184:187], a[128:131]\n\t" \
  "s_waitcnt lgkmcnt(4)\n\t" \
  "v_mfma_f32_16x16x32_bf16 a[24:27], v[196:199], v[172:175], a[24:27]\n\t" \
  "v_mfma_f32_16x16x32_bf16 a[60:63], v[196:199], v[176:179], a[60:63]\n\t" \
  "v_mfma_f32_16x16x32_bf16 a[96:99], v[196:199], v[180:183], a[96:99]\n\t" \
  "v_mfma_f32_16x16x32_bf16 a[132:135], v[196:199], v[184:187], a[132:135]\n\t" \
  "s_waitcnt lgkmcnt(2)\n\t" \
  "v_mfma_f32_16x16x32_bf16 a[28:31], v[200:203], v[172:175], a[28:31]\n\t" \
  "v_mfma_f32_16x16x32_bf16 a[64:67], v[200:203], v[176:179], a[64:67]\n\t" \
  "v_mfma_f32_16x16x32_bf16 a[100:103], v[200:203], v[180:183], a[100:103]\n\t" \
  "v_mfma_f32_16x16x32_bf16 a[136:139], v[200:203], v[184:187], a[136:139]\n\t" \
  "s_waitcnt lgkmcnt(0)\n\t" \
  "v_mfma_f32_16x16x32_bf16 a[32:35], v[204:207], v[172:175], a[32:35]\n\t" \
  "v_mfma_f32_16x16x32_bf16 a[68:71], v[204:207], v[176:179], a[68:71]\n\t" \
  "v_mfma_f32_16x16x32_bf16 a[104:107], v[204:207], v[180:183], a[104:107]\n\t" \
  "v_mfma_f32_16x16x32_bf16 a[140:143], v[204:207], v[184:187], a[140:143]\n\t" \
  "s_waitcnt vmcnt(20)\n\t" \
  "s_barrier\n\t" \
  "v_add_u32 v208, 0, v210\n\t" \
  "v_add_u32 v209, 0, v211\n\t" \
  "ds_read_b64_tr_b16 v[192:193], v208 offset:0\n\t" \
  "ds_read_b64_tr_b16 v[194:195], v208 offset:4608\n\t" \
  "ds_read_b64_tr_b16 v[196:197], v208 offset:32\n\t" \
  "ds_read_b64_tr_b16 v[198:199], v208 offset:4640\n\t" \
  "ds_read_b64_tr_b16 v[200:201], v208 offset:64\n\t" \
  "ds_read_b64_tr_b16 v[202:203], v208 offset:4672\n\t" \
  "ds_read_b64_tr_b16 v[204:205], v208 offset:96\n\t" \
  "ds_read_b64_tr_b16 v[206:207], v208 offset:4704\n\t" \
  "s_waitcnt vmcnt(15)\n\t" \
  "s_waitcnt lgkmcnt(6)\n\t" \
  "v_mfma_f32_16x16x32_bf16 a[0:3], v[192:195], v[32:35], a[0:3]\n\t" \
  "s_add_u32 s72, s72, 1\n\t" \
  "v_mfma_f32_16x16x32_bf16 a[36:39], v[192:195], v[36:39], a[36:39]\n\t" \
  "s_and_b32 s72, s72, 3\n\t" \
  "v_mfma_f32_16x16x32_bf16 a[72:75], v[192:195], v[40:43], a[72:75]\n\t" \
  "v_mfma_f32_16x16x32_bf16 a[108:111], v[192:195], v[44:47], a[108:111]\n\t" \
  "s_lshl_b32 s61, s64, 2\n\t" \
  "v_mfma_f32_16x16x32_bf16 a[144:147], v[192:195], v[48:51], a[144:147]\n\t" \
  "ds_read_b64_tr_b16 v[192:193], v208 offset:128\n\t" \
  "ds_read_b64_tr_b16 v[194:195], v208 offset:4736\n\t" \
  "s_waitcnt lgkmcnt(6)\n\t" \
  "v_mfma_f32_16x16x32_bf16 a[4:7], v[196:199], v[32:35], a[4:7]\n\t" \
  "s_add_u32 s61, s61, s72\n\t" \
  "v_mfma_f32_16x16x32_bf16 a[40:43], v[196:199], v[36:39], a[40:43]\n\t" \
  "s_lshl_b32 s61, s61, 1\n\t" \
  "v_mfma_f32_16x16x32_bf16 a[76:79], v[196:199], v[40:43], a[76:79]\n\t" \
  "s_add_u32 s61, s61, s66\n\t" \
  "v_mfma_f32_16x16x32_bf16 a[112:115], v[196:199], v[44:47], a[112:115]\n\t" \
  "v_mfma_f32_16x16x32_bf16 a[148:151], v[196:199], v[48:51], a[148:151]\n\t" \
  "ds_read_b64_tr_b16 v[196:197], v209 offset:160\n\t" \
  "ds_read_b64_tr_b16 v[198:199], v209 offset:4768\n\t" \
  "s_waitcnt lgkmcnt(6)\n\t" \
  "v_mfma_f32_16x16x32_bf16 a[8:11], v[200:203], v[32:35], a[8:11]\n\t" \
  "s_mul_i32 s68, s61, 147456\n\t" \
  "s_mul_i32 s61, s85, 73728\n\t" \
  "v_mfma_f32_16x16x32_bf16 a[44:47], v[200:203], v[36:39], a[44:47]\n\t" \
  "v_mfma_f32_16x16x32_bf16 a[80:83], v[200:203], v[40:43], a[80:83]\n\t" \
  "s_add_u32 s68, s68, s61\n\t" \
  "v_mfma_f32_16x16x32_bf16 a[116:119], v[200:203], v[44:47], a[116:119]\n\t" \
  "s_sub_i32 s69, s72, s65\n\t" \
  "v_mfma_f32_16x16x32_bf16 a[152:155], v[200:203], v[48:51], a[152:155]\n\t" \
  "ds_read_b64_tr_b16 v[200:201], v209 offset:192\n\t" \
  "ds_read_b64_tr_b16 v[202:203], v209 offset:4800\n\t" \
  "s_waitcnt lgkmcnt(6)\n\t" \
  "v_mfma_f32_16x16x32_bf16 a[12:15], v[204:207], v[32:35], a[12:15]\n\t" \
  "s_lshl_b32 s69, s69, 13\n\t" \
  "v_mfma_f32_16x16x32_bf16 a[48:51], v[204:207], v[36:39], a[48:51]\n\t" \
  "s_add_u32 s62, s67, 73728\n\t" \
  "v_mfma_f32_16x16x32_bf16 a[84:87], v[204:207], v[40:43], a[84:87]\n\t" \
  "v_mfma_f32_16x16x32_bf16 a[120:123], v[204:207], v[44:47], a[120:123]\n\t" \
  "s_add_u32 m0, s62, 0\n\t" \
  "v_mfma_f32_16x16x32_bf16 a[156:159], v[204:207], v[48:51], a[156:159]\n\t" \
  "ds_read_b64_tr_b16 v[204:205], v209 offset:224\n\t" \
  "ds_read_b64_tr_b16 v[206:207], v209 offset:4832\n\t" \
  "s_waitcnt lgkmcnt(6)\n\t" \
  "v_mfma_f32_16x16x32_bf16 a[16:19], v[192:195], v[32:35], a[16:19]\n\t" \
  "buffer_load_dwordx4 v232, s[40:43], s68 offen lds\n\t" \
  "v_mfma_f32_16x16x32_bf16 a[52:55], v[192:195], v[36:39], a[52:55]\n\t" \
  "s_add_u32 m0, s62, 1024\n\t" \
  "v_mfma_f32_16x16x32_bf16 a[88:91], v[192:195], v[40:43], a[88:91]\n\t" \
  "buffer_load_dwordx4 v233, s[40:43], s68 offen lds\n\t" \
  "v_mfma_f32_16x16x32_bf16 a[124:127], v[192:195], v[44:47], a[124:127]\n\t" \
  "v_mfma_f32_16x16x32_bf16 a[160:163], v[192:195], v[48:51], a[160:163]\n\t" \
  "ds_read_b64_tr_b16 v[192:193], v209 offset:256\n\t" \
  "ds_read_b64_tr_b16 v[194:195], v209 offset:4864\n\t" \
  "s_waitcnt lgkmcnt(6)\n\t" \
  "v_mfma_f32_16x16x32_bf16 a[20:23], v[196:199], v[32:35], a[20:23]\n\t" \
  "s_add_u32 m0, s62, 2048\n\t" \
  "s_nop 0\n\t" \
  "buffer_load_dwordx4 v234, s[40:43], s68 offen lds\n\t" \
  "v_mfma_f32_16x16x32_bf16 a[56:59], v[196:199], v[36:39], a[56:59]\n\t" \
  "v_mfma_f32_16x16x32_bf16 a[92:95], v[196:199], v[40:43], a[92:95]\n\t" \
  "s_add_u32 m0, s62, 3072\n\t" \
  "v_mfma_f32_16x16x32_bf16 a[128:131], v[196:199], v[44:47], a[128:131]\n\t" \
  "ds_read_b64_tr_b16 v[196:197], v208 offset:9216\n\t" \
  "ds_read_b64_tr_b16 v[198:199], v208 offset:13824\n\t" \
  "s_waitcnt lgkmcnt(6)\n\t" \
  "v_mfma_f32_16x16x32_bf16 a[24:27], v[200:203], v[32:35], a[24:27]\n\t" \
  "buffer_load_dwordx4 v235, s[40:43], s68 offen lds\n\t" \
  "v_mfma_f32_16x16x32_bf16 a[60:63], v[200:203], v[36:39], a[60:63]\n\t" \
  "s_add_u32 m0, s62, 4096\n\t" \
  "v_mfma_f32_16x16x32_bf16 a[96:99], v[200:203], v[40:43], a[96:99]\n\t" \
  "buffer_load_dwordx4 v236, s[40:43], s68 offen lds\n\t" \
  "v_mfma_f32_16x16x32_bf16 a[132:135], v[200:203], v[44:47], a[132:135]\n\t" \
  "ds_read_b64_tr_b16 v[200:201], v208 offset:9248\n\t" \
  "ds_read_b64_tr_b16 v[202:203], v208 offset:13856\n\t" \
  "s_waitcnt lgkmcnt(4)\n\t" \
  "v_mfma_f32_16x16x32_bf16 a[28:31], v[204:207], v[32:35], a[28:31]\n\t" \
  "s_add_u32 m0, s62, 5120\n\t" \
  "v_mfma_f32_16x16x32_bf16 a[64:67], v[204:207], v[36:39], a[64:67]\n\t" \
  "buffer_load_dwordx4 v237, s[40:43], s68 offen lds\n\t" \
  "v_mfma_f32_16x16x32_bf16 a[100:103], v[204:207], v[40:43], a[100:103]\n\t" \
  "v_mfma_f32_16x16x32_bf16 a[136:139], v[204:207], v[44:47], a[136:139]\n\t" \
  "ds_read_b64_tr_b16 v[204:205], v208 offset:9280\n\t" \
  "ds_read_b64_tr_b16 v[206:207], v208 offset:13888\n\t" \
  "v_mfma_f32_16x16x32_bf16 a[32:35], v[192:195], v[32:35], a[32:35]\n\t" \
  "s_add_u32 m0, s62, 6144\n\t" \
  "s_nop 0\n\t" \
  "buffer_load_dwordx4 v238, s[40:43], s68 offen lds\n\t" \
  "v_mfma_f32_16x16x32_bf16 a[68:71], v[192:195], v[36:39], a[68:71]\n\t" \
  "v_mfma_f32_16x16x32_bf16 a[104:107], v[192:195], v[40:43], a[104:107]\n\t" \
  "s_add_u32 m0, s62, 7168\n\t" \
  "v_mfma_f32_16x16x32_bf16 a[140:143], v[192:195], v[44:47], a[140:143]\n\t" \
  "ds_read_b64_tr_b16 v[192:193], v208 offset:9312\n\t" \
  "ds_read_b64_tr_b16 v[194:195], v208 offset:13920\n\t" \
  "s_waitcnt vmcnt(17)\n\t" \
  "s_waitcnt lgkmcnt(6)\n\t" \
  "v_mfma_f32_16x16x32_bf16 a[0:3], v[196:199], v[52:55], a[0:3]\n\t" \
  "buffer_load_dwordx4 v239, s[40:43], s68 offen lds\n\t" \
  "v_mfma_f32_16x16x32_bf16 a[36:39], v[196:199], v[56:59], a[36:39]\n\t" \
  "s_add_u32 m0, s62, 8192\n\t" \
  "v_mfma_f32_16x16x32_bf16 a[72:75], v[196:199], v[60:63], a[72:75]\n\t" \
  "buffer_load_dwordx4 v240, s[40:43], s68 offen lds\n\t" \
  "v_mfma_f32_16x16x32_bf16 a[108:111], v[196:199], v[64:67], a[108:111]\n\t" \
  "v_mfma_f32_16x16x32_bf16 a[144:147], v[196:199], v[68:71], a[144:147]\n\t" \
  "ds_read_b64_tr_b16 v[196:197], v208 offset:9344\n\t" \
  "ds_read_b64_tr_b16 v[198:199], v208 offset:13952\n\t" \
  "s_waitcnt lgkmcnt(6)\n\t" \
  "v_mfma_f32_16x16x32_bf16 a[4:7], v[200:203], v[52:55], a[4:7]\n\t" \
  "s_add_u32 s61, s68, 36864\n\t" \
  "s_add_u32 m0, s62, 9216\n\t" \
  "v_mfma_f32_16x16x32_bf16 a[40:43], v[200:203], v[56:59], a[40:43]\n\t" \
  "v_mfma_f32_16x16x32_bf16 a[76:79], v[200:203], v[60:63], a[76:79]\n\t" \
  "buffer_load_dwordx4 v232, s[40:43], s61 offen lds\n\t" \
  "v_mfma_f32_16x16x32_bf16 a[112:115], v[200:203], v[64:67], a[112:115]\n\t" \
  "s_add_u32 m0, s62, 10240\n\t" \
  "v_mfma_f32_16x16x32_bf16 a[148:151], v[200:203], v[68:71], a[148:151]\n\t" \
  "ds_read_b64_tr_b16 v[200:201], v209 offset:9376\n\t" \
  "ds_read_b64_tr_b16 v[202:203], v209 offset:13984\n\t" \
  "s_waitcnt lgkmcnt(6)\n\t" \
  "v_mfma_f32_16x16x32_bf16 a[8:11], v[204:207], v[52:55], a[8:11]\n\t" \
  "buffer_load_dwordx4 v233, s[40:43], s61 offen lds\n\t" \
  "v_mfma_f32_16x16x32_bf16 a[44:47], v[204:207], v[56:59], a[44:47]\n\t" \
  "s_add_u32 m0, s62, 11264\n\t" \
  "v_mfma_f32_16x16x32_bf16 a[80:83], v[204:207], v[60:63], a[80:83]\n\t" \
  "v_mfma_f32_16x16x32_bf16 a[116:119], v[204:207], v[64:67], a[116:119]\n\t" \
  "buffer_load_dwordx4 v234, s[40:43], s61 offen lds\n\t" \
  "v_mfma_f32_16x16x32_bf16 a[152:155], v[204:207], v[68:71], a[152:155]\n\t" \
  "ds_read_b64_tr_b16 v[204:205], v209 offset:9408\n\t" \
  "ds_read_b64_tr_b16 v[206:207], v209 offset:14016\n\t" \
  "s_waitcnt lgkmcnt(6)\n\t" \
  "v_mfma_f32_16x16x32_bf16 a[12:15], v[192:195], v[52:55], a[12:15]\n\t" \
  "s_add_u32 m0, s62, 12288\n\t" \
  "v_mfma_f32_16x16x32_bf16 a[48:51], v[192:195], v[56:59], a[48:51]\n\t" \
  "buffer_load_dwordx4 v235, s[40:43], s61 offen lds\n\t" \
  "v_mfma_f32_16x16x32_bf16 a[84:87], v[192:195], v[60:63], a[84:87]\n\t" \
  "s_add_u32 m0, s62, 13312\n\t" \
  "v_mfma_f32_16x16x32_bf16 a[120:123], v[192:195], v[64:67], a[120:123]\n\t" \
  "v_mfma_f32_16x16x32_bf16 a[156:159], v[192:195], v[68:71], a[156:159]\n\t" \
  "ds_read_b64_tr_b16 v[192:193], v209 offset:9440\n\t" \
  "ds_read_b64_tr_b16 v[194:195], v209 offset:14048\n\t" \
  "s_waitcnt lgkmcnt(6)\n\t" \
  "v_mfma_f32_16x16x32_bf16 a[16:19], v[196:199], v[52:55], a[16:19]\n\t" \
  "buffer_load_dwordx4 v236, s[40:43], s61 offen lds\n\t" \
  "s_add_u32 m0, s62, 14336\n\t" \
  "v_mfma_f32_16x16x32_bf16 a[52:55], v[196:199], v[56:59], a[52:55]\n\t" \
  "v_mfma_f32_16x16x32_bf16 a[88:91], v[196:199], v[60:63], a[88:91]\n\t" \
  "buffer_load_dwordx4 v237, s[40:43], s61 offen lds\n\t" \
  "v_mfma_f32_16x16x32_bf16 a[124:127], v[196:199], v[64:67], a[124:127]\n\t" \
  "s_add_u32 m0, s62, 15360\n\t" \
  "v_mfma_f32_16x16x32_bf16 a[160:163], v[196:199], v[68:71], a[160:163]\n\t" \
  "ds_read_b64_tr_b16 v[196:197], v209 offset:9472\n\t" \
  "ds_read_b64_tr_b16 v[198:199], v209 offset:14080\n\t" \
  "s_waitcnt lgkmcnt(4)\n\t" \
  "v_mfma_f32_16x16x32_bf16 a[20:23], v[200:203], v[52:55], a[20:23]\n\t" \
  "buffer_load_dwordx4 v238, s[40:43], s61 offen lds\n\t" \
  "v_mfma_f32_16x16x32_bf16 a[56:59], v[200:203], v[56:59], a[56:59]\n\t" \
  "s_add_u32 m0, s62, 16384\n\t" \
  "v_mfma_f32_16x16x32_bf16 a[92:95], v[200:203], v[60:63], a[92:95]\n\t" \
  "v_mfma_f32_16x16x32_bf16 a[128:131], v[200:203], v[64:67], a[128:131]\n\t" \
  "ds_read_b64_tr_b16 v[200:201], v208 offset:18432\n\t" \
  "ds_read_b64_tr_b16 v[202:203], v208 offset:23040\n\t" \
  "v_mfma_f32_16x16x32_bf16 a[24:27], v[204:207], v[52:55], a[24:27]\n\t" \
  "buffer_load_dwordx4 v239, s[40:43], s61 offen lds\n\t" \
  "s_add_u32 m0, s62, 17408\n\t" \
  "v_mfma_f32_16x16x32_bf16 a[60:63], v[204:207], v[56:59], a[60:63]\n\t" \
  "v_mfma_f32_16x16x32_bf16 a[96:99], v[204:207], v[60:63], a[96:99]\n\t" \
  "buffer_load_dwordx4 v240, s[40:43], s61 offen lds\n\t" \
  "v_mfma_f32_16x16x32_bf16 a[132:135], v[204:207], v[64:67], a[132:135]\n\t" \
  "ds_read_b64_tr_b16 v[204:205], v208 offset:18464\n\t" \
  "ds_read_b64_tr_b16 v[206:207], v208 offset:23072\n\t" \
  "s_waitcnt lgkmcnt(5)\n\t" \
  "v_mfma_f32_16x16x32_bf16 a[28:31], v[192:195], v[52:55], a[28:31]\n\t" \
  "v_mfma_f32_16x16x32_bf16 a[64:67], v[192:195], v[56:59], a[64:67]\n\t" \
  "s_add_u32 s80, s83, s69\n\t" \
  "v_mfma_f32_16x16x32_bf16 a[100:103], v[192:195], v[60:63], a[100:103]\n\t" \
  "v_mfma_f32_16x16x32_bf16 a[136:139], v[192:195], v[64:67], a[136:139]\n\t" \
  "ds_read_b64_tr_b16 v[192:193], v208 offset:18496\n\t" \
  "ds_read_b64_tr_b16 v[194:195], v208 offset:23104\n\t" \
  "s_waitcnt lgkmcnt(6)\n\t" \
  "v_mfma_f32_16x16x32_bf16 a[32:35], v[196:199], v[52:55], a[32:35]\n\t" \
  "s_add_u32 s81, s83, s69\n\t" \
  "v_mfma_f32_16x16x32_bf16 a[68:71], v[196:199], v[56:59], a[68:71]\n\t" \
  "v_mfma_f32_16x16x32_bf16 a[104:107], v[196:199], v[60:63], a[104:107]\n\t" \
  "s_add_u32 s81, s81, 131072\n\t" \
  "v_mfma_f32_16x16x32_bf16 a[140:143], v[196:199], v[64:67], a[140:143]\n\t" \
  "ds_read_b64_tr_b16 v[196:197], v208 offset:18528\n\t" \
  "ds_read_b64_tr_b16 v[198:199], v208 offset:23136\n\t" \
  "s_waitcnt vmcnt(23)\n\t" \
  "s_waitcnt lgkmcnt(6)\n\t" \
  "v_mfma_f32_16x16x32_bf16 a[0:3], v[200:203], v[72:75], a[0:3]\n\t" \
  "s_add_u32 s82, s83, s69\n\t" \
  "v_mfma_f32_16x16x32_bf16 a[36:39], v[200:203], v[76:79], a[36:39]\n\t" \
  "v_mfma_f32_16x16x32_bf16 a[72:75], v[200:203], v[80:83], a[72:75]\n\t" \
  "v_mfma_f32_16x16x32_bf16 a[108:111], v[200:203], v[84:87], a[108:111]\n\t" \
  "s_add_u32 s82, s82, 262144\n\t" \
  "v_mfma_f32_16x16x32_bf16 a[144:147], v[200:203], v[88:91], a[144:147]\n\t" \
  "ds_read_b64_tr_b16 v[200:201], v208 offset:18560\n\t" \
  "ds_read_b64_tr_b16 v[202:203], v208 offset:23168\n\t" \
  "s_waitcnt lgkmcnt(6)\n\t" \
  "v_mfma_f32_16x16x32_bf16 a[4:7], v[204:207], v[72:75], a[4:7]\n\t" \
  "s_add_u32 s87, s83, s69\n\t" \
  "v_mfma_f32_16x16x32_bf16 a[40:43], v[204:207], v[76:79], a[40:43]\n\t" \
  "v_mfma_f32_16x16x32_bf16 a[76:79], v[204:207], v[80:83], a[76:79]\n\t" \
  "v_mfma_f32_16x16x32_bf16 a[112:115], v[204:207], v[84:87], a[112:115]\n\t" \
  "s_add_u32 s87, s87, 393216\n\t" \
  "v_mfma_f32_16x16x32_bf16 a[148:151], v[204:207], v[88:91], a[148:151]\n\t" \
  "ds_read_b64_tr_b16 v[204:205], v209 offset:18592\n\t" \
  "ds_read_b64_tr_b16 v[206:207], v209 offset:23200\n\t" \
  "s_waitcnt lgkmcnt(6)\n\t" \
  "v_mfma_f32_16x16x32_bf16 a[8:11], v[192:195], v[72:75], a[8:11]\n\t" \
  "s_add_u32 s95, s75, s69\n\t" \
  "v_mfma_f32_16x16x32_bf16 a[44:47], v[192:195], v[76:79], a[44:47]\n\t" \
  "v_mfma_f32_16x16x32_bf16 a[80:83], v[192:195], v[80:83], a[80:83]\n\t" \
  "v_mfma_f32_16x16x32_bf16 a[116:119], v[192:195], v[84:87], a[116:119]\n\t" \
  "buffer_load_dwordx4 v[112:115], v244, s[56:59], s80 offen sc1\n\t" \
  "v_mfma_f32_16x16x32_bf16 a[152:155], v[192:195], v[88:91], a[152:155]\n\t" \
  "ds_read_b64_tr_b16 v[192:193], v209 offset:18624\n\t" \
  "ds_read_b64_tr_b16 v[194:195], v209 offset:23232\n\t" \
  "s_waitcnt lgkmcnt(6)\n\t" \
  "v_mfma_f32_16x16x32_bf16 a[12:15], v[196:199], v[72:75], a[12:15]\n\t" \
  "buffer_load_dwordx4 v[116:119], v244, s[56:59], s81 offen sc1\n\t" \
  "v_mfma_f32_16x16x32_bf16 a[48:51], v[196:199], v[76:79], a[48:51]\n\t" \
  "v_mfma_f32_16x16x32_bf16 a[84:87], v[196:199], v[80:83], a[84:87]\n\t" \
  "v_mfma_f32_16x16x32_bf16 a[120:123], v[196:199], v[84:87], a[120:123]\n\t" \
  "buffer_load_dwordx4 v[120:123], v244, s[56:59], s82 offen sc1\n\t" \
  "v_mfma_f32_16x16x32_bf16 a[156:159], v[196:199], v[88:91], a[156:159]\n\t" \
  "ds_read_b64_tr_b16 v[196:197], v209 offset:18656\n\t" \
  "ds_read_b64_tr_b16 v[198:199], v209 offset:23264\n\t" \
  "s_waitcnt lgkmcnt(6)\n\t" \
  "v_mfma_f32_16x16x32_bf16 a[16:19], v[200:203], v[72:75], a[16:19]\n\t" \
  "buffer_load_dwordx4 v[124:127], v244, s[56:59], s87 offen sc1\n\t" \
  "v_mfma_f32_16x16x32_bf16 a[52:55], v[200:203], v[76:79], a[52:55]\n\t" \
  "v_mfma_f32_16x16x32_bf16 a[88:91], v[200:203], v[80:83], a[88:91]\n\t" \
  "v_mfma_f32_16x16x32_bf16 a[124:127], v[200:203], v[84:87], a[124:127]\n\t" \
  "buffer_load_dwordx4 v[128:131], v244, s[56:59], s95 offen sc1\n\t" \
  "v_mfma_f32_16x16x32_bf16 a[160:163], v[200:203], v[88:91], a[160:163]\n\t" \
  "ds_read_b64_tr_b16 v[200:201], v209 offset:18688\n\t" \
  "ds_read_b64_tr_b16 v[202:203], v209 offset:23296\n\t" \
  "s_waitcnt lgkmcnt(4)\n\t" \
  "v_mfma_f32_16x16x32_bf16 a[20:23], v[204:207], v[72:75], a[20:23]\n\t" \
  "buffer_load_dwordx4 v[132:135], v244, s[56:59], s80 offen offset:1024 sc1\n\t" \
  "v_mfma_f32_16x16x32_bf16 a[56:59], v[204:207], v[76:79], a[56:59]\n\t" \
  "v_mfma_f32_16x16x32_bf16 a[92:95], v[204:207], v[80:83], a[92:95]\n\t" \
  "v_mfma_f32_16x16x32_bf16 a[128:131], v[204:207], v[84:87], a[128:131]\n\t" \
  "ds_read_b64_tr_b16 v[204:205], v208 offset:27648\n\t" \
  "ds_read_b64_tr_b16 v[206:207], v208 offset:32256\n\t" \
  "v_mfma_f32_16x16x32_bf16 a[24:27], v[192:195], v[72:75], a[24:27]\n\t" \
  "buffer_load_dwordx4 v[136:139], v244, s[56:59], s81 offen offset:1024 sc1\n\t" \
  "v_mfma_f32_16x16x32_bf16 a[60:63], v[192:195], v[76:79], a[60:63]\n\t" \
  "buffer_load_dwordx4 v[140:143], v244, s[56:59], s82 offen offset:1024 sc1\n\t" \
  "v_mfma_f32_16x16x32_bf16 a[96:99], v[192:195], v[80:83], a[96:99]\n\t" \
  "v_mfma_f32_16x16x32_bf16 a[132:135], v[192:195], v[84:87], a[132:135]\n\t" \
  "ds_read_b64_tr_b16 v[192:193], v208 offset:27680\n\t" \
  "ds_read_b64_tr_b16 v[194:195], v208 offset:32288\n\t" \
  "s_waitcnt lgkmcnt(6)\n\t" \
  "v_mfma_f32_16x16x32_bf16 a[28:31], v[196:199], v[72:75], a[28:31]\n\t" \
  "buffer_load_dwordx4 v[144:147], v244, s[56:59], s87 offen offset:1024 sc1\n\t" \
  "v_mfma_f32_16x16x32_bf16 a[64:67], v[196:199], v[76:79], a[64:67]\n\t" \
  "v_mfma_f32_16x16x32_bf16 a[100:103], v[196:199], v[80:83], a[100:103]\n\t" \
  "buffer_load_dwordx4 v[148:151], v244, s[56:59], s95 offen offset:1024 sc1\n\t" \
  "v_mfma_f32_16x16x32_bf16 a[136:139], v[196:199], v[84:87], a[136:139]\n\t" \
  "ds_read_b64_tr_b16 v[196:197], v208 offset:27712\n\t" \
  "ds_read_b64_tr_b16 v[198:199], v208 offset:32320\n\t" \
  "s_waitcnt lgkmcnt(6)\n\t" \
  "v_mfma_f32_16x16x32_bf16 a[32:35], v[200:203], v[72:75], a[32:35]\n\t" \
  "v_mfma_f32_16x16x32_bf16 a[68:71], v[200:203], v[76:79], a[68:71]\n\t" \
  "buffer_load_dwordx4 v[152:155], v244, s[56:59], s80 offen offset:2048 sc1\n\t" \
  "v_mfma_f32_16x16x32_bf16 a[104:107], v[200:203], v[80:83], a[104:107]\n\t" \
  "v_mfma_f32_16x16x32_bf16 a[140:143], v[200:203], v[84:87], a[140:143]\n\t" \
  "ds_read_b64_tr_b16 v[200:201], v208 offset:27744\n\t" \
  "ds_read_b64_tr_b16 v[202:203], v208 offset:32352\n\t" \
  "s_waitcnt vmcnt(29)\n\t" \
  "s_waitcnt lgkmcnt(6)\n\t" \
  "v_mfma_f32_16x16x32_bf16 a[0:3], v[204:207], v[92:95], a[0:3]\n\t" \
  "buffer_load_dwordx4 v[156:159], v244, s[56:59], s81 offen offset:2048 sc1\n\t" \
  "v_mfma_f32_16x16x32_bf16 a[36:39], v[204:207], v[96:99], a[36:39]\n\t" \
  "v_mfma_f32_16x16x32_bf16 a[72:75], v[204:207], v[100:103], a[72:75]\n\t" \
  "buffer_load_dwordx4 v[160:163], v244, s[56:59], s82 offen offset:2048 sc1\n\t" \
  "v_mfma_f32_16x16x32_bf16 a[108:111], v[204:207], v[104:107], a[108:111]\n\t" \
  "v_mfma_f32_16x16x32_bf16 a[144:147], v[204:207], v[108:111], a[144:147]\n\t" \
  "ds_read_b64_tr_b16 v[204:205], v208 offset:27776\n\t" \
  "ds_read_b64_tr_b16 v[206:207], v208 offset:32384\n\t" \
  "s_waitcnt lgkmcnt(6)\n\t" \
  "v_mfma_f32_16x16x32_bf16 a[4:7], v[192:195], v[92:95], a[4:7]\n\t" \
  "buffer_load_dwordx4 v[164:167], v244, s[56:59], s87 offen offset:2048 sc1\n\t" \
  "v_mfma_f32_16x16x32_bf16 a[40:43], v[192:195], v[96:99], a[40:43]\n\t" \
  "v_mfma_f32_16x16x32_bf16 a[76:79], v[192:195], v[100:103], a[76:79]\n\t" \
  "buffer_load_dwordx4 v[168:171], v244, s[56:59], s95 offen offset:2048 sc1\n\t" \
  "v_mfma_f32_16x16x32_bf16 a[112:115], v[192:195], v[104:107], a[112:115]\n\t" \
  "v_mfma_f32_16x16x32_bf16 a[148:151], v[192:195], v[108:111], a[148:151]\n\t" \
  "ds_read_b64_tr_b16 v[192:193], v209 offset:27808\n\t" \
  "ds_read_b64_tr_b16 v[194:195], v209 offset:32416\n\t" \
  "s_waitcnt lgkmcnt(6)\n\t" \
  "v_mfma_f32_16x16x32_bf16 a[8:11], v[196:199], v[92:95], a[8:11]\n\t" \
  "buffer_load_dwordx4 v[172:175], v244, s[56:59], s80 offen offset:3072 sc1\n\t" \
  "v_mfma_f32_16x16x32_bf16 a[44:47], v[196:199], v[96:99], a[44:47]\n\t" \
  "v_mfma_f32_16x16x32_bf16 a[80:83], v[196:199], v[100:103], a[80:83]\n\t" \
  "buffer_load_dwordx4 v[176:179], v244, s[56:59], s81 offen offset:3072 sc1\n\t" \
  "v_mfma_f32_16x16x32_bf16 a[116:119], v[196:199], v[104:107], a[116:119]\n\t" \
  "v_mfma_f32_16x16x32_bf16 a[152:155], v[196:199], v[108:111], a[152:155]\n\t" \
  "ds_read_b64_tr_b16 v[196:197], v209 offset:27840\n\t" \
  "ds_read_b64_tr_b16 v[198:199], v209 offset:32448\n\t" \
  "s_waitcnt lgkmcnt(6)\n\t" \
  "v_mfma_f32_16x16x32_bf16 a[12:15], v[200:203], v[92:95], a[12:15]\n\t" \
  "buffer_load_dwordx4 v[180:183], v244, s[56:59], s82 offen offset:3072 sc1\n\t" \
  "v_mfma_f32_16x16x32_bf16 a[48:51], v[200:203], v[96:99], a[48:51]\n\t" \
  "v_mfma_f32_16x16x32_bf16 a[84:87], v[200:203], v[100:103], a[84:87]\n\t" \
  "buffer_load_dwordx4 v[184:187], v244, s[56:59], s87 offen offset:3072 sc1\n\t" \
  "v_mfma_f32_16x16x32_bf16 a[120:123], v[200:203], v[104:107], a[120:123]\n\t" \
  "v_mfma_f32_16x16x32_bf16 a[156:159], v[200:203], v[108:111], a[156:159]\n\t" \
  "ds_read_b64_tr_b16 v[200:201], v209 offset:27872\n\t" \
  "ds_read_b64_tr_b16 v[202:203], v209 offset:32480\n\t" \
  "s_waitcnt lgkmcnt(4)\n\t" \
  "v_mfma_f32_16x16x32_bf16 a[16:19], v[204:207], v[92:95], a[16:19]\n\t" \
  "buffer_load_dwordx4 v[188:191], v244, s[56:59], s95 offen offset:3072 sc1\n\t" \
  "v_mfma_f32_16x16x32_bf16 a[52:55], v[204:207], v[96:99], a[52:55]\n\t" \
  "v_mfma_f32_16x16x32_bf16 a[88:91], v[204:207], v[100:103], a[88:91]\n\t" \
  "v_mfma_f32_16x16x32_bf16 a[124:127], v[204:207], v[104:107], a[124:127]\n\t" \
  "v_mfma_f32_16x16x32_bf16 a[160:163], v[204:207], v[108:111], a[160:163]\n\t" \
  "ds_read_b64_tr_b16 v[204:205], v209 offset:27904\n\t" \
  "ds_read_b64_tr_b16 v[206:207], v209 offset:32512\n\t" \
  "v_mfma_f32_16x16x32_bf16 a[20:23], v[192:195], v[92:95], a[20:23]\n\t" \
  "v_mfma_f32_16x16x32_bf16 a[56:59], v[192:195], v[96:99], a[56:59]\n\t" \
  "v_mfma_f32_16x16x32_bf16 a[92:95], v[192:195], v[100:103], a[92:95]\n\t" \
  "v_mfma_f32_16x16x32_bf16 a[128:131], v[192:195], v[104:107], a[128:131]\n\t" \
  "s_waitcnt lgkmcnt(4)\n\t" \
  "v_mfma_f32_16x16x32_bf16 a[24:27], v[196:199], v[92:95], a[24:27]\n\t" \
  "v_mfma_f32_16x16x32_bf16 a[60:63], v[196:199], v[96:99], a[60:63]\n\t" \
  "v_mfma_f32_16x16x32_bf16 a[96:99], v[196:199], v[100:103], a[96:99]\n\t" \
  "v_mfma_f32_16x16x32_bf16 a[132:135], v[196:199], v[104:107], a[132:135]\n\t" \
  "s_waitcnt lgkmcnt(2)\n\t" \
  "v_mfma_f32_16x16x32_bf16 a[28:31], v[200:203], v[92:95], a[28:31]\n\t" \
  "v_mfma_f32_16x16x32_bf16 a[64:67], v[200:203], v[96:99], a[64:67]\n\t" \
  "v_mfma_f32_16x16x32_bf16 a[100:103], v[200:203], v[100:103], a[100:103]\n\t" \
  "v_mfma_f32_16x16x32_bf16 a[136:139], v[200:203], v[104:107], a[136:139]\n\t" \
  "s_waitcnt lgkmcnt(0)\n\t" \
  "v_mfma_f32_16x16x32_bf16 a[32:35], v[204:207], v[92:95], a[32:35]\n\t" \
  "v_mfma_f32_16x16x32_bf16 a[68:71], v[204:207], v[96:99], a[68:71]\n\t" \
  "v_mfma_f32_16x16x32_bf16 a[104:107], v[204:207], v[100:103], a[104:107]\n\t" \
  "v_mfma_f32_16x16x32_bf16 a[140:143], v[204:207], v[104:107], a[140:143]\n\t" \
  "s_waitcnt vmcnt(20)\n\t" \
  "s_barrier\n\t" \
  "v_add_u32 v208, 73728, v210\n\t" \
  "v_add_u32 v209, 73728, v211\n\t" \
  "ds_read_b64_tr_b16 v[192:193], v208 offset:0\n\t" \
  "ds_read_b64_tr_b16 v[194:195], v208 offset:4608\n\t" \
  "ds_read_b64_tr_b16 v[196:197], v208 offset:32\n\t" \
  "ds_read_b64_tr_b16 v[198:199], v208 offset:4640\n\t" \
  "ds_read_b64_tr_b16 v[200:201], v208 offset:64\n\t" \
  "ds_read_b64_tr_b16 v[202:203], v208 offset:4672\n\t" \
  "ds_read_b64_tr_b16 v[204:205], v208 offset:96\n\t" \
  "ds_read_b64_tr_b16 v[206:207], v208 offset:4704\n\t" \
  "s_waitcnt vmcnt(15)\n\t" \
  "s_waitcnt lgkmcnt(6)\n\t" \
  "v_mfma_f32_16x16x32_bf16 a[0:3], v[192:195], v[112:115], a[0:3]\n\t" \
  "v_mfma_f32_16x16x32_bf16 a[36:39], v[192:195], v[116:119], a[36:39]\n\t" \
  "v_mfma_f32_16x16x32_bf16 a[72:75], v[192:195], v[120:123], a[72:75]\n\t" \
  "v_mfma_f32_16x16x32_bf16 a[108:111], v[192:195], v[124:127], a[108:111]\n\t" \
  "v_mfma_f32_16x16x32_bf16 a[144:147], v[192:195], v[128:131], a[144:147]\n\t" \
  "ds_read_b64_tr_b16 v[192:193], v208 offset:128\n\t" \
  "ds_read_b64_tr_b16 v[194:195], v208 offset:4736\n\t" \
  "s_waitcnt lgkmcnt(6)\n\t" \
  "v_mfma_f32_16x16x32_bf16 a[4:7], v[196:199], v[112:115], a[4:7]\n\t" \
  "v_mfma_f32_16x16x32_bf16 a[40:43], v[196:199], v[116:119], a[40:43]\n\t" \
  "v_mfma_f32_16x16x32_bf16 a[76:79], v[196:199], v[120:123], a[76:79]\n\t" \
  "v_mfma_f32_16x16x32_bf16 a[112:115], v[196:199], v[124:127], a[112:115]\n\t" \
  "v_mfma_f32_16x16x32_bf16 a[148:151], v[196:199], v[128:131], a[148:151]\n\t" \
  "ds_read_b64_tr_b16 v[196:197], v209 offset:160\n\t" \
  "ds_read_b64_tr_b16 v[198:199], v209 offset:4768\n\t" \
  "s_waitcnt lgkmcnt(6)\n\t" \
  "v_mfma_f32_16x16x32_bf16 a[8:11], v[200:203], v[112:115], a[8:11]\n\t" \
  "v_mfma_f32_16x16x32_bf16 a[44:47], v[200:203], v[116:119], a[44:47]\n\t" \
  "v_mfma_f32_16x16x32_bf16 a[80:83], v[200:203], v[120:123], a[80:83]\n\t" \
  "v_mfma_f32_16x16x32_bf16 a[116:119], v[200:203], v[124:127], a[116:119]\n\t" \
  "v_mfma_f32_16x16x32_bf16 a[152:155], v[200:203], v[128:131], a[152:155]\n\t" \
  "ds_read_b64_tr_b16 v[200:201], v209 offset:192\n\t" \
  "ds_read_b64_tr_b16 v[202:203], v209 offset:4800\n\t" \
  "s_waitcnt lgkmcnt(6)\n\t" \
  "v_mfma_f32_16x16x32_bf16 a[12:15], v[204:207], v[112:115], a[12:15]\n\t" \
  "v_mfma_f32_16x16x32_bf16 a[48:51], v[204:207], v[116:119], a[48:51]\n\t" \
  "v_mfma_f32_16x16x32_bf16 a[84:87], v[204:207], v[120:123], a[84:87]\n\t" \
  "v_mfma_f32_16x16x32_bf16 a[120:123], v[204:207], v[124:127], a[120:123]\n\t" \
  "v_mfma_f32_16x16x32_bf16 a[156:159], v[204:207], v[128:131], a[156:159]\n\t" \
  "ds_read_b64_tr_b16 v[204:205], v209 offset:224\n\t" \
  "ds_read_b64_tr_b16 v[206:207], v209 offset:4832\n\t" \
  "s_waitcnt lgkmcnt(6)\n\t" \
  "v_mfma_f32_16x16x32_bf16 a[16:19], v[192:195], v[112:115], a[16:19]\n\t" \
  "v_mfma_f32_16x16x32_bf16 a[52:55], v[192:195], v[116:119], a[52:55]\n\t" \
  "v_mfma_f32_16x16x32_bf16 a[88:91], v[192:195], v[120:123], a[88:91]\n\t" \
  "v_mfma_f32_16x16x32_bf16 a[124:127], v[192:195], v[124:127], a[124:127]\n\t" \
  "v_mfma_f32_16x16x32_bf16 a[160:163], v[192:195], v[128:131], a[160:163]\n\t" \
  "ds_read_b64_tr_b16 v[192:193], v209 offset:256\n\t" \
  "ds_read_b64_tr_b16 v[194:195], v209 offset:4864\n\t" \
  "s_waitcnt lgkmcnt(6)\n\t" \
  "v_mfma_f32_16x16x32_bf16 a[20:23], v[196:199], v[112:115], a[20:23]\n\t" \
  "v_mfma_f32_16x16x32_bf16 a[56:59], v[196:199], v[116:119], a[56:59]\n\t" \
  "v_mfma_f32_16x16x32_bf16 a[92:95], v[196:199], v[120:123], a[92:95]\n\t" \
  "v_mfma_f32_16x16x32_bf16 a[128:131], v[196:199], v[124:127], a[128:131]\n\t" \
  "ds_read_b64_tr_b16 v[196:197], v208 offset:9216\n\t" \
  "ds_read_b64_tr_b16 v[198:199], v208 offset:13824\n\t" \
  "s_waitcnt lgkmcnt(6)\n\t" \
  "v_mfma_f32_16x16x32_bf16 a[24:27], v[200:203], v[112:115], a[24:27]\n\t" \
  "v_mfma_f32_16x16x32_bf16 a[60:63], v[200:203], v[116:119], a[60:63]\n\t" \
  "v_mfma_f32_16x16x32_bf16 a[96:99], v[200:203], v[120:123], a[96:99]\n\t" \
  "v_mfma_f32_16x16x32_bf16 a[132:135], v[200:203], v[124:127], a[132:135]\n\t" \
  "ds_read_b64_tr_b16 v[200:201], v208 offset:9248\n\t" \
  "ds_read_b64_tr_b16 v[202:203], v208 offset:13856\n\t" \
  "s_waitcnt lgkmcnt(6)\n\t" \
  "v_mfma_f32_16x16x32_bf16 a[28:31], v[204:207], v[112:115], a[28:31]\n\t" \
  "v_mfma_f32_16x16x32_bf16 a[64:67], v[204:207], v[116:119], a[64:67]\n\t" \
  "v_mfma_f32_16x16x32_bf16 a[100:103], v[204:207], v[120:123], a[100:103]\n\t" \
  "v_mfma_f32_16x16x32_bf16 a[136:139], v[204:207], v[124:127], a[136:139]\n\t" \
  "ds_read_b64_tr_b16 v[204:205], v208 offset:9280\n\t" \
  "ds_read_b64_tr_b16 v[206:207], v208 offset:13888\n\t" \
  "s_waitcnt lgkmcnt(6)\n\t" \
  "v_mfma_f32_16x16x32_bf16 a[32:35], v[192:195], v[112:115], a[32:35]\n\t" \
  "v_mfma_f32_16x16x32_bf16 a[68:71], v[192:195], v[116:119], a[68:71]\n\t" \
  "v_mfma_f32_16x16x32_bf16 a[104:107], v[192:195], v[120:123], a[104:107]\n\t" \
  "v_mfma_f32_16x16x32_bf16 a[140:143], v[192:195], v[124:127], a[140:143]\n\t" \
  "ds_read_b64_tr_b16 v[192:193], v208 offset:9312\n\t" \
  "ds_read_b64_tr_b16 v[194:195], v208 offset:13920\n\t" \
  "s_waitcnt vmcnt(10)\n\t" \
  "s_waitcnt lgkmcnt(6)\n\t" \
  "v_mfma_f32_16x16x32_bf16 a[0:3], v[196:199], v[132:135], a[0:3]\n\t" \
  "v_mfma_f32_16x16x32_bf16 a[36:39], v[196:199], v[136:139], a[36:39]\n\t" \
  "v_mfma_f32_16x16x32_bf16 a[72:75], v[196:199], v[140:143], a[72:75]\n\t" \
  "v_mfma_f32_16x16x32_bf16 a[108:111], v[196:199], v[144:147], a[108:111]\n\t" \
  "v_mfma_f32_16x16x32_bf16 a[144:147], v[196:199], v[148:151], a[144:147]\n\t" \
  "ds_read_b64_tr_b16 v[196:197], v208 offset:9344\n\t" \
  "ds_read_b64_tr_b16 v[198:199], v208 offset:13952\n\t" \
  "s_waitcnt lgkmcnt(6)\n\t" \
  "v_mfma_f32_16x16x32_bf16 a[4:7], v[200:203], v[132:135], a[4:7]\n\t" \
  "v_mfma_f32_16x16x32_bf16 a[40:43], v[200:203], v[136:139], a[40:43]\n\t" \
  "v_mfma_f32_16x16x32_bf16 a[76:79], v[200:203], v[140:143], a[76:79]\n\t" \
  "v_mfma_f32_16x16x32_bf16 a[112:115], v[200:203], v[144:147], a[112:115]\n\t" \
  "v_mfma_f32_16x16x32_bf16 a[148:151], v[200:203], v[148:151], a[148:151]\n\t" \
  "ds_read_b64_tr_b16 v[200:201], v209 offset:9376\n\t" \
  "ds_read_b64_tr_b16 v[202:203], v209 offset:13984\n\t" \
  "s_waitcnt lgkmcnt(6)\n\t" \
  "v_mfma_f32_16x16x32_bf16 a[8:11], v[204:207], v[132:135], a[8:11]\n\t" \
  "v_mfma_f32_16x16x32_bf16 a[44:47], v[204:207], v[136:139], a[44:47]\n\t" \
  "v_mfma_f32_16x16x32_bf16 a[80:83], v[204:207], v[140:143], a[80:83]\n\t" \
  "v_mfma_f32_16x16x32_bf16 a[116:119], v[204:207], v[144:147], a[116:119]\n\t" \
  "v_mfma_f32_16x16x32_bf16 a[152:155], v[204:207], v[148:151], a[152:155]\n\t" \
  "ds_read_b64_tr_b16 v[204:205], v209 offset:9408\n\t" \
  "ds_read_b64_tr_b16 v[206:207], v209 offset:14016\n\t" \
  "s_waitcnt lgkmcnt(6)\n\t" \
  "v_mfma_f32_16x16x32_bf16 a[12:15], v[192:195], v[132:135], a[12:15]\n\t" \
  "v_mfma_f32_16x16x32_bf16 a[48:51], v[192:195], v[136:139], a[48:51]\n\t" \
  "v_mfma_f32_16x16x32_bf16 a[84:87], v[192:195], v[140:143], a[84:87]\n\t" \
  "v_mfma_f32_16x16x32_bf16 a[120:123], v[192:195], v[144:147], a[120:123]\n\t" \
  "v_mfma_f32_16x16x32_bf16 a[156:159], v[192:195], v[148:151], a[156:159]\n\t" \
  "ds_read_b64_tr_b16 v[192:193], v209 offset:9440\n\t" \
  "ds_read_b64_tr_b16 v[194:195], v209 offset:14048\n\t" \
  "s_waitcnt lgkmcnt(6)\n\t" \
  "v_mfma_f32_16x16x32_bf16 a[16:19], v[196:199], v[132:135], a[16:19]\n\t" \
  "v_mfma_f32_16x16x32_bf16 a[52:55], v[196:199], v[136:139], a[52:55]\n\t" \
  "v_mfma_f32_16x16x32_bf16 a[88:91], v[196:199], v[140:143], a[88:91]\n\t" \
  "v_mfma_f32_16x16x32_bf16 a[124:127], v[196:199], v[144:147], a[124:127]\n\t" \
  "v_mfma_f32_16x16x32_bf16 a[160:163], v[196:199], v[148:151], a[160:163]\n\t" \
  "ds_read_b64_tr_b16 v[196:197], v209 offset:9472\n\t" \
  "ds_read_b64_tr_b16 v[198:199], v209 offset:14080\n\t" \
  "s_waitcnt lgkmcnt(6)\n\t" \
  "v_mfma_f32_16x16x32_bf16 a[20:23], v[200:203], v[132:135], a[20:23]\n\t" \
  "v_mfma_f32_16x16x32_bf16 a[56:59], v[200:203], v[136:139], a[56:59]\n\t" \
  "v_mfma_f32_16x16x32_bf16 a[92:95], v[200:203], v[140:143], a[92:95]\n\t" \
  "v_mfma_f32_16x16x32_bf16 a[128:131], v[200:203], v[144:147], a[128:131]\n\t" \
  "ds_read_b64_tr_b16 v[200:201], v208 offset:18432\n\t" \
  "ds_read_b64_tr_b16 v[202:203], v208 offset:23040\n\t" \
  "s_waitcnt lgkmcnt(6)\n\t" \
  "v_mfma_f32_16x16x32_bf16 a[24:27], v[204:207], v[132:135], a[24:27]\n\t" \
  "v_mfma_f32_16x16x32_bf16 a[60:63], v[204:207], v[136:139], a[60:63]\n\t" \
  "v_mfma_f32_16x16x32_bf16 a[96:99], v[204:207], v[140:143], a[96:99]\n\t" \
  "v_mfma_f32_16x16x32_bf16 a[132:135], v[204:207], v[144:147], a[132:135]\n\t" \
  "ds_read_b64_tr_b16 v[204:205], v208 offset:18464\n\t" \
  "ds_read_b64_tr_b16 v[206:207], v208 offset:23072\n\t" \
  "s_waitcnt lgkmcnt(6)\n\t" \
  "v_mfma_f32_16x16x32_bf16 a[28:31], v[192:195], v[132:135], a[28:31]\n\t" \
  "v_mfma_f32_16x16x32_bf16 a[64:67], v[192:195], v[136:139], a[64:67]\n\t" \
  "v_mfma_f32_16x16x32_bf16 a[100:103], v[192:195], v[140:143], a[100:103]\n\t" \
  "v_mfma_f32_16x16x32_bf16 a[136:139], v[192:195], v[144:147], a[136:139]\n\t" \
  "ds_read_b64_tr_b16 v[192:193], v208 offset:18496\n\t" \
  "ds_read_b64_tr_b16 v[194:195], v208 offset:23104\n\t" \
  "s_waitcnt lgkmcnt(6)\n\t" \
  "v_mfma_f32_16x16x32_bf16 a[32:35], v[196:199], v[132:135], a[32:35]\n\t" \
  "v_mfma_f32_16x16x32_bf16 a[68:71], v[196:199], v[136:139], a[68:71]\n\t" \
  "v_mfma_f32_16x16x32_bf16 a[104:107], v[196:199], v[140:143], a[104:107]\n\t" \
  "v_mfma_f32_16x16x32_bf16 a[140:143], v[196:199], v[144:147], a[140:143]\n\t" \
  "ds_read_b64_tr_b16 v[196:197], v208 offset:18528\n\t" \
  "ds_read_b64_tr_b16 v[198:199], v208 offset:23136\n\t" \
  "s_waitcnt vmcnt(5)\n\t" \
  "s_waitcnt lgkmcnt(6)\n\t" \
  "v_mfma_f32_16x16x32_bf16 a[0:3], v[200:203], v[152:155], a[0:3]\n\t" \
  "v_mfma_f32_16x16x32_bf16 a[36:39], v[200:203], v[156:159], a[36:39]\n\t" \
  "v_mfma_f32_16x16x32_bf16 a[72:75], v[200:203], v[160:163], a[72:75]\n\t" \
  "v_mfma_f32_16x16x32_bf16 a[108:111], v[200:203], v[164:167], a[108:111]\n\t" \
  "v_mfma_f32_16x16x32_bf16 a[144:147], v[200:203], v[168:171], a[144:147]\n\t" \
  "ds_read_b64_tr_b16 v[200:201], v208 offset:18560\n\t" \
  "ds_read_b64_tr_b16 v[202:203], v208 offset:23168\n\t" \
  "s_waitcnt lgkmcnt(6)\n\t" \
  "v_mfma_f32_16x16x32_bf16 a[4:7], v[204:207], v[152:155], a[4:7]\n\t" \
  "v_mfma_f32_16x16x32_bf16 a[40:43], v[204:207], v[156:159], a[40:43]\n\t" \
  "v_mfma_f32_16x16x32_bf16 a[76:79], v[204:207], v[160:163], a[76:79]\n\t" \
  "v_mfma_f32_16x16x32_bf16 a[112:115], v[204:207], v[164:167], a[112:115]\n\t" \
  "v_mfma_f32_16x16x32_bf16 a[148:151], v[204:207], v[168:171], a[148:151]\n\t" \
  "ds_read_b64_tr_b16 v[204:205], v209 offset:18592\n\t" \
  "ds_read_b64_tr_b16 v[206:207], v209 offset:23200\n\t" \
  "s_waitcnt lgkmcnt(6)\n\t" \
  "v_mfma_f32_16x16x32_bf16 a[8:11], v[192:195], v[152:155], a[8:11]\n\t" \
  "v_mfma_f32_16x16x32_bf16 a[44:47], v[192:195], v[156:159], a[44:47]\n\t" \
  "v_mfma_f32_16x16x32_bf16 a[80:83], v[192:195], v[160:163], a[80:83]\n\t" \
  "v_mfma_f32_16x16x32_bf16 a[116:119], v[192:195], v[164:167], a[116:119]\n\t" \
  "v_mfma_f32_16x16x32_bf16 a[152:155], v[192:195], v[168:171], a[152:155]\n\t" \
  "ds_read_b64_tr_b16 v[192:193], v209 offset:18624\n\t" \
  "ds_read_b64_tr_b16 v[194:195], v209 offset:23232\n\t" \
  "s_waitcnt lgkmcnt(6)\n\t" \
  "v_mfma_f32_16x16x32_bf16 a[12:15], v[196:199], v[152:155], a[12:15]\n\t" \
  "v_mfma_f32_16x16x32_bf16 a[48:51], v[196:199], v[156:159], a[48:51]\n\t" \
  "v_mfma_f32_16x16x32_bf16 a[84:87], v[196:199], v[160:163], a[84:87]\n\t" \
  "v_mfma_f32_16x16x32_bf16 a[120:123], v[196:199], v[164:167], a[120:123]\n\t" \
  "v_mfma_f32_16x16x32_bf16 a[156:159], v[196:199], v[168:171], a[156:159]\n\t" \
  "ds_read_b64_tr_b16 v[196:197], v209 offset:18656\n\t" \
  "ds_read_b64_tr_b16 v[198:199], v209 offset:23264\n\t" \
  "s_waitcnt lgkmcnt(6)\n\t" \
  "v_mfma_f32_16x16x32_bf16 a[16:19], v[200:203], v[152:155], a[16:19]\n\t" \
  "v_mfma_f32_16x16x32_bf16 a[52:55], v[200:203], v[156:159], a[52:55]\n\t" \
  "v_mfma_f32_16x16x32_bf16 a[88:91], v[200:203], v[160:163], a[88:91]\n\t" \
  "v_mfma_f32_16x16x32_bf16 a[124:127], v[200:203], v[164:167], a[124:127]\n\t" \
  "v_mfma_f32_16x16x32_bf16 a[160:163], v[200:203], v[168:171], a[160:163]\n\t" \
  "ds_read_b64_tr_b16 v[200:201], v209 offset:18688\n\t" \
  "ds_read_b64_tr_b16 v[202:203], v209 offset:23296\n\t" \
  "s_waitcnt lgkmcnt(6)\n\t" \
  "v_mfma_f32_16x16x32_bf16 a[20:23], v[204:207], v[152:155], a[20:23]\n\t" \
  "v_mfma_f32_16x16x32_bf16 a[56:59], v[204:207], v[156:159], a[56:59]\n\t" \
  "v_mfma_f32_16x16x32_bf16 a[92:95], v[204:207], v[160:163], a[92:95]\n\t" \
  "v_mfma_f32_16x16x32_bf16 a[128:131], v[204:207], v[164:167], a[128:131]\n\t" \
  "ds_read_b64_tr_b16 v[204:205], v208 offset:27648\n\t" \
  "ds_read_b64_tr_b16 v[206:207], v208 offset:32256\n\t" \
  "s_waitcnt lgkmcnt(6)\n\t" \
  "v_mfma_f32_16x16x32_bf16 a[24:27], v[192:195], v[152:155], a[24:27]\n\t" \
  "v_mfma_f32_16x16x32_bf16 a[60:63], v[192:195], v[156:159], a[60:63]\n\t" \
  "v_mfma_f32_16x16x32_bf16 a[96:99], v[192:195], v[160:163], a[96:99]\n\t" \
  "v_mfma_f32_16x16x32_bf16 a[132:135], v[192:195], v[164:167], a[132:135]\n\t" \
  "ds_read_b64_tr_b16 v[192:193], v208 offset:27680\n\t" \
  "ds_read_b64_tr_b16 v[194:195], v208 offset:32288\n\t" \
  "s_waitcnt lgkmcnt(6)\n\t" \
  "v_mfma_f32_16x16x32_bf16 a[28:31], v[196:199], v[152:155], a[28:31]\n\t" \
  "v_mfma_f32_16x16x32_bf16 a[64:67], v[196:199], v[156:159], a[64:67]\n\t" \
  "v_mfma_f32_16x16x32_bf16 a[100:103], v[196:199], v[160:163], a[100:103]\n\t" \
  "v_mfma_f32_16x16x32_bf16 a[136:139], v[196:199], v[164:167], a[136:139]\n\t" \
  "ds_read_b64_tr_b16 v[196:197], v208 offset:27712\n\t" \
  "ds_read_b64_tr_b16 v[198:199], v208 offset:32320\n\t" \
  "s_waitcnt lgkmcnt(6)\n\t" \
  "v_mfma_f32_16x16x32_bf16 a[32:35], v[200:203], v[152:155], a[32:35]\n\t" \
  "v_mfma_f32_16x16x32_bf16 a[68:71], v[200:203], v[156:159], a[68:71]\n\t" \
  "v_mfma_f32_16x16x32_bf16 a[104:107], v[200:203], v[160:163], a[104:107]\n\t" \
  "v_mfma_f32_16x16x32_bf16 a[140:143], v[200:203], v[164:167], a[140:143]\n\t" \
  "ds_read_b64_tr_b16 v[200:201], v208 offset:27744\n\t" \
  "ds_read_b64_tr_b16 v[202:203], v208 offset:32352\n\t" \
  "s_waitcnt vmcnt(0)\n\t" \
  "s_waitcnt lgkmcnt(6)\n\t" \
  "v_mfma_f32_16x16x32_bf16 a[0:3], v[204:207], v[172:175], a[0:3]\n\t" \
  "v_mfma_f32_16x16x32_bf16 a[36:39], v[204:207], v[176:179], a[36:39]\n\t" \
  "v_mfma_f32_16x16x32_bf16 a[72:75], v[204:207], v[180:183], a[72:75]\n\t" \
  "v_mfma_f32_16x16x32_bf16 a[108:111], v[204:207], v[184:187], a[108:111]\n\t" \
  "v_mfma_f32_16x16x32_bf16 a[144:147], v[204:207], v[188:191], a[144:147]\n\t" \
  "ds_read_b64_tr_b16 v[204:205], v208 offset:27776\n\t" \
  "ds_read_b64_tr_b16 v[206:207], v208 offset:32384\n\t" \
  "s_waitcnt lgkmcnt(6)\n\t" \
  "v_mfma_f32_16x16x32_bf16 a[4:7], v[192:195], v[172:175], a[4:7]\n\t" \
  "v_mfma_f32_16x16x32_bf16 a[40:43], v[192:195], v[176:179], a[40:43]\n\t" \
  "v_mfma_f32_16x16x32_bf16 a[76:79], v[192:195], v[180:183], a[76:79]\n\t" \
  "v_mfma_f32_16x16x32_bf16 a[112:115], v[192:195], v[184:187], a[112:115]\n\t" \
  "v_mfma_f32_16x16x32_bf16 a[148:151], v[192:195], v[188:191], a[148:151]\n\t" \
  "ds_read_b64_tr_b16 v[192:193], v209 offset:27808\n\t" \
  "ds_read_b64_tr_b16 v[194:195], v209 offset:32416\n\t" \
  "s_waitcnt lgkmcnt(6)\n\t" \
  "v_mfma_f32_16x16x32_bf16 a[8:11], v[196:199], v[172:175], a[8:11]\n\t" \
  "v_mfma_f32_16x16x32_bf16 a[44:47], v[196:199], v[176:179], a[44:47]\n\t" \
  "v_mfma_f32_16x16x32_bf16 a[80:83], v[196:199], v[180:183], a[80:83]\n\t" \
  "v_mfma_f32_16x16x32_bf16 a[116:119], v[196:199], v[184:187], a[116:119]\n\t" \
  "v_mfma_f32_16x16x32_bf16 a[152:155], v[196:199], v[188:191], a[152:155]\n\t" \
  "ds_read_b64_tr_b16 v[196:197], v209 offset:27840\n\t" \
  "ds_read_b64_tr_b16 v[198:199], v209 offset:32448\n\t" \
  "s_waitcnt lgkmcnt(6)\n\t" \
  "v_mfma_f32_16x16x32_bf16 a[12:15], v[200:203], v[172:175], a[12:15]\n\t" \
  "v_mfma_f32_16x16x32_bf16 a[48:51], v[200:203], v[176:179], a[48:51]\n\t" \
  "v_mfma_f32_16x16x32_bf16 a[84:87], v[200:203], v[180:183], a[84:87]\n\t" \
  "v_mfma_f32_16x16x32_bf16 a[120:123], v[200:203], v[184:187], a[120:123]\n\t" \
  "v_mfma_f32_16x16x32_bf16 a[156:159], v[200:203], v[188:191], a[156:159]\n\t" \
  "ds_read_b64_tr_b16 v[200:201], v209 offset:27872\n\t" \
  "ds_read_b64_tr_b16 v[202:203], v209 offset:32480\n\t" \
  "s_waitcnt lgkmcnt(6)\n\t" \
  "v_mfma_f32_16x16x32_bf16 a[16:19], v[204:207], v[172:175], a[16:19]\n\t" \
  "v_mfma_f32_16x16x32_bf16 a[52:55], v[204:207], v[176:179], a[52:55]\n\t" \
  "v_mfma_f32_16x16x32_bf16 a[88:91], v[204:207], v[180:183], a[88:91]\n\t" \
  "v_mfma_f32_16x16x32_bf16 a[124:127], v[204:207], v[184:187], a[124:127]\n\t" \
  "v_mfma_f32_16x16x32_bf16 a[160:163], v[204:207], v[188:191], a[160:163]\n\t" \
  "ds_read_b64_tr_b16 v[204:205], v209 offset:27904\n\t" \
  "ds_read_b64_tr_b16 v[206:207], v209 offset:32512\n\t" \
  "s_waitcnt lgkmcnt(6)\n\t" \
  "v_mfma_f32_16x16x32_bf16 a[20:23], v[192:195], v[172:175], a[20:23]\n\t" \
  "v_mfma_f32_16x16x32_bf16 a[56:59], v[192:195], v[176:179], a[56:59]\n\t" \
  "v_mfma_f32_16x16x32_bf16 a[92:95], v[192:195], v[180:183], a[92:95]\n\t" \
  "v_mfma_f32_16x16x32_bf16 a[128:131], v[192:195], v[184:187], a[128:131]\n\t" \
  "s_waitcnt lgkmcnt(4)\n\t" \
  "v_mfma_f32_16x16x32_bf16 a[24:27], v[196:199], v[172:175], a[24:27]\n\t" \
  "v_mfma_f32_16x16x32_bf16 a[60:63], v[196:199], v[176:179], a[60:63]\n\t" \
  "v_mfma_f32_16x16x32_bf16 a[96:99], v[196:199], v[180:183], a[96:99]\n\t" \
  "v_mfma_f32_16x16x32_bf16 a[132:135], v[196:199], v[184:187], a[132:135]\n\t" \
  "s_waitcnt lgkmcnt(2)\n\t" \
  "v_mfma_f32_16x16x32_bf16 a[28:31], v[200:203], v[172:175], a[28:31]\n\t" \
  "v_mfma_f32_16x16x32_bf16 a[64:67], v[200:203], v[176:179], a[64:67]\n\t" \
  "v_mfma_f32_16x16x32_bf16 a[100:103], v[200:203], v[180:183], a[100:103]\n\t" \
  "v_mfma_f32_16x16x32_bf16 a[136:139], v[200:203], v[184:187], a[136:139]\n\t" \
  "s_waitcnt lgkmcnt(0)\n\t" \
  "v_mfma_f32_16x16x32_bf16 a[32:35], v[204:207], v[172:175], a[32:35]\n\t" \
  "v_mfma_f32_16x16x32_bf16 a[68:71], v[204:207], v[176:179], a[68:71]\n\t" \
  "v_mfma_f32_16x16x32_bf16 a[104:107], v[204:207], v[180:183], a[104:107]\n\t" \
  "v_mfma_f32_16x16x32_bf16 a[140:143], v[204:207], v[184:187], a[140:143]\n\t" \
  "s_mul_i32 s61, s73, 18432\n\t" \
  "v_add_u32 v208, s61, v244\n\t" \
  "s_xor_b32 s62, s73, 2\n\t" \
  "s_mul_i32 s61, s62, 18432\n\t" \
  "v_add_u32 v209, s61, v244\n\t" \
  "s_mul_i32 s61, s73, 3072\n\t" \
  "s_add_u32 s61, s61, 147456\n\t" \
  "v_add_u32 v210, s61, v244\n\t" \
  "s_mul_i32 s61, s62, 3072\n\t" \
  "s_add_u32 s61, s61, 147456\n\t" \
  "v_add_u32 v211, s61, v244\n\t" \
  "s_nop 7\n\t" \
  "s_cmp_lg_u32 s66, 0\n\t" \
  "s_cbranch_scc1 FRH1_%=\n\t" \
  "ds_write_b128 v208, a[72:75] offset:0\n\t" \
  "v_accvgpr_read_b32 v32, a0\n\t" \
  "v_accvgpr_read_b32 v33, a1\n\t" \
  "v_accvgpr_read_b32 v34, a2\n\t" \
  "v_accvgpr_read_b32 v35, a3\n\t" \
  "ds_write_b128 v208, a[76:79] offset:1024\n\t" \
  "v_accvgpr_read_b32 v36, a4\n\t" \
  "v_accvgpr_read_b32 v37, a5\n\t" \
  "v_accvgpr_read_b32 v38, a6\n\t" \
  "v_accvgpr_read_b32 v39, a7\n\t" \
  "ds_write_b128 v208, a[80:83] offset:2048\n\t" \
  "v_accvgpr_read_b32 v40, a8\n\t" \
  "v_accvgpr_read_b32 v41, a9\n\t" \
  "v_accvgpr_read_b32 v42, a10\n\t" \
  "v_accvgpr_read_b32 v43, a11\n\t" \
  "ds_write_b128 v208, a[84:87] offset:3072\n\t" \
  "v_accvgpr_read_b32 v44, a12\n\t" \
  "v_accvgpr_read_b32 v45, a13\n\t" \
  "v_accvgpr_read_b32 v46, a14\n\t" \
  "v_accvgpr_read_b32 v47, a15\n\t" \
  "ds_write_b128 v208, a[88:91] offset:4096\n\t" \
  "v_accvgpr_read_b32 v48, a16\n\t" \
  "v_accvgpr_read_b32 v49, a17\n\t" \
  "v_accvgpr_read_b32 v50, a18\n\t" \
  "v_accvgpr_read_b32 v51, a19\n\t" \
  "ds_write_b128 v208, a[92:95] offset:5120\n\t" \
  "v_accvgpr_read_b32 v52, a20\n\t" \
  "v_accvgpr_read_b32 v53, a21\n\t" \
  "v_accvgpr_read_b32 v54, a22\n\t" \
  "v_accvgpr_read_b32 v55, a23\n\t" \
  "ds_write_b128 v208, a[96:99] offset:6144\n\t" \
  "v_accvgpr_read_b32 v56, a24\n\t" \
  "v_accvgpr_read_b32 v57, a25\n\t" \
  "v_accvgpr_read_b32 v58, a26\n\t" \
  "v_accvgpr_read_b32 v59, a27\n\t" \
  "ds_write_b128 v208, a[100:103] offset:7168\n\t" \
  "v_accvgpr_read_b32 v60, a28\n\t" \
  "v_accvgpr_read_b32 v61, a29\n\t" \
  "v_accvgpr_read_b32 v62, a30\n\t" \
  "v_accvgpr_read_b32 v63, a31\n\t" \
  "ds_write_b128 v208, a[104:107] offset:8192\n\t" \
  "v_accvgpr_read_b32 v64, a32\n\t" \
  "v_accvgpr_read_b32 v65, a33\n\t" \
  "v_accvgpr_read_b32 v66, a34\n\t" \
  "v_accvgpr_read_b32 v67, a35\n\t" \
  "ds_write_b128 v208, a[108:111] offset:9216\n\t" \
  "v_accvgpr_read_b32 v68, a36\n\t" \
  "v_accvgpr_read_b32 v69, a37\n\t" \
  "v_accvgpr_read_b32 v70, a38\n\t" \
  "v_accvgpr_read_b32 v71, a39\n\t" \
  "ds_write_b128 v208, a[112:115] offset:10240\n\t" \
  "v_accvgpr_read_b32 v72, a40\n\t" \
  "v_accvgpr_read_b32 v73, a41\n\t" \
  "v_accvgpr_read_b32 v74, a42\n\t" \
  "v_accvgpr_read_b32 v75, a43\n\t" \
  "ds_write_b128 v208, a[116:119] offset:11264\n\t" \
  "v_accvgpr_read_b32 v76, a44\n\t" \
  "v_accvgpr_read_b32 v77, a45\n\t" \
  "v_accvgpr_read_b32 v78, a46\n\t" \
  "v_accvgpr_read_b32 v79, a47\n\t" \
  "ds_write_b128 v208, a[120:123] offset:12288\n\t" \
  "v_accvgpr_read_b32 v80, a48\n\t" \
  "v_accvgpr_read_b32 v81, a49\n\t" \
  "v_accvgpr_read_b32 v82, a50\n\t" \
  "v_accvgpr_read_b32 v83, a51\n\t" \
  "ds_write_b128 v208, a[124:127] offset:13312\n\t" \
  "v_accvgpr_read_b32 v84, a52\n\t" \
  "v_accvgpr_read_b32 v85, a53\n\t" \
  "v_accvgpr_read_b32 v86, a54\n\t" \
  "v_accvgpr_read_b32 v87, a55\n\t" \
  "ds_write_b128 v208, a[128:131] offset:14336\n\t" \
  "v_accvgpr_read_b32 v88, a56\n\t" \
  "v_accvgpr_read_b32 v89, a57\n\t" \
  "v_accvgpr_read_b32 v90, a58\n\t" \
  "v_accvgpr_read_b32 v91, a59\n\t" \
  "s_waitcnt lgkmcnt(8)\n\t" \
  "ds_write_b128 v208, a[132:135] offset:15360\n\t" \
  "v_accvgpr_read_b32 v92, a60\n\t" \
  "v_accvgpr_read_b32 v93, a61\n\t" \
  "v_accvgpr_read_b32 v94, a62\n\t" \
  "v_accvgpr_read_b32 v95, a63\n\t" \
  "ds_write_b128 v208, a[136:139] offset:16384\n\t" \
  "v_accvgpr_read_b32 v96, a64\n\t" \
  "v_accvgpr_read_b32 v97, a65\n\t" \
  "v_accvgpr_read_b32 v98, a66\n\t" \
  "v_accvgpr_read_b32 v99, a67\n\t" \
  "ds_write_b128 v208, a[140:143] offset:17408\n\t" \
  "v_accvgpr_read_b32 v100, a68\n\t" \
  "v_accvgpr_read_b32 v101, a69\n\t" \
  "v_accvgpr_read_b32 v102, a70\n\t" \
  "v_accvgpr_read_b32 v103, a71\n\t" \
  "ds_write_b128 v210, a[156:159] offset:0\n\t" \
  "v_accvgpr_read_b32 v104, a144\n\t" \
  "v_accvgpr_read_b32 v105, a145\n\t" \
  "v_accvgpr_read_b32 v106, a146\n\t" \
  "v_accvgpr_read_b32 v107, a147\n\t" \
  "ds_write_b128 v210, a[160:163] offset:1024\n\t" \
  "v_accvgpr_read_b32 v108, a148\n\t" \
  "v_accvgpr_read_b32 v109, a149\n\t" \
  "v_accvgpr_read_b32 v110, a150\n\t" \
  "v_accvgpr_read_b32 v111, a151\n\t" \
  "v_accvgpr_read_b32 v112, a152\n\t" \
  "v_accvgpr_read_b32 v113, a153\n\t" \
  "v_accvgpr_read_b32 v114, a154\n\t" \
  "v_accvgpr_read_b32 v115, a155\n\t" \
  "s_waitcnt lgkmcnt(0)\n\t" \
  "s_barrier\n\t" \
  "ds_read_b128 v[116:119], v209 offset:0\n\t" \
  "ds_read_b128 v[120:123], v209 offset:1024\n\t" \
  "ds_read_b128 v[124:127], v209 offset:2048\n\t" \
  "ds_read_b128 v[128:131], v209 offset:3072\n\t" \
  "ds_read_b128 v[132:135], v209 offset:4096\n\t" \
  "ds_read_b128 v[136:139], v209 offset:5120\n\t" \
  "ds_read_b128 v[140:143], v209 offset:6144\n\t" \
  "s_waitcnt lgkmcnt(6)\n\t" \
  "v_add_f32 v32, v32, v116\n\t" \
  "v_add_f32 v33, v33, v117\n\t" \
  "v_add_f32 v34, v34, v118\n\t" \
  "v_add_f32 v35, v35, v119\n\t" \
  "v_cvt_pk_bf16_f32 v160, v32, v33\n\t" \
  "v_cvt_pk_bf16_f32 v161, v34, v35\n\t" \
  "ds_read_b128 v[144:147], v209 offset:7168\n\t" \
  "s_waitcnt lgkmcnt(6)\n\t" \
  "v_add_f32 v36, v36, v120\n\t" \
  "v_add_f32 v37, v37, v121\n\t" \
  "v_add_f32 v38, v38, v122\n\t" \
  "v_add_f32 v39, v39, v123\n\t" \
  "v_cvt_pk_bf16_f32 v162, v36, v37\n\t" \
  "v_cvt_pk_bf16_f32 v163, v38, v39\n\t" \
  "ds_read_b128 v[116:119], v209 offset:8192\n\t" \
  "s_waitcnt lgkmcnt(5)\n\t" \
  "v_add_f32 v40, v40, v124\n\t" \
  "v_add_f32 v41, v41, v125\n\t" \
  "v_add_f32 v42, v42, v126\n\t" \
  "v_add_f32 v43, v43, v127\n\t" \
  "v_cvt_pk_bf16_f32 v164, v40, v41\n\t" \
  "v_cvt_pk_bf16_f32 v165, v42, v43\n\t" \
  "ds_read_b128 v[120:123], v209 offset:9216\n\t" \
  "v_add_f32 v44, v44, v128\n\t" \
  "v_add_f32 v45, v45, v129\n\t" \
  "v_add_f32 v46, v46, v130\n\t" \
  "v_add_f32 v47, v47, v131\n\t" \
  "v_cvt_pk_bf16_f32 v166, v44, v45\n\t" \
  "v_cvt_pk_bf16_f32 v167, v46, v47\n\t" \
  "ds_read_b128 v[124:127], v209 offset:10240\n\t" \
  "s_waitcnt lgkmcnt(5)\n\t" \
  "v_add_f32 v48, v48, v132\n\t" \
  "v_add_f32 v49, v49, v133\n\t" \
  "v_add_f32 v50, v50, v134\n\t" \
  "v_add_f32 v51, v51, v135\n\t" \
  "v_cvt_pk_bf16_f32 v168, v48, v49\n\t" \
  "v_cvt_pk_bf16_f32 v169, v50, v51\n\t" \
  "ds_read_b128 v[128:131], v209 offset:11264\n\t" \
  "v_add_f32 v52, v52, v136\n\t" \
  "v_add_f32 v53, v53, v137\n\t" \
  "v_add_f32 v54, v54, v138\n\t" \
  "v_add_f32 v55, v55, v139\n\t" \
  "v_cvt_pk_bf16_f32 v170, v52, v53\n\t" \
  "v_cvt_pk_bf16_f32 v171, v54, v55\n\t" \
  "ds_read_b128 v[132:135], v209 offset:12288\n\t" \
  "s_waitcnt lgkmcnt(5)\n\t" \
  "v_add_f32 v56, v56, v140\n\t" \
  "v_add_f32 v57, v57, v141\n\t" \
  "v_add_f32 v58, v58, v142\n\t" \
  "v_add_f32 v59, v59, v143\n\t" \
  "v_cvt_pk_bf16_f32 v172, v56, v57\n\t" \
  "v_cvt_pk_bf16_f32 v173, v58, v59\n\t" \
  "ds_read_b128 v[136:139], v209 offset:13312\n\t" \
  "v_add_f32 v60, v60, v144\n\t" \
  "v_add_f32 v61, v61, v145\n\t" \
  "v_add_f32 v62, v62, v146\n\t" \
  "v_add_f32 v63, v63, v147\n\t" \
  "v_cvt_pk_bf16_f32 v174, v60, v61\n\t" \
  "v_cvt_pk_bf16_f32 v175, v62, v63\n\t" \
  "ds_read_b128 v[140:143], v209 offset:14336\n\t" \
  "s_waitcnt lgkmcnt(5)\n\t" \
  "v_add_f32 v64, v64, v116\n\t" \
  "v_add_f32 v65, v65, v117\n\t" \
  "v_add_f32 v66, v66, v118\n\t" \
  "v_add_f32 v67, v67, v119\n\t" \
  "v_cvt_pk_bf16_f32 v176, v64, v65\n\t" \
  "v_cvt_pk_bf16_f32 v177, v66, v67\n\t" \
  "ds_read_b128 v[144:147], v209 offset:15360\n\t" \
  "v_add_f32 v68, v68, v120\n\t" \
  "v_add_f32 v69, v69, v121\n\t" \
  "v_add_f32 v70, v70, v122\n\t" \
  "v_add_f32 v71, v71, v123\n\t" \
  "v_cvt_pk_bf16_f32 v178, v68, v69\n\t" \
  "v_cvt_pk_bf16_f32 v179, v70, v71\n\t" \
  "ds_read_b128 v[116:119], v209 offset:16384\n\t" \
  "s_waitcnt lgkmcnt(5)\n\t" \
  "v_add_f32 v72, v72, v124\n\t" \
  "v_add_f32 v73, v73, v125\n\t" \
  "v_add_f32 v74, v74, v126\n\t" \
  "v_add_f32 v75, v75, v127\n\t" \
  "v_cvt_pk_bf16_f32 v180, v72, v73\n\t" \
  "v_cvt_pk_bf16_f32 v181, v74, v75\n\t" \
  "ds_read_b128 v[120:123], v209 offset:17408\n\t" \
  "v_add_f32 v76, v76, v128\n\t" \
  "v_add_f32 v77, v77, v129\n\t" \
  "v_add_f32 v78, v78, v130\n\t" \
  "v_add_f32 v79, v79, v131\n\t" \
  "v_cvt_pk_bf16_f32 v182, v76, v77\n\t" \
  "v_cvt_pk_bf16_f32 v183, v78, v79\n\t" \
  "ds_read_b128 v[124:127], v211 offset:0\n\t" \
  "s_waitcnt lgkmcnt(5)\n\t" \
  "v_add_f32 v80, v80, v132\n\t" \
  "v_add_f32 v81, v81, v133\n\t" \
  "v_add_f32 v82, v82, v134\n\t" \
  "v_add_f32 v83, v83, v135\n\t" \
  "v_cvt_pk_bf16_f32 v184, v80, v81\n\t" \
  "v_cvt_pk_bf16_f32 v185, v82, v83\n\t" \
  "ds_read_b128 v[128:131], v211 offset:1024\n\t" \
  "v_add_f32 v84, v84, v136\n\t" \
  "v_add_f32 v85, v85, v137\n\t" \
  "v_add_f32 v86, v86, v138\n\t" \
  "v_add_f32 v87, v87, v139\n\t" \
  "v_cvt_pk_bf16_f32 v186, v84, v85\n\t" \
  "v_cvt_pk_bf16_f32 v187, v86, v87\n\t" \
  "ds_read_b128 v[132:135], v211 offset:2048\n\t" \
  "s_waitcnt lgkmcnt(5)\n\t" \
  "v_add_f32 v88, v88, v140\n\t" \
  "v_add_f32 v89, v89, v141\n\t" \
  "v_add_f32 v90, v90, v142\n\t" \
  "v_add_f32 v91, v91, v143\n\t" \
  "v_cvt_pk_bf16_f32 v188, v88, v89\n\t" \
  "v_cvt_pk_bf16_f32 v189, v90, v91\n\t" \
  "v_add_f32 v92, v92, v144\n\t" \
  "v_add_f32 v93, v93, v145\n\t" \
  "v_add_f32 v94, v94, v146\n\t" \
  "v_add_f32 v95, v95, v147\n\t" \
  "v_cvt_pk_bf16_f32 v190, v92, v93\n\t" \
  "v_cvt_pk_bf16_f32 v191, v94, v95\n\t" \
  "s_waitcnt lgkmcnt(3)\n\t" \
  "v_add_f32 v96, v96, v116\n\t" \
  "v_add_f32 v97, v97, v117\n\t" \
  "v_add_f32 v98, v98, v118\n\t" \
  "v_add_f32 v99, v99, v119\n\t" \
  "v_cvt_pk_bf16_f32 v192, v96, v97\n\t" \
  "v_cvt_pk_bf16_f32 v193, v98, v99\n\t" \
  "v_add_f32 v100, v100, v120\n\t" \
  "v_add_f32 v101, v101, v121\n\t" \
  "v_add_f32 v102, v102, v122\n\t" \
  "v_add_f32 v103, v103, v123\n\t" \
  "v_cvt_pk_bf16_f32 v194, v100, v101\n\t" \
  "v_cvt_pk_bf16_f32 v195, v102, v103\n\t" \
  "s_waitcnt lgkmcnt(1)\n\t" \
  "v_add_f32 v104, v104, v124\n\t" \
  "v_add_f32 v105, v105, v125\n\t" \
  "v_add_f32 v106, v106, v126\n\t" \
  "v_add_f32 v107, v107, v127\n\t" \
  "v_cvt_pk_bf16_f32 v196, v104, v105\n\t" \
  "v_cvt_pk_bf16_f32 v197, v106, v107\n\t" \
  "v_add_f32 v108, v108, v128\n\t" \
  "v_add_f32 v109, v109, v129\n\t" \
  "v_add_f32 v110, v110, v130\n\t" \
  "v_add_f32 v111, v111, v131\n\t" \
  "v_cvt_pk_bf16_f32 v198, v108, v109\n\t" \
  "v_cvt_pk_bf16_f32 v199, v110, v111\n\t" \
  "s_waitcnt lgkmcnt(0)\n\t" \
  "v_add_f32 v112, v112, v132\n\t" \
  "v_add_f32 v113, v113, v133\n\t" \
  "v_add_f32 v114, v114, v134\n\t" \
  "v_add_f32 v115, v115, v135\n\t" \
  "v_cvt_pk_bf16_f32 v200, v112, v113\n\t" \
  "v_cvt_pk_bf16_f32 v201, v114, v115\n\t" \
  "v_mov_b32 v202, 0\n\t" \
  "v_mov_b32 v203, 0\n\t" \
  "s_branch FRD_%=\n\t" \
  "FRH1_%=:\n\t" \
  "ds_write_b128 v208, a[0:3] offset:0\n\t" \
  "v_accvgpr_read_b32 v32, a72\n\t" \
  "v_accvgpr_read_b32 v33, a73\n\t" \
  "v_accvgpr_read_b32 v34, a74\n\t" \
  "v_accvgpr_read_b32 v35, a75\n\t" \
  "ds_write_b128 v208, a[4:7] offset:1024\n\t" \
  "v_accvgpr_read_b32 v36, a76\n\t" \
  "v_accvgpr_read_b32 v37, a77\n\t" \
  "v_accvgpr_read_b32 v38, a78\n\t" \
  "v_accvgpr_read_b32 v39, a79\n\t" \
  "ds_write_b128 v208, a[8:11] offset:2048\n\t" \
  "v_accvgpr_read_b32 v40, a80\n\t" \
  "v_accvgpr_read_b32 v41, a81\n\t" \
  "v_accvgpr_read_b32 v42, a82\n\t" \
  "v_accvgpr_read_b32 v43, a83\n\t" \
  "ds_write_b128 v208, a[12:15] offset:3072\n\t" \
  "v_accvgpr_read_b32 v44, a84\n\t" \
  "v_accvgpr_read_b32 v45, a85\n\t" \
  "v_accvgpr_read_b32 v46, a86\n\t" \
  "v_accvgpr_read_b32 v47, a87\n\t" \
  "ds_write_b128 v208, a[16:19] offset:4096\n\t" \
  "v_accvgpr_read_b32 v48, a88\n\t" \
  "v_accvgpr_read_b32 v49, a89\n\t" \
  "v_accvgpr_read_b32 v50, a90\n\t" \
  "v_accvgpr_read_b32 v51, a91\n\t" \
  "ds_write_b128 v208, a[20:23] offset:5120\n\t" \
  "v_accvgpr_read_b32 v52, a92\n\t" \
  "v_accvgpr_read_b32 v53, a93\n\t" \
  "v_accvgpr_read_b32 v54, a94\n\t" \
  "v_accvgpr_read_b32 v55, a95\n\t" \
  "ds_write_b128 v208, a[24:27] offset:6144\n\t" \
  "v_accvgpr_read_b32 v56, a96\n\t" \
  "v_accvgpr_read_b32 v57, a97\n\t" \
  "v_accvgpr_read_b32 v58, a98\n\t" \
  "v_accvgpr_read_b32 v59, a99\n\t" \
  "ds_write_b128 v208, a[28:31] offset:7168\n\t" \
  "v_accvgpr_read_b32 v60, a100\n\t" \
  "v_accvgpr_read_b32 v61, a101\n\t" \
  "v_accvgpr_read_b32 v62, a102\n\t" \
  "v_accvgpr_read_b32 v63, a103\n\t" \
  "ds_write_b128 v208, a[32:35] offset:8192\n\t" \
  "v_accvgpr_read_b32 v64, a104\n\t" \
  "v_accvgpr_read_b32 v65, a105\n\t" \
  "v_accvgpr_read_b32 v66, a106\n\t" \
  "v_accvgpr_read_b32 v67, a107\n\t" \
  "ds_write_b128 v208, a[36:39] offset:9216\n\t" \
  "v_accvgpr_read_b32 v68, a108\n\t" \
  "v_accvgpr_read_b32 v69, a109\n\t" \
  "v_accvgpr_read_b32 v70, a110\n\t" \
  "v_accvgpr_read_b32 v71, a111\n\t" \
  "ds_write_b128 v208, a[40:43] offset:10240\n\t" \
  "v_accvgpr_read_b32 v72, a112\n\t" \
  "v_accvgpr_read_b32 v73, a113\n\t" \
  "v_accvgpr_read_b32 v74, a114\n\t" \
  "v_accvgpr_read_b32 v75, a115\n\t" \
  "ds_write_b128 v208, a[44:47] offset:11264\n\t" \
  "v_accvgpr_read_b32 v76, a116\n\t" \
  "v_accvgpr_read_b32 v77, a117\n\t" \
  "v_accvgpr_read_b32 v78, a118\n\t" \
  "v_accvgpr_read_b32 v79, a119\n\t" \
  "ds_write_b128 v208, a[48:51] offset:12288\n\t" \
  "v_accvgpr_read_b32 v80, a120\n\t" \
  "v_accvgpr_read_b32 v81, a121\n\t" \
  "v_accvgpr_read_b32 v82, a122\n\t" \
  "v_accvgpr_read_b32 v83, a123\n\t" \
  "ds_write_b128 v208, a[52:55] offset:13312\n\t" \
  "v_accvgpr_read_b32 v84, a124\n\t" \
  "v_accvgpr_read_b32 v85, a125\n\t" \
  "v_accvgpr_read_b32 v86, a126\n\t" \
  "v_accvgpr_read_b32 v87, a127\n\t" \
  "ds_write_b128 v208, a[56:59] offset:14336\n\t" \
  "v_accvgpr_read_b32 v88, a128\n\t" \
  "v_accvgpr_read_b32 v89, a129\n\t" \
  "v_accvgpr_read_b32 v90, a130\n\t" \
  "v_accvgpr_read_b32 v91, a131\n\t" \
  "s_waitcnt lgkmcnt(8)\n\t" \
  "ds_write_b128 v208, a[60:63] offset:15360\n\t" \
  "v_accvgpr_read_b32 v92, a132\n\t" \
  "v_accvgpr_read_b32 v93, a133\n\t" \
  "v_accvgpr_read_b32 v94, a134\n\t" \
  "v_accvgpr_read_b32 v95, a135\n\t" \
  "ds_write_b128 v208, a[64:67] offset:16384\n\t" \
  "v_accvgpr_read_b32 v96, a136\n\t" \
  "v_accvgpr_read_b32 v97, a137\n\t" \
  "v_accvgpr_read_b32 v98, a138\n\t" \
  "v_accvgpr_read_b32 v99, a139\n\t" \
  "ds_write_b128 v208, a[68:71] offset:17408\n\t" \
  "v_accvgpr_read_b32 v100, a140\n\t" \
  "v_accvgpr_read_b32 v101, a141\n\t" \
  "v_accvgpr_read_b32 v102, a142\n\t" \
  "v_accvgpr_read_b32 v103, a143\n\t" \
  "ds_write_b128 v210, a[144:147] offset:0\n\t" \
  "v_accvgpr_read_b32 v104, a156\n\t" \
  "v_accvgpr_read_b32 v105, a157\n\t" \
  "v_accvgpr_read_b32 v106, a158\n\t" \
  "v_accvgpr_read_b32 v107, a159\n\t" \
  "ds_write_b128 v210, a[148:151] offset:1024\n\t" \
  "v_accvgpr_read_b32 v108, a160\n\t" \
  "v_accvgpr_read_b32 v109, a161\n\t" \
  "v_accvgpr_read_b32 v110, a162\n\t" \
  "v_accvgpr_read_b32 v111, a163\n\t" \
  "ds_write_b128 v210, a[152:155] offset:2048\n\t" \
  "s_waitcnt lgkmcnt(0)\n\t" \
  "s_barrier\n\t" \
  "ds_read_b128 v[116:119], v209 offset:0\n\t" \
  "ds_read_b128 v[120:123], v209 offset:1024\n\t" \
  "ds_read_b128 v[124:127], v209 offset:2048\n\t" \
  "ds_read_b128 v[128:131], v209 offset:3072\n\t" \
  "ds_read_b128 v[132:135], v209 offset:4096\n\t" \
  "ds_read_b128 v[136:139], v209 offset:5120\n\t" \
  "ds_read_b128 v[140:143], v209 offset:6144\n\t" \
  "s_waitcnt lgkmcnt(6)\n\t" \
  "v_add_f32 v32, v32, v116\n\t" \
  "v_add_f32 v33, v33, v117\n\t" \
  "v_add_f32 v34, v34, v118\n\t" \
  "v_add_f32 v35, v35, v119\n\t" \
  "v_cvt_pk_bf16_f32 v160, v32, v33\n\t" \
  "v_cvt_pk_bf16_f32 v161, v34, v35\n\t" \
  "ds_read_b128 v[144:147], v209 offset:7168\n\t" \
  "s_waitcnt lgkmcnt(6)\n\t" \
  "v_add_f32 v36, v36, v120\n\t" \
  "v_add_f32 v37, v37, v121\n\t" \
  "v_add_f32 v38, v38, v122\n\t" \
  "v_add_f32 v39, v39, v123\n\t" \
  "v_cvt_pk_bf16_f32 v162, v36, v37\n\t" \
  "v_cvt_pk_bf16_f32 v163, v38, v39\n\t" \
  "ds_read_b128 v[116:119], v209 offset:8192\n\t" \
  "s_waitcnt lgkmcnt(5)\n\t" \
  "v_add_f32 v40, v40, v124\n\t" \
  "v_add_f32 v41, v41, v125\n\t" \
  "v_add_f32 v42, v42, v126\n\t" \
  "v_add_f32 v43, v43, v127\n\t" \
  "v_cvt_pk_bf16_f32 v164, v40, v41\n\t" \
  "v_cvt_pk_bf16_f32 v165, v42, v43\n\t" \
  "ds_read_b128 v[120:123], v209 offset:9216\n\t" \
  "v_add_f32 v44, v44, v128\n\t" \
  "v_add_f32 v45, v45, v129\n\t" \
  "v_add_f32 v46, v46, v130\n\t" \
  "v_add_f32 v47, v47, v131\n\t" \
  "v_cvt_pk_bf16_f32 v166, v44, v45\n\t" \
  "v_cvt_pk_bf16_f32 v167, v46, v47\n\t" \
  "ds_read_b128 v[124:127], v209 offset:10240\n\t" \
  "s_waitcnt lgkmcnt(5)\n\t" \
  "v_add_f32 v48, v48, v132\n\t" \
  "v_add_f32 v49, v49, v133\n\t" \
  "v_add_f32 v50, v50, v134\n\t" \
  "v_add_f32 v51, v51, v135\n\t" \
  "v_cvt_pk_bf16_f32 v168, v48, v49\n\t" \
  "v_cvt_pk_bf16_f32 v169, v50, v51\n\t" \
  "ds_read_b128 v[128:131], v209 offset:11264\n\t" \
  "v_add_f32 v52, v52, v136\n\t" \
  "v_add_f32 v53, v53, v137\n\t" \
  "v_add_f32 v54, v54, v138\n\t" \
  "v_add_f32 v55, v55, v139\n\t" \
  "v_cvt_pk_bf16_f32 v170, v52, v53\n\t" \
  "v_cvt_pk_bf16_f32 v171, v54, v55\n\t" \
  "ds_read_b128 v[132:135], v209 offset:12288\n\t" \
  "s_waitcnt lgkmcnt(5)\n\t" \
  "v_add_f32 v56, v56, v140\n\t" \
  "v_add_f32 v57, v57, v141\n\t" \
  "v_add_f32 v58, v58, v142\n\t" \
  "v_add_f32 v59, v59, v143\n\t" \
  "v_cvt_pk_bf16_f32 v172, v56, v57\n\t" \
  "v_cvt_pk_bf16_f32 v173, v58, v59\n\t" \
  "ds_read_b128 v[136:139], v209 offset:13312\n\t" \
  "v_add_f32 v60, v60, v144\n\t" \
  "v_add_f32 v61, v61, v145\n\t" \
  "v_add_f32 v62, v62, v146\n\t" \
  "v_add_f32 v63, v63, v147\n\t" \
  "v_cvt_pk_bf16_f32 v174, v60, v61\n\t" \
  "v_cvt_pk_bf16_f32 v175, v62, v63\n\t" \
  "ds_read_b128 v[140:143], v209 offset:14336\n\t" \
  "s_waitcnt lgkmcnt(5)\n\t" \
  "v_add_f32 v64, v64, v116\n\t" \
  "v_add_f32 v65, v65, v117\n\t" \
  "v_add_f32 v66, v66, v118\n\t" \
  "v_add_f32 v67, v67, v119\n\t" \
  "v_cvt_pk_bf16_f32 v176, v64, v65\n\t" \
  "v_cvt_pk_bf16_f32 v177, v66, v67\n\t" \
  "ds_read_b128 v[144:147], v209 offset:15360\n\t" \
  "v_add_f32 v68, v68, v120\n\t" \
  "v_add_f32 v69, v69, v121\n\t" \
  "v_add_f32 v70, v70, v122\n\t" \
  "v_add_f32 v71, v71, v123\n\t" \
  "v_cvt_pk_bf16_f32 v178, v68, v69\n\t" \
  "v_cvt_pk_bf16_f32 v179, v70, v71\n\t" \
  "ds_read_b128 v[116:119], v209 offset:16384\n\t" \
  "s_waitcnt lgkmcnt(5)\n\t" \
  "v_add_f32 v72, v72, v124\n\t" \
  "v_add_f32 v73, v73, v125\n\t" \
  "v_add_f32 v74, v74, v126\n\t" \
  "v_add_f32 v75, v75, v127\n\t" \
  "v_cvt_pk_bf16_f32 v180, v72, v73\n\t" \
  "v_cvt_pk_bf16_f32 v181, v74, v75\n\t" \
  "ds_read_b128 v[120:123], v209 offset:17408\n\t" \
  "v_add_f32 v76, v76, v128\n\t" \
  "v_add_f32 v77, v77, v129\n\t" \
  "v_add_f32 v78, v78, v130\n\t" \
  "v_add_f32 v79, v79, v131\n\t" \
  "v_cvt_pk_bf16_f32 v182, v76, v77\n\t" \
  "v_cvt_pk_bf16_f32 v183, v78, v79\n\t" \
  "ds_read_b128 v[124:127], v211 offset:0\n\t" \
  "s_waitcnt lgkmcnt(5)\n\t" \
  "v_add_f32 v80, v80, v132\n\t" \
  "v_add_f32 v81, v81, v133\n\t" \
  "v_add_f32 v82, v82, v134\n\t" \
  "v_add_f32 v83, v83, v135\n\t" \
  "v_cvt_pk_bf16_f32 v184, v80, v81\n\t" \
  "v_cvt_pk_bf16_f32 v185, v82, v83\n\t" \
  "ds_read_b128 v[128:131], v211 offset:1024\n\t" \
  "v_add_f32 v84, v84, v136\n\t" \
  "v_add_f32 v85, v85, v137\n\t" \
  "v_add_f32 v86, v86, v138\n\t" \
  "v_add_f32 v87, v87, v139\n\t" \
  "v_cvt_pk_bf16_f32 v186, v84, v85\n\t" \
  "v_cvt_pk_bf16_f32 v187, v86, v87\n\t" \
  "s_waitcnt lgkmcnt(4)\n\t" \
  "v_add_f32 v88, v88, v140\n\t" \
  "v_add_f32 v89, v89, v141\n\t" \
  "v_add_f32 v90, v90, v142\n\t" \
  "v_add_f32 v91, v91, v143\n\t" \
  "v_cvt_pk_bf16_f32 v188, v88, v89\n\t" \
  "v_cvt_pk_bf16_f32 v189, v90, v91\n\t" \
  "v_add_f32 v92, v92, v144\n\t" \
  "v_add_f32 v93, v93, v145\n\t" \
  "v_add_f32 v94, v94, v146\n\t" \
  "v_add_f32 v95, v95, v147\n\t" \
  "v_cvt_pk_bf16_f32 v190, v92, v93\n\t" \
  "v_cvt_pk_bf16_f32 v191, v94, v95\n\t" \
  "s_waitcnt lgkmcnt(2)\n\t" \
  "v_add_f32 v96, v96, v116\n\t" \
  "v_add_f32 v97, v97, v117\n\t" \
  "v_add_f32 v98, v98, v118\n\t" \
  "v_add_f32 v99, v99, v119\n\t" \
  "v_cvt_pk_bf16_f32 v192, v96, v97\n\t" \
  "v_cvt_pk_bf16_f32 v193, v98, v99\n\t" \
  "v_add_f32 v100, v100, v120\n\t" \
  "v_add_f32 v101, v101, v121\n\t" \
  "v_add_f32 v102, v102, v122\n\t" \
  "v_add_f32 v103, v103, v123\n\t" \
  "v_cvt_pk_bf16_f32 v194, v100, v101\n\t" \
  "v_cvt_pk_bf16_f32 v195, v102, v103\n\t" \
  "s_waitcnt lgkmcnt(0)\n\t" \
  "v_add_f32 v104, v104, v124\n\t" \
  "v_add_f32 v105, v105, v125\n\t" \
  "v_add_f32 v106, v106, v126\n\t" \
  "v_add_f32 v107, v107, v127\n\t" \
  "v_cvt_pk_bf16_f32 v196, v104, v105\n\t" \
  "v_cvt_pk_bf16_f32 v197, v106, v107\n\t" \
  "v_add_f32 v108, v108, v128\n\t" \
  "v_add_f32 v109, v109, v129\n\t" \
  "v_add_f32 v110, v110, v130\n\t" \
  "v_add_f32 v111, v111, v131\n\t" \
  "v_cvt_pk_bf16_f32 v198, v108, v109\n\t" \
  "v_cvt_pk_bf16_f32 v199, v110, v111\n\t" \
  "v_mov_b32 v200, 0\n\t" \
  "v_mov_b32 v201, 0\n\t" \
  "v_mov_b32 v202, 0\n\t" \
  "v_mov_b32 v203, 0\n\t" \
  "FRD_%=:\n\t" \
  "s_mul_i32 s75, s73, 49152\n\t" \
  "s_mul_i32 s61, s64, 12288\n\t" \
  "s_add_u32 s62, s75, s61\n\t" \
  "buffer_store_dwordx4 v[160:163], v244, s[44:47], s62 offen sc1\n\t" \
  "buffer_store_dwordx4 v[164:167], v244, s[44:47], s62 offen offset:1024 sc1\n\t" \
  "buffer_store_dwordx4 v[168:171], v244, s[44:47], s62 offen offset:2048 sc1\n\t" \
  "buffer_store_dwordx4 v[172:175], v244, s[44:47], s62 offen offset:3072 sc1\n\t" \
  "s_add_u32 s62, s62, 4096\n\t" \
  "buffer_store_dwordx4 v[176:179], v244, s[44:47], s62 offen sc1\n\t" \
  "buffer_store_dwordx4 v[180:183], v244, s[44:47], s62 offen offset:1024 sc1\n\t" \
  "buffer_store_dwordx4 v[184:187], v244, s[44:47], s62 offen offset:2048 sc1\n\t" \
  "buffer_store_dwordx4 v[188:191], v244, s[44:47], s62 offen offset:3072 sc1\n\t" \
  "s_add_u32 s62, s62, 4096\n\t" \
  "buffer_store_dwordx4 v[192:195], v244, s[44:47], s62 offen sc1\n\t" \
  "buffer_store_dwordx4 v[196:199], v244, s[44:47], s62 offen offset:1024 sc1\n\t" \
  "buffer_store_dwordx4 v[200:203], v244, s[44:47], s62 offen offset:2048 sc1\n\t" \
  "v_lshrrev_b32 v248, 4, v244\n\t" \
  "v_and_b32 v249, 15, v248\n\t" \
  "v_lshrrev_b32 v248, 4, v248\n\t" \
  "v_mul_u32_u24 v249, 0x900, v249\n\t" \
  "v_lshl_add_u32 v247, v248, 3, v249\n\t" \
  "s_lshl_b32 s61, s64, 1\n\t" \
  "s_add_u32 s61, s61, 0\n\t" \
  "s_cmp_ge_u32 s61, 9\n\t" \
  "s_cselect_b32 s76, 1, 0\n\t" \
  "s_mul_i32 s63, s76, 9\n\t" \
  "s_sub_u32 s63, s61, s63\n\t" \
  "s_lshl_b32 s68, s66, 1\n\t" \
  "s_add_u32 s76, s76, s68\n\t" \
  "s_lshl_b32 s68, s85, 2\n\t" \
  "s_add_u32 s76, s76, s68\n\t" \
  "s_mul_i32 s68, s66, 3\n\t" \
  "s_add_u32 s68, s68, s61\n\t" \
  "s_sub_u32 s68, s68, 18\n\t" \
  "s_cmp_ge_u32 s61, 18\n\t" \
  "s_cselect_b32 s76, 8, s76\n\t" \
  "s_cselect_b32 s63, s68, s63\n\t" \
  "s_add_u32 s68, s61, s66\n\t" \
  "s_lshl_b32 s69, s85, 2\n\t" \
  "s_add_u32 s63, s63, s69\n\t" \
  "s_cmp_ge_u32 s63, 9\n\t" \
  "s_cselect_b32 s69, 9, 0\n\t" \
  "s_sub_u32 s63, s63, s69\n\t" \
  "s_mul_i32 s76, s76, 36864\n\t" \
  "s_lshl_b32 s63, s63, 5\n\t" \
  "s_add_u32 s76, s76, s63\n\t" \
  "s_cmp_gt_u32 s68, 20\n\t" \
  "s_cselect_b32 s76, 0x7f000000, s76\n\t" \
  "s_lshl_b32 s61, s64, 1\n\t" \
  "s_add_u32 s61, s61, 1\n\t" \
  "s_cmp_ge_u32 s61, 9\n\t" \
  "s_cselect_b32 s77, 1, 0\n\t" \
  "s_mul_i32 s63, s77, 9\n\t" \
  "s_sub_u32 s63, s61, s63\n\t" \
  "s_lshl_b32 s68, s66, 1\n\t" \
  "s_add_u32 s77, s77, s68\n\t" \
  "s_lshl_b32 s68, s85, 2\n\t" \
  "s_add_u32 s77, s77, s68\n\t" \
  "s_mul_i32 s68, s66, 3\n\t" \
  "s_add_u32 s68, s68, s61\n\t" \
  "s_sub_u32 s68, s68, 18\n\t" \
  "s_cmp_ge_u32 s61, 18\n\t" \
  "s_cselect_b32 s77, 8, s77\n\t" \
  "s_cselect_b32 s63, s68, s63\n\t" \
  "s_add_u32 s68, s61, s66\n\t" \
  "s_lshl_b32 s69, s85, 2\n\t" \
  "s_add_u32 s63, s63, s69\n\t" \
  "s_cmp_ge_u32 s63, 9\n\t" \
  "s_cselect_b32 s69, 9, 0\n\t" \
  "s_sub_u32 s63, s63, s69\n\t" \
  "s_mul_i32 s77, s77, 36864\n\t" \
  "s_lshl_b32 s63, s63, 5\n\t" \
  "s_add_u32 s77, s77, s63\n\t" \
  "s_cmp_gt_u32 s68, 20\n\t" \
  "s_cselect_b32 s77, 0x7f000000, s77\n\t" \
  "s_lshl_b32 s61, s64, 1\n\t" \
  "s_add_u32 s61, s61, 8\n\t" \
  "s_cmp_ge_u32 s61, 9\n\t" \
  "s_cselect_b32 s78, 1, 0\n\t" \
  "s_mul_i32 s63, s78, 9\n\t" \
  "s_sub_u32 s63, s61, s63\n\t" \
  "s_lshl_b32 s68, s66, 1\n\t" \
  "s_add_u32 s78, s78, s68\n\t" \
  "s_lshl_b32 s68, s85, 2\n\t" \
  "s_add_u32 s78, s78, s68\n\t" \
  "s_mul_i32 s68, s66, 3\n\t" \
  "s_add_u32 s68, s68, s61\n\t" \
  "s_sub_u32 s68, s68, 18\n\t" \
  "s_cmp_ge_u32 s61, 18\n\t" \
  "s_cselect_b32 s78, 8, s78\n\t" \
  "s_cselect_b32 s63, s68, s63\n\t" \
  "s_add_u32 s68, s61, s66\n\t" \
  "s_lshl_b32 s69, s85, 2\n\t" \
  "s_add_u32 s63, s63, s69\n\t" \
  "s_cmp_ge_u32 s63, 9\n\t" \
  "s_cselect_b32 s69, 9, 0\n\t" \
  "s_sub_u32 s63, s63, s69\n\t" \
  "s_mul_i32 s78, s78, 36864\n\t" \
  "s_lshl_b32 s63, s63, 5\n\t" \
  "s_add_u32 s78, s78, s63\n\t" \
  "s_cmp_gt_u32 s68, 20\n\t" \
  "s_cselect_b32 s78, 0x7f000000, s78\n\t" \
  "s_lshl_b32 s61, s64, 1\n\t" \
  "s_add_u32 s61, s61, 9\n\t" \
  "s_cmp_ge_u32 s61, 9\n\t" \
  "s_cselect_b32 s80, 1, 0\n\t" \
  "s_mul_i32 s63, s80, 9\n\t" \
  "s_sub_u32 s63, s61, s63\n\t" \
  "s_lshl_b32 s68, s66, 1\n\t" \
  "s_add_u32 s80, s80, s68\n\t" \
  "s_lshl_b32 s68, s85, 2\n\t" \
  "s_add_u32 s80, s80, s68\n\t" \
  "s_mul_i32 s68, s66, 3\n\t" \
  "s_add_u32 s68, s68, s61\n\t" \
  "s_sub_u32 s68, s68, 18\n\t" \
  "s_cmp_ge_u32 s61, 18\n\t" \
  "s_cselect_b32 s80, 8, s80\n\t" \
  "s_cselect_b32 s63, s68, s63\n\t" \
  "s_add_u32 s68, s61, s66\n\t" \
  "s_lshl_b32 s69, s85, 2\n\t" \
  "s_add_u32 s63, s63, s69\n\t" \
  "s_cmp_ge_u32 s63, 9\n\t" \
  "s_cselect_b32 s69, 9, 0\n\t" \
  "s_sub_u32 s63, s63, s69\n\t" \
  "s_mul_i32 s80, s80, 36864\n\t" \
  "s_lshl_b32 s63, s63, 5\n\t" \
  "s_add_u32 s80, s80, s63\n\t" \
  "s_cmp_gt_u32 s68, 20\n\t" \
  "s_cselect_b32 s80, 0x7f000000, s80\n\t" \
  "s_lshl_b32 s61, s64, 1\n\t" \
  "s_add_u32 s61, s61, 16\n\t" \
  "s_cmp_ge_u32 s61, 9\n\t" \
  "s_cselect_b32 s81, 1, 0\n\t" \
  "s_mul_i32 s63, s81, 9\n\t" \
  "s_sub_u32 s63, s61, s63\n\t" \
  "s_lshl_b32 s68, s66, 1\n\t" \
  "s_add_u32 s81, s81, s68\n\t" \
  "s_lshl_b32 s68, s85, 2\n\t" \
  "s_add_u32 s81, s81, s68\n\t" \
  "s_mul_i32 s68, s66, 3\n\t" \
  "s_add_u32 s68, s68, s61\n\t" \
  "s_sub_u32 s68, s68, 18\n\t" \
  "s_cmp_ge_u32 s61, 18\n\t" \
  "s_cselect_b32 s81, 8, s81\n\t" \
  "s_cselect_b32 s63, s68, s63\n\t" \
  "s_add_u32 s68, s61, s66\n\t" \
  "s_lshl_b32 s69, s85, 2\n\t" \
  "s_add_u32 s63, s63, s69\n\t" \
  "s_cmp_ge_u32 s63, 9\n\t" \
  "s_cselect_b32 s69, 9, 0\n\t" \
  "s_sub_u32 s63, s63, s69\n\t" \
  "s_mul_i32 s81, s81, 36864\n\t" \
  "s_lshl_b32 s63, s63, 5\n\t" \
  "s_add_u32 s81, s81, s63\n\t" \
  "s_cmp_gt_u32 s68, 20\n\t" \
  "s_cselect_b32 s81, 0x7f000000, s81\n\t" \
  "s_lshl_b32 s61, s64, 1\n\t" \
  "s_add_u32 s61, s61, 17\n\t" \
  "s_cmp_ge_u32 s61, 9\n\t" \
  "s_cselect_b32 s82, 1, 0\n\t" \
  "s_mul_i32 s63, s82, 9\n\t" \
  "s_sub_u32 s63, s61, s63\n\t" \
  "s_lshl_b32 s68, s66, 1\n\t" \
  "s_add_u32 s82, s82, s68\n\t" \
  "s_lshl_b32 s68, s85, 2\n\t" \
  "s_add_u32 s82, s82, s68\n\t" \
  "s_mul_i32 s68, s66, 3\n\t" \
  "s_add_u32 s68, s68, s61\n\t" \
  "s_sub_u32 s68, s68, 18\n\t" \
  "s_cmp_ge_u32 s61, 18\n\t" \
  "s_cselect_b32 s82, 8, s82\n\t" \
  "s_cselect_b32 s63, s68, s63\n\t" \
  "s_add_u32 s68, s61, s66\n\t" \
  "s_lshl_b32 s69, s85, 2\n\t" \
  "s_add_u32 s63, s63, s69\n\t" \
  "s_cmp_ge_u32 s63, 9\n\t" \
  "s_cselect_b32 s69, 9, 0\n\t" \
  "s_sub_u32 s63, s63, s69\n\t" \
  "s_mul_i32 s82, s82, 36864\n\t" \
  "s_lshl_b32 s63, s63, 5\n\t" \
  "s_add_u32 s82, s82, s63\n\t" \
  "s_cmp_gt_u32 s68, 20\n\t" \
  "s_cselect_b32 s82, 0x7f000000, s82\n\t" \
  "s_waitcnt vmcnt(0)\n\t" \
  "s_barrier\n\t" \
  "s_cmp_lg_u32 s73, 0\n\t" \
  "s_cbranch_scc1 FT4W_%=\n\t" \
  "s_add_u32 s61, s79, 4096\n\t" \
  "v_mov_b32 v250, s52\n\t" \
  "v_mov_b32 v251, s53\n\t" \
  "v_mov_b32 v254, 0\n\t" \
  "s_mov_b64 exec, 1\n\t" \
  "s_nop 1\n\t" \
  "buffer_store_dwordx2 v[250:251], v254, s[96:99], s61 offen sc1\n\t" \
  "s_mov_b64 exec, -1\n\t" \
  "s_nop 4\n\t" \
  "FP4L_%=:\n\t" \
  "buffer_load_dwordx2 v[250:251], v253, s[96:99], 0 offen sc1\n\t" \
  "s_waitcnt vmcnt(0)\n\t" \
  "v_cmp_eq_u64 vcc, s[52:53], v[250:251]\n\t" \
  "s_nop 4\n\t" \
  "s_cmp_eq_u64 vcc, exec\n\t" \
  "s_cbranch_scc1 FP4D_%=\n\t" \
  "s_sleep 2\n\t" \
  "s_add_u32 s74, s74, 1\n\t" \
  "s_cmp_lt_u32 s74, 0x40000\n\t" \
  "s_cbranch_scc1 FP4L_%=\n\t" \
  "FP4D_%=:\n\t" \
  "s_or_b32 s61, s64, s65\n\t" \
  "s_cmp_lg_u32 s61, 0\n\t" \
  "s_cbranch_scc1 FT4W_%=\n\t" \
  "v_mov_b32 v250, s52\n\t" \
  "v_mov_b32 v251, s53\n\t" \
  "v_mov_b32 v254, 0\n\t" \
  "s_mov_b64 exec, 1\n\t" \
  "s_nop 1\n\t" \
  "buffer_store_dwordx2 v[250:251], v254, s[96:99], 0 offen sc1\n\t" \
  "s_mov_b64 exec, -1\n\t" \
  "s_nop 4\n\t" \
  "FT4W_%=:\n\t" \
  "s_barrier\n\t" \
  "s_lshl_b32 s61, s64, 10\n\t" \
  "s_add_u32 s62, s75, s61\n\t" \
  "s_add_u32 s63, s62, 0\n\t" \
  "buffer_load_dwordx4 v[32:35], v244, s[44:47], s63 offen sc1\n\t" \
  "s_add_u32 s63, s62, 12288\n\t" \
  "buffer_load_dwordx4 v[36:39], v244, s[44:47], s63 offen sc1\n\t" \
  "s_add_u32 s63, s62, 24576\n\t" \
  "buffer_load_dwordx4 v[40:43], v244, s[44:47], s63 offen sc1\n\t" \
  "s_add_u32 s63, s62, 36864\n\t" \
  "buffer_load_dwordx4 v[44:47], v244, s[44:47], s63 offen sc1\n\t" \
  "s_add_u32 s63, s62, 4096\n\t" \
  "buffer_load_dwordx4 v[48:51], v244, s[44:47], s63 offen sc1\n\t" \
  "s_add_u32 s63, s62, 16384\n\t" \
  "buffer_load_dwordx4 v[52:55], v244, s[44:47], s63 offen sc1\n\t" \
  "s_add_u32 s63, s62, 28672\n\t" \
  "buffer_load_dwordx4 v[56:59], v244, s[44:47], s63 offen sc1\n\t" \
  "s_add_u32 s63, s62, 40960\n\t" \
  "buffer_load_dwordx4 v[60:63], v244, s[44:47], s63 offen sc1\n\t" \
  "s_add_u32 s63, s62, 8192\n\t" \
  "buffer_load_dwordx4 v[64:67], v244, s[44:47], s63 offen sc1\n\t" \
  "s_add_u32 s63, s62, 20480\n\t" \
  "buffer_load_dwordx4 v[68:71], v244, s[44:47], s63 offen sc1\n\t" \
  "s_add_u32 s63, s62, 32768\n\t" \
  "buffer_load_dwordx4 v[72:75], v244, s[44:47], s63 offen sc1\n\t" \
  "s_add_u32 s63, s62, 45056\n\t" \
  "buffer_load_dwordx4 v[76:79], v244, s[44:47], s63 offen sc1\n\t" \
  "s_waitcnt vmcnt(11)\n\t" \
  "v_lshlrev_b32 v100, 16, v32\n\t" \
  "v_and_b32 v101, 0xffff0000, v32\n\t" \
  "v_lshlrev_b32 v102, 16, v33\n\t" \
  "v_and_b32 v103, 0xffff0000, v33\n\t" \
  "s_waitcnt vmcnt(10)\n\t" \
  "v_lshlrev_b32 v104, 16, v36\n\t" \
  "v_and_b32 v105, 0xffff0000, v36\n\t" \
  "v_lshlrev_b32 v106, 16, v37\n\t" \
  "v_and_b32 v107, 0xffff0000, v37\n\t" \
  "v_add_f32 v100, v100, v104\n\t" \
  "v_add_f32 v101, v101, v105\n\t" \
  "v_add_f32 v102, v102, v106\n\t" \
  "v_add_f32 v103, v103, v107\n\t" \
  "s_waitcnt vmcnt(9)\n\t" \
  "v_lshlrev_b32 v104, 16, v40\n\t" \
  "v_and_b32 v105, 0xffff0000, v40\n\t" \
  "v_lshlrev_b32 v106, 16, v41\n\t" \
  "v_and_b32 v107, 0xffff0000, v41\n\t" \
  "v_add_f32 v100, v100, v104\n\t" \
  "v_add_f32 v101, v101, v105\n\t" \
  "v_add_f32 v102, v102, v106\n\t" \
  "v_add_f32 v103, v103, v107\n\t" \
  "s_waitcnt vmcnt(8)\n\t" \
  "v_lshlrev_b32 v104, 16, v44\n\t" \
  "v_and_b32 v105, 0xffff0000, v44\n\t" \
  "v_lshlrev_b32 v106, 16, v45\n\t" \
  "v_and_b32 v107, 0xffff0000, v45\n\t" \
  "v_add_f32 v100, v100, v104\n\t" \
  "v_add_f32 v101, v101, v105\n\t" \
  "v_add_f32 v102, v102, v106\n\t" \
  "v_add_f32 v103, v103, v107\n\t" \
  "v_cvt_pk_bf16_f32 v120, v100, v101\n\t" \
  "v_cvt_pk_bf16_f32 v121, v102, v103\n\t" \
  "buffer_store_dwordx2 v[120:121], v247, s[48:51], s76 offen\n\t" \
  "v_lshlrev_b32 v108, 16, v34\n\t" \
  "v_and_b32 v109, 0xffff0000, v34\n\t" \
  "v_lshlrev_b32 v110, 16, v35\n\t" \
  "v_and_b32 v111, 0xffff0000, v35\n\t" \
  "v_lshlrev_b32 v112, 16, v38\n\t" \
  "v_and_b32 v113, 0xffff0000, v38\n\t" \
  "v_lshlrev_b32 v114, 16, v39\n\t" \
  "v_and_b32 v115, 0xffff0000, v39\n\t" \
  "v_add_f32 v108, v108, v112\n\t" \
  "v_add_f32 v109, v109, v113\n\t" \
  "v_add_f32 v110, v110, v114\n\t" \
  "v_add_f32 v111, v111, v115\n\t" \
  "v_lshlrev_b32 v112, 16, v42\n\t" \
  "v_and_b32 v113, 0xffff0000, v42\n\t" \
  "v_lshlrev_b32 v114, 16, v43\n\t" \
  "v_and_b32 v115, 0xffff0000, v43\n\t" \
  "v_add_f32 v108, v108, v112\n\t" \
  "v_add_f32 v109, v109, v113\n\t" \
  "v_add_f32 v110, v110, v114\n\t" \
  "v_add_f32 v111, v111, v115\n\t" \
  "v_lshlrev_b32 v112, 16, v46\n\t" \
  "v_and_b32 v113, 0xffff0000, v46\n\t" \
  "v_lshlrev_b32 v114, 16, v47\n\t" \
  "v_and_b32 v115, 0xffff0000, v47\n\t" \
  "v_add_f32 v108, v108, v112\n\t" \
  "v_add_f32 v109, v109, v113\n\t" \
  "v_add_f32 v110, v110, v114\n\t" \
  "v_add_f32 v111, v111, v115\n\t" \
  "v_cvt_pk_bf16_f32 v122, v108, v109\n\t" \
  "v_cvt_pk_bf16_f32 v123, v110, v111\n\t" \
  "buffer_store_dwordx2 v[122:123], v247, s[48:51], s77 offen\n\t" \
  "s_waitcnt vmcnt(9)\n\t" \
  "v_lshlrev_b32 v100, 16, v48\n\t" \
  "v_and_b32 v101, 0xffff0000, v48\n\t" \
  "v_lshlrev_b32 v102, 16, v49\n\t" \
  "v_and_b32 v103, 0xffff0000, v49\n\t" \
  "s_waitcnt vmcnt(8)\n\t" \
  "v_lshlrev_b32 v104, 16, v52\n\t" \
  "v_and_b32 v105, 0xffff0000, v52\n\t" \
  "v_lshlrev_b32 v106, 16, v53\n\t" \
  "v_and_b32 v107, 0xffff0000, v53\n\t" \
  "v_add_f32 v100, v100, v104\n\t" \
  "v_add_f32 v101, v101, v105\n\t" \
  "v_add_f32 v102, v102, v106\n\t" \
  "v_add_f32 v103, v103, v107\n\t" \
  "s_waitcnt vmcnt(7)\n\t" \
  "v_lshlrev_b32 v104, 16, v56\n\t" \
  "v_and_b32 v105, 0xffff0000, v56\n\t" \
  "v_lshlrev_b32 v106, 16, v57\n\t" \
  "v_and_b32 v107, 0xffff0000, v57\n\t" \
  "v_add_f32 v100, v100, v104\n\t" \
  "v_add_f32 v101, v101, v105\n\t" \
  "v_add_f32 v102, v102, v106\n\t" \
  "v_add_f32 v103, v103, v107\n\t" \
  "s_waitcnt vmcnt(6)\n\t" \
  "v_lshlrev_b32 v104, 16, v60\n\t" \
  "v_and_b32 v105, 0xffff0000, v60\n\t" \
  "v_lshlrev_b32 v106, 16, v61\n\t" \
  "v_and_b32 v107, 0xffff0000, v61\n\t" \
  "v_add_f32 v100, v100, v104\n\t" \
  "v_add_f32 v101, v101, v105\n\t" \
  "v_add_f32 v102, v102, v106\n\t" \
  "v_add_f32 v103, v103, v107\n\t" \
  "v_cvt_pk_bf16_f32 v124, v100, v101\n\t" \
  "v_cvt_pk_bf16_f32 v125, v102, v103\n\t" \
  "buffer_store_dwordx2 v[124:125], v247, s[48:51], s78 offen\n\t" \
  "v_lshlrev_b32 v108, 16, v50\n\t" \
  "v_and_b32 v109, 0xffff0000, v50\n\t" \
  "v_lshlrev_b32 v110, 16, v51\n\t" \
  "v_and_b32 v111, 0xffff0000, v51\n\t" \
  "v_lshlrev_b32 v112, 16, v54\n\t" \
  "v_and_b32 v113, 0xffff0000, v54\n\t" \
  "v_lshlrev_b32 v114, 16, v55\n\t" \
  "v_and_b32 v115, 0xffff0000, v55\n\t" \
  "v_add_f32 v108, v108, v112\n\t" \
  "v_add_f32 v109, v109, v113\n\t" \
  "v_add_f32 v110, v110, v114\n\t" \
  "v_add_f32 v111, v111, v115\n\t" \
  "v_lshlrev_b32 v112, 16, v58\n\t" \
  "v_and_b32 v113, 0xffff0000, v58\n\t" \
  "v_lshlrev_b32 v114, 16, v59\n\t" \
  "v_and_b32 v115, 0xffff0000, v59\n\t" \
  "v_add_f32 v108, v108, v112\n\t" \
  "v_add_f32 v109, v109, v113\n\t" \
  "v_add_f32 v110, v110, v114\n\t" \
  "v_add_f32 v111, v111, v115\n\t" \
  "v_lshlrev_b32 v112, 16, v62\n\t" \
  "v_and_b32 v113, 0xffff0000, v62\n\t" \
  "v_lshlrev_b32 v114, 16, v63\n\t" \
  "v_and_b32 v115, 0xffff0000, v63\n\t" \
  "v_add_f32 v108, v108, v112\n\t" \
  "v_add_f32 v109, v109, v113\n\t" \
  "v_add_f32 v110, v110, v114\n\t" \
  "v_add_f32 v111, v111, v115\n\t" \
  "v_cvt_pk_bf16_f32 v126, v108, v109\n\t" \
  "v_cvt_pk_bf16_f32 v127, v110, v111\n\t" \
  "buffer_store_dwordx2 v[126:127], v247, s[48:51], s80 offen\n\t" \
  "s_waitcnt vmcnt(7)\n\t" \
  "v_lshlrev_b32 v100, 16, v64\n\t" \
  "v_and_b32 v101, 0xffff0000, v64\n\t" \
  "v_lshlrev_b32 v102, 16, v65\n\t" \
  "v_and_b32 v103, 0xffff0000, v65\n\t" \
  "s_waitcnt vmcnt(6)\n\t" \
  "v_lshlrev_b32 v104, 16, v68\n\t" \
  "v_and_b32 v105, 0xffff0000, v68\n\t" \
  "v_lshlrev_b32 v106, 16, v69\n\t" \
  "v_and_b32 v107, 0xffff0000, v69\n\t" \
  "v_add_f32 v100, v100, v104\n\t" \
  "v_add_f32 v101, v101, v105\n\t" \
  "v_add_f32 v102, v102, v106\n\t" \
  "v_add_f32 v103, v103, v107\n\t" \
  "s_waitcnt vmcnt(5)\n\t" \
  "v_lshlrev_b32 v104, 16, v72\n\t" \
  "v_and_b32 v105, 0xffff0000, v72\n\t" \
  "v_lshlrev_b32 v106, 16, v73\n\t" \
  "v_and_b32 v107, 0xffff0000, v73\n\t" \
  "v_add_f32 v100, v100, v104\n\t" \
  "v_add_f32 v101, v101, v105\n\t" \
  "v_add_f32 v102, v102, v106\n\t" \
  "v_add_f32 v103, v103, v107\n\t" \
  "s_waitcnt vmcnt(4)\n\t" \
  "v_lshlrev_b32 v104, 16, v76\n\t" \
  "v_and_b32 v105, 0xffff0000, v76\n\t" \
  "v_lshlrev_b32 v106, 16, v77\n\t" \
  "v_and_b32 v107, 0xffff0000, v77\n\t" \
  "v_add_f32 v100, v100, v104\n\t" \
  "v_add_f32 v101, v101, v105\n\t" \
  "v_add_f32 v102, v102, v106\n\t" \
  "v_add_f32 v103, v103, v107\n\t" \
  "v_cvt_pk_bf16_f32 v128, v100, v101\n\t" \
  "v_cvt_pk_bf16_f32 v129, v102, v103\n\t" \
  "buffer_store_dwordx2 v[128:129], v247, s[48:51], s81 offen\n\t" \
  "v_lshlrev_b32 v108, 16, v66\n\t" \
  "v_and_b32 v109, 0xffff0000, v66\n\t" \
  "v_lshlrev_b32 v110, 16, v67\n\t" \
  "v_and_b32 v111, 0xffff0000, v67\n\t" \
  "v_lshlrev_b32 v112, 16, v70\n\t" \
  "v_and_b32 v113, 0xffff0000, v70\n\t" \
  "v_lshlrev_b32 v114, 16, v71\n\t" \
  "v_and_b32 v115, 0xffff0000, v71\n\t" \
  "v_add_f32 v108, v108, v112\n\t" \
  "v_add_f32 v109, v109, v113\n\t" \
  "v_add_f32 v110, v110, v114\n\t" \
  "v_add_f32 v111, v111, v115\n\t" \
  "v_lshlrev_b32 v112, 16, v74\n\t" \
  "v_and_b32 v113, 0xffff0000, v74\n\t" \
  "v_lshlrev_b32 v114, 16, v75\n\t" \
  "v_and_b32 v115, 0xffff0000, v75\n\t" \
  "v_add_f32 v108, v108, v112\n\t" \
  "v_add_f32 v109, v109, v113\n\t" \
  "v_add_f32 v110, v110, v114\n\t" \
  "v_add_f32 v111, v111, v115\n\t" \
  "v_lshlrev_b32 v112, 16, v78\n\t" \
  "v_and_b32 v113, 0xffff0000, v78\n\t" \
  "v_lshlrev_b32 v114, 16, v79\n\t" \
  "v_and_b32 v115, 0xffff0000, v79\n\t" \
  "v_add_f32 v108, v108, v112\n\t" \
  "v_add_f32 v109, v109, v113\n\t" \
  "v_add_f32 v110, v110, v114\n\t" \
  "v_add_f32 v111, v111, v115\n\t" \
  "v_cvt_pk_bf16_f32 v130, v108, v109\n\t" \
  "v_cvt_pk_bf16_f32 v131, v110, v111\n\t" \
  "buffer_store_dwordx2 v[130:131], v247, s[48:51], s82 offen\n\t" \
  "s_mov_b32 %0, 0\n\t" \
  "s_branch END_%=\n\t" \
  "END_%=:\n\t"

#define CLOB_V "v32","v33","v34","v35","v36","v37","v38","v39","v40","v41","v42","v43","v44","v45","v46","v47","v48","v49","v50","v51","v52","v53","v54","v55","v56","v57","v58","v59","v60","v61","v62","v63","v64","v65","v66","v67","v68","v69","v70","v71","v72","v73","v74","v75","v76","v77","v78","v79","v80","v81","v82","v83","v84","v85","v86","v87","v88","v89","v90","v91","v92","v93","v94","v95","v96","v97","v98","v99","v100","v101","v102","v103","v104","v105","v106","v107","v108","v109","v110","v111","v112","v113","v114","v115","v116","v117","v118","v119","v120","v121","v122","v123","v124","v125","v126","v127","v128","v129","v130","v131","v132","v133","v134","v135","v136","v137","v138","v139","v140","v141","v142","v143","v144","v145","v146","v147","v148","v149","v150","v151","v152","v153","v154","v155","v156","v157","v158","v159","v160","v161","v162","v163","v164","v165","v166","v167","v168","v169","v170","v171","v172","v173","v174","v175","v176","v177","v178","v179","v180","v181","v182","v183","v184","v185","v186","v187","v188","v189","v190","v191","v192","v193","v194","v195","v196","v197","v198","v199","v200","v201","v202","v203","v204","v205","v206","v207","v208","v209","v210","v211","v212","v213","v214","v215","v216","v217","v218","v219","v220","v221","v222","v223","v224","v225","v226","v227","v228","v229","v230","v231","v232","v233","v234","v235","v236","v237","v238","v239","v240","v241","v242","v243","v244","v245","v246","v247","v248","v249","v250","v251","v252","v253","v254","v255"
#define CLOB_A "a0","a1","a2","a3","a4","a5","a6","a7","a8","a9","a10","a11","a12","a13","a14","a15","a16","a17","a18","a19","a20","a21","a22","a23","a24","a25","a26","a27","a28","a29","a30","a31","a32","a33","a34","a35","a36","a37","a38","a39","a40","a41","a42","a43","a44","a45","a46","a47","a48","a49","a50","a51","a52","a53","a54","a55","a56","a57","a58","a59","a60","a61","a62","a63","a64","a65","a66","a67","a68","a69","a70","a71","a72","a73","a74","a75","a76","a77","a78","a79","a80","a81","a82","a83","a84","a85","a86","a87","a88","a89","a90","a91","a92","a93","a94","a95","a96","a97","a98","a99","a100","a101","a102","a103","a104","a105","a106","a107","a108","a109","a110","a111","a112","a113","a114","a115","a116","a117","a118","a119","a120","a121","a122","a123","a124","a125","a126","a127","a128","a129","a130","a131","a132","a133","a134","a135","a136","a137","a138","a139","a140","a141","a142","a143","a144","a145","a146","a147","a148","a149","a150","a151","a152","a153","a154","a155","a156","a157","a158","a159","a160","a161","a162","a163","a164","a165","a166","a167","a168","a169","a170","a171","a172","a173","a174","a175","a176","a177","a178","a179","a180","a181","a182","a183","a184","a185","a186","a187","a188","a189","a190","a191","a192","a193","a194","a195","a196","a197","a198","a199","a200","a201","a202","a203","a204","a205","a206","a207","a208","a209","a210","a211","a212","a213","a214","a215","a216","a217","a218","a219","a220","a221","a222","a223","a224","a225","a226","a227","a228","a229","a230","a231","a232","a233","a234","a235","a236","a237","a238","a239","a240","a241","a242","a243","a244","a245","a246","a247","a248","a249","a250","a251","a252","a253","a254","a255"
#define CLOB_S "s40","s41","s42","s43","s44","s45","s46","s47","s48","s49","s50","s51","s52","s53","s54","s55","s56","s57","s58","s59","s60","s61","s62","s63","s64","s65","s66","s67","s68","s69","s70","s71","s72","s73","s74","s75","s76","s77","s78","s79","s80","s81","s82","s83","s84","s85","s86","s87","s88","s89","s90","s91","s92","s93","s94","s95","s96","s97","s98","s99"

typedef unsigned short u16;
typedef unsigned long long u64;
constexpr int XCH=153600, MRG=98304;
constexpr size_t WS_PW=32u<<20, WS_SY=64u<<20, WS_ST=80u<<20;
__device__ __forceinline__ u64 pk(unsigned lo,unsigned hi){ return (u64)lo | ((u64)hi<<32); }
__device__ __forceinline__ unsigned rfl(unsigned x){ return __builtin_amdgcn_readfirstlane(x); }
__device__ __forceinline__ u64 rfl64(u64 x){ return pk(rfl((unsigned)x), rfl((unsigned)(x>>32))); }


typedef __attribute__((ext_vector_type(8))) __bf16 bf16x8;
typedef __attribute__((ext_vector_type(4))) float f32x4;
typedef __attribute__((ext_vector_type(4))) unsigned u32x4;
typedef __attribute__((ext_vector_type(2))) float f32x2;
typedef __attribute__((ext_vector_type(2))) __bf16 bf16x2;
__device__ __forceinline__ unsigned cvtpk(float lo,float hi){ f32x2 v={lo,hi}; bf16x2 b=__builtin_convertvector(v,bf16x2); return __builtin_bit_cast(unsigned,b); }
__device__ __forceinline__ bf16x8 gather8(const u16* p,size_t stride_lo,size_t off_hi){
  u32x4 w;
  w[0]=(unsigned)p[0]|((unsigned)p[stride_lo]<<16); w[1]=(unsigned)p[2*stride_lo]|((unsigned)p[3*stride_lo]<<16);
  w[2]=(unsigned)p[off_hi]|((unsigned)p[off_hi+stride_lo]<<16); w[3]=(unsigned)p[off_hi+2*stride_lo]|((unsigned)p[off_hi+3*stride_lo]<<16);
  return __builtin_bit_cast(bf16x8,w);
}
__device__ __forceinline__ void exact_attention(const u16* x,const u16* wq,const u16* kc,const u16* vc,char* ows,unsigned b,unsigned kvh,unsigned w,unsigned lane){
  const unsigned q=lane&15,g=lane>>4;
  auto rs=__builtin_amdgcn_make_buffer_rsrc(ows,0,9437184,0x20000);
  for(unsigned T=w;T<18;T+=4){
    const unsigned head=T/9, rt=T%9, hq=2*kvh+head;
    bf16x8 Q[4];
    const u16* xr=x+(size_t)(b*144+rt*16+q)*576+8*g;
    for(int sg=0;sg<4;++sg){
      f32x4 a0={0,0,0,0},a1={0,0,0,0};
      const u16* wc=wq+hq*128+32*sg+8*(q>>2)+(q&3);
      for(int ks=0;ks<18;++ks){
        const bf16x8 xb=*(const bf16x8*)(xr+32*ks);
        const u16* wr=wc+(size_t)(32*ks+8*g)*4096;
        a0=__builtin_amdgcn_mfma_f32_16x16x32_bf16(gather8(wr,4096,4*4096),xb,a0,0,0,0);
        a1=__builtin_amdgcn_mfma_f32_16x16x32_bf16(gather8(wr+4,4096,4*4096),xb,a1,0,0,0);
      }
      const float c=0.12751743082f;
      u32x4 p={cvtpk(a0[0]*c,a0[1]*c),cvtpk(a0[2]*c,a0[3]*c),cvtpk(a1[0]*c,a1[1]*c),cvtpk(a1[2]*c,a1[3]*c)};
      Q[sg]=__builtin_bit_cast(bf16x8,p);
    }
    f32x4 O[8]; for(int d=0;d<8;++d) O[d]=f32x4{0,0,0,0};
    float m=-__builtin_inff(), l=0.f;
    for(int st=0;st<32;++st){
      f32x4 s0={0,0,0,0},s1={0,0,0,0};
      const u16* k0=kc+(size_t)(32*st+q)*128+8*g;
      for(int sg=0;sg<4;++sg){
        s0=__builtin_amdgcn_mfma_f32_16x16x32_bf16(*(const bf16x8*)(k0+32*sg),Q[sg],s0,0,0,0);
        s1=__builtin_amdgcn_mfma_f32_16x16x32_bf16(*(const bf16x8*)(k0+16*128+32*sg),Q[sg],s1,0,0,0);
      }
      float mx=fmaxf(fmaxf(fmaxf(s0[0],s0[1]),fmaxf(s0[2],s0[3])),fmaxf(fmaxf(s1[0],s1[1]),fmaxf(s1[2],s1[3])));
      mx=fmaxf(mx,__shfl_xor(mx,16)); mx=fmaxf(mx,__shfl_xor(mx,32));
      const float mn=fmaxf(m,mx), al=__builtin_amdgcn_exp2f(m-mn);
      m=mn;
      float e[8]; for(int j=0;j<4;++j){ e[j]=__builtin_amdgcn_exp2f(s0[j]-mn); e[4+j]=__builtin_amdgcn_exp2f(s1[j]-mn); }
      float ps=0; for(int j=0;j<8;++j) ps+=e[j];
      ps+=__shfl_xor(ps,16); ps+=__shfl_xor(ps,32);
      l=l*al+ps;
      u32x4 pw={cvtpk(e[0],e[1]),cvtpk(e[2],e[3]),cvtpk(e[4],e[5]),cvtpk(e[6],e[7])};
      const bf16x8 P=__builtin_bit_cast(bf16x8,pw);
      const u16* v0=vc+(size_t)(32*st+4*g)*128+q;
      for(int d=0;d<8;++d){
        O[d]*=al;
        O[d]=__builtin_amdgcn_mfma_f32_16x16x32_bf16(gather8(v0+16*d,128,16*128),P,O[d],0,0,0);
      }
    }
    const float il=1.0f/l;
    for(int sp=0;sp<4;++sp){
      u32x4 o={cvtpk(O[2*sp][0]*il,O[2*sp][1]*il),cvtpk(O[2*sp][2]*il,O[2*sp][3]*il),cvtpk(O[2*sp+1][0]*il,O[2*sp+1][1]*il),cvtpk(O[2*sp+1][2]*il,O[2*sp+1][3]*il)};
      __builtin_amdgcn_raw_buffer_store_b128(o,rs,((9*b+rt)*128+hq*4+sp)*1024+lane*16,0,16);
    }
  }
}

struct Ptrs { const u16 *xA,*kA,*vA,*wqA,*woA,*xB,*kB,*vB,*wqB,*woB; char* ws; u16* out; };
template<bool FB>
__device__ __forceinline__ int run_block(const Ptrs& P, unsigned bid, unsigned tid){
  char* ws=P.ws; u16* out=P.out;
  const unsigned xcd=bid&7, idx=bid>>3;
  const unsigned l=xcd>>2, cj=xcd&3, ci=idx>>3, kvh=cj*4+ci, b=idx&7;
  const unsigned lane=tid&63, w=rfl(tid>>6), p=w&1, head=w>>1;
  const u16* x=l?P.xB:P.xA; const u16* kc=l?P.kB:P.kA; const u16* vc=l?P.vB:P.vA; const u16* wq=l?P.wqB:P.wqA; const u16* wo=l?P.woB:P.woA;
  if(FB){
    asm volatile("s_waitcnt vmcnt(0) lgkmcnt(0)" ::: "memory");
    exact_attention(x,wq,kc+(size_t)(b*16+kvh)*131072,vc+(size_t)(b*16+kvh)*131072,ws+(size_t)l*9437184,b,kvh,w,lane);
    asm volatile("s_waitcnt vmcnt(0)" ::: "memory");
    __syncthreads();
  }
  const unsigned g=lane>>4, q4=(lane>>2)&3, pp=lane&3, key=lane&15;
  const unsigned wbase=g*4096+q4*64+pp*16+head*2048;
  const unsigned ksw=64*(key&7)+16*(g^(2*((key>>2)&1)));
  const unsigned xl=(key>>3)*1024+ksw;
  const unsigned dsw=(lane>>5)*64+16*((lane&3)^(2*((lane>>4)&1))), drow=(lane>>2)&7;
  u64 o1=pk(wbase+1024*p, wbase-1024*p);
  u64 o2=pk(xl+p*8192, xl+16384);
  u64 o3=pk(((lane&31)>>2)*8192+64*(lane>>5)+16*(lane&3)+w*128, drow*1152+dsw);
  u64 o4=pk(XCH+head*4096+p*2048+lane*16, XCH+head*4096+lane*16);
  u64 o5=pk(lane*16, MRG+w*8192+lane*16);
  u64 o6=pk(MRG+(w^1)*8192+lane*16, 0);
  const unsigned kb=(key>>3)*2048+ksw;
  const unsigned vb=(g>>1)*2048+64*(4*(g&1)+q4)+16*(pp>>1)+8*(pp&1), vb0=vb+32*(g&1), vb1=vb+32*(1-(g&1));
  u64 o7=pk(kb+p*8192, kb+(1-p)*8192);
  u64 o8=pk(vb0+p*8192, vb1+p*8192), o10=pk(vb0+(1-p)*8192, vb1+(1-p)*8192);
  const unsigned dkv=drow*256+dsw+p*128+head*8192;
  u64 o9=pk(dkv,dkv);
  const unsigned ab=head*36864+(4*g+q4)*288+8*pp;
  u64 o26=pk(ab+128*p, ab-160*p);
  const unsigned hq=2*kvh+head, c0=b;
  u64 sx=rfl64((u64)(x)+(u64)b*165888), sw=rfl64((u64)wq+kvh*512), sk=rfl64((u64)kc+(u64)(b*16+kvh)*262144), sv=rfl64((u64)vc+(u64)(b*16+kvh)*262144);
  u64 so=rfl64((u64)ws+(u64)l*9437184);
  u64 s15=rfl64(pk(2*c0*262144, c0*128)), s16=rfl64(pk(w*1024,idx)), s17=rfl64(pk(p*1024+head*8192, ((9*b+4*p)*128+hq*4)*1024)), s18=rfl64(pk(((9*b+8)*128+hq*4+2*p)*1024, p));
  unsigned wsz=rfl(576*8192-kvh*512);
  u64 s21=rfl64((u64)wo+ci*288), s22=rfl64((u64)ws+WS_PW+(u64)((l*8+b)*4+ci)*196608), s23=rfl64((u64)out+((u64)(9*b*16)*1152+l*576+144*ci)*2);
  u64 s24=rfl64((u64)ws+WS_SY+(u64)(l*8+b)*16384), s25=rfl64(pk(cj,ci));
  int flag;
  if(!FB){
    asm volatile(KZ_ASM : "=s"(flag)
       : "v"(o1),"v"(o2),"v"(o3),"v"(o4),"v"(o5),"v"(o6),"v"(o7),"v"(o8),"v"(o9),"v"(o10),
         "s"(sx),"s"(sw),"s"(sk),"s"(sv),"s"(so),"s"(s15),"s"(s16),"s"(s17),"s"(s18),"s"(wsz),
         "s"(s21),"s"(s22),"s"(s23),"s"(s24),"s"(s25),"v"(o26)
       : CLOB_V, CLOB_A, CLOB_S, "memory","scc","vcc");
  } else {
    asm volatile(KZ_FB_ASM : "=s"(flag)
       : "v"(o1),"v"(o2),"v"(o3),"v"(o4),"v"(o5),"v"(o6),"v"(o7),"v"(o8),"v"(o9),"v"(o10),
         "s"(sx),"s"(sw),"s"(sk),"s"(sv),"s"(so),"s"(s15),"s"(s16),"s"(s17),"s"(s18),"s"(wsz),
         "s"(s21),"s"(s22),"s"(s23),"s"(s24),"s"(s25),"v"(o26)
       : CLOB_V, CLOB_A, CLOB_S, "memory","scc","vcc");
  }
  return flag;
}
__global__ __launch_bounds__(256,1) void kz(Ptrs P){
  const int flag=run_block<false>(P,blockIdx.x,threadIdx.x);
  if(flag){
    const volatile __attribute__((address_space(4))) u64* ka=(const volatile __attribute__((address_space(4))) u64*)__builtin_amdgcn_kernarg_segment_ptr();
    Ptrs Q; Q.xA=(const u16*)ka[0]; Q.kA=(const u16*)ka[1]; Q.vA=(const u16*)ka[2]; Q.wqA=(const u16*)ka[3]; Q.woA=(const u16*)ka[4];
    Q.xB=(const u16*)ka[5]; Q.kB=(const u16*)ka[6]; Q.vB=(const u16*)ka[7]; Q.wqB=(const u16*)ka[8]; Q.woB=(const u16*)ka[9]; Q.ws=(char*)ka[10]; Q.out=(u16*)ka[11];
    unsigned tid2=threadIdx.x, bid2=blockIdx.x;
    asm volatile("" : "+v"(tid2), "+s"(bid2));
    run_block<true>(Q,bid2,tid2);
  }
}
extern "C" void kernel_launch(void* const* d_in,const int* in_sizes,int n_in,void* d_out,int out_size,void* d_ws,size_t ws_size,hipStream_t stream){
  (void)hipFuncSetAttribute((const void*)kz,hipFuncAttributeMaxDynamicSharedMemorySize,163840);
Ptrs P; P.xA=(const u16*)d_in[0]; P.kA=(const u16*)d_in[1]; P.vA=(const u16*)d_in[2]; P.wqA=(const u16*)d_in[3]; P.woA=(const u16*)d_in[4];
  P.xB=(const u16*)d_in[5]; P.kB=(const u16*)d_in[6]; P.vB=(const u16*)d_in[7]; P.wqB=(const u16*)d_in[8]; P.woB=(const u16*)d_in[9]; P.ws=(char*)d_ws; P.out=(u16*)d_out;
  kz<<<256,256,163840,stream>>>(P);
}
```
